```python
import torch

def _softmax(x, axis=-1):
    return torch.softmax(x, dim=axis)
def _gelu(x):
    return torch.nn.functional.gelu(x, approximate="tanh")
def _silu(x):
    return torch.nn.functional.silu(x)
def _sigmoid(x):
    return torch.sigmoid(x)
def _layernorm(x, axis=-1, eps=1e-5):
    m = x.mean(dim=axis, keepdim=True)
    v = x.var(dim=axis, keepdim=True, unbiased=False)
    return (x - m) / torch.sqrt(v + eps)
def _scatter_add(tgt, idx, src, axis=0):
    return tgt.clone().index_add_(axis, idx, src)
def _segment_sum(src, seg, n):
    out = torch.zeros((n,) + src.shape[1:], dtype=src.dtype)
    return out.index_add_(0, seg, src)
def _pad(x, pw):
    flat = tuple(v for pair in reversed(pw) for v in pair)
    return torch.nn.functional.pad(x, flat)

kernel_name = 'sequential_preLN__grouped_kv_mqa__glu_gated_swiglu__prompt'

def setup_inputs():

    _w = (lambda a: (lambda t: t.to(torch.bfloat16) if t.is_floating_point() else t)(torch.as_tensor(a)))
    def _A_setup():
        B, S, D, H = 1, 2048, 640, 32
        Dh = D // H
        return {
            "x": _w((lambda *s: torch.randn(*s, dtype=torch.bfloat16) * 0.05)(B, S, D)),
            "wq": _w((lambda *s: torch.randn(*s, dtype=torch.bfloat16) * 0.05)(D, H * Dh)),
            "wk": _w((lambda *s: torch.randn(*s, dtype=torch.bfloat16) * 0.05)(D, Dh)),
            "wv": _w((lambda *s: torch.randn(*s, dtype=torch.bfloat16) * 0.05)(D, Dh)),
            "wo": _w((lambda *s: torch.randn(*s, dtype=torch.bfloat16) * 0.05)(H * Dh, D)),
        }
    _A_in = _A_setup()

    def _B_setup():
        B, S, D, F = 1, 2048, 640, 1728
        return {
            "x": _w((lambda *s: torch.randn(*s, dtype=torch.bfloat16) * 0.05)(B, S, D)),
            "w1": _w((lambda *s: torch.randn(*s, dtype=torch.bfloat16) * 0.4)(D, F) * (2.0 / D) ** 0.5),
            "w3": _w((lambda *s: torch.randn(*s, dtype=torch.bfloat16) * 0.4)(D, F) * (2.0 / D) ** 0.5),
            "w2": _w((lambda *s: torch.randn(*s, dtype=torch.bfloat16) * 0.4)(F, D) * (2.0 / F) ** 0.5),
        }
    _B_in = _B_setup()

    return {
        "A_x": _A_in["x"],
        "A_wq": _A_in["wq"],
        "A_wk": _A_in["wk"],
        "A_wv": _A_in["wv"],
        "A_wo": _A_in["wo"],
        "B_x": _B_in["x"],
        "B_w1": _B_in["w1"],
        "B_w3": _B_in["w3"],
        "B_w2": _B_in["w2"],
    }

def reference(A_x, A_wq, A_wk, A_wv, A_wo, B_x, B_w1, B_w3, B_w2):
    def _A_ref(x, wq, wk, wv, wo):
        B, S, D = x.shape
        H = 32
        Dh = D // H
        q = (lambda x, *d: x.permute(*d))((x @ wq).reshape(B, S, H, Dh), 0, 2, 1, 3)
        k = x @ wk
        v = x @ wv
        scores = torch.einsum("bhqd,bkd->bhqk", q, k) * (Dh ** -0.5)
        p = _softmax(scores, axis=-1)
        o = torch.einsum("bhqk,bkd->bhqd", p, v)
        return (lambda x, *d: x.permute(*d))(o, 0, 2, 1, 3).reshape(B, S, H * Dh) @ wo

    def _B_ref(x, w1, w3, w2):
        gate = torch.einsum("bsd,df->bsf", x, w1)
        up = torch.einsum("bsd,df->bsf", x, w3)
        h = _silu(gate) * up
        out = torch.einsum("bsf,fd->bsd", h, w2)
        return out

    x = A_x
    h = x + _A_ref(_layernorm(x, axis=-1), A_wq, A_wk, A_wv, A_wo)
    h2 = h + _B_ref(_layernorm(h, axis=-1), B_w1, B_w3, B_w2)
    return h2
```

```cpp
#include <hip/hip_runtime.h>
#include <hip/hip_bf16.h>
#include <stdint.h>

typedef short bf16x8 __attribute__((ext_vector_type(8)));
typedef short s16x4 __attribute__((ext_vector_type(4)));
typedef float f32x2 __attribute__((ext_vector_type(2)));
typedef float f32x4 __attribute__((ext_vector_type(4)));
typedef float f32x16 __attribute__((ext_vector_type(16)));
typedef unsigned u32x2 __attribute__((ext_vector_type(2)));
typedef unsigned u32x4 __attribute__((ext_vector_type(4)));
typedef __bf16 bf16x2_t __attribute__((ext_vector_type(2)));
typedef unsigned short u16;
#define LAS __attribute__((address_space(3)))
#define DI __device__ __forceinline__

constexpr int S = 2048, D = 640, H = 32, DH = 20, F = 1728;
constexpr size_t WS_Q = 0;
constexpr size_t WS_KV = WS_Q + (size_t)S * D * 2;
constexpr size_t WS_KN = WS_KV + 64 * 4096;
constexpr size_t WS_O = WS_KN + 4096;
constexpr size_t WS_H = WS_O + (size_t)S * D * 2;
constexpr size_t WS_ST = WS_H + (size_t)S * D * 2;
constexpr size_t WS_HH = WS_ST + (size_t)S * 8 * 8;
constexpr size_t WS_WO = WS_HH + (size_t)S * F * 2;
constexpr size_t WS_W13 = WS_WO + (size_t)D * D * 2;
constexpr size_t WS_W2 = WS_W13 + (size_t)D * F * 4;
constexpr size_t WS_CS = WS_W2 + (size_t)F * D * 2;
constexpr size_t WS_END = WS_CS + 20 * 2 * F * 4;

#define MFMA16(a, b, c) __builtin_amdgcn_mfma_f32_16x16x32_bf16((a), (b), (c), 0, 0, 0)
#define MFMA32(a, b, c) __builtin_amdgcn_mfma_f32_32x32x16_bf16((a), (b), (c), 0, 0, 0)
#define SBAR() __builtin_amdgcn_sched_barrier(0)

DI unsigned pk2(float a, float b) { f32x2 v = {a, b}; bf16x2_t r = __builtin_convertvector(v, bf16x2_t); return __builtin_bit_cast(unsigned, r); }
DI float bf_lo(unsigned u) { return __uint_as_float(u << 16); }
DI float bf_hi(unsigned u) { return __uint_as_float(u & 0xffff0000u); }
DI float rbf(float x) { return bf_lo(pk2(x, 0.f)); }
DI void glds16(const void* g, unsigned lds_base) {
  unsigned sv; asm volatile("s_mov_b32 %0, m0\n\ts_mov_b32 m0, %2\n\ts_nop 0\n\tglobal_load_lds_dwordx4 %1, off\n\ts_mov_b32 m0, %0" : "=&s"(sv) : "v"(g), "s"(lds_base) : "memory"); }
typedef short v4i16_t __attribute__((ext_vector_type(4)));
DI s16x4 trd(const LAS char* p) { return __builtin_bit_cast(s16x4, __builtin_amdgcn_ds_read_tr16_b64_v4i16((LAS v4i16_t*)p)); }
DI bf16x8 cat(s16x4 a, s16x4 b) { return __builtin_shufflevector(a, b, 0, 1, 2, 3, 4, 5, 6, 7); }
DI unsigned uni(unsigned x) { return __builtin_amdgcn_readfirstlane(x); }
DI void ln_consts(float mean, float var, float& mu, float& rs) { mu = rbf(mean); float den = rbf(__builtin_sqrtf(rbf(rbf(var) + 1e-5f))); rs = 1.0f / den; }

DI int kmap_lin(int ks, int g, int half) { return 32 * ks + 16 * half + 4 * g; }
DI int kmap_o(int ks, int g, int half) { int c = 4 * ks + g, p = c / 5, r = c - 5 * p; return r == 4 ? (40 * p + 20 * half + 16) : (40 * p + 20 * (r >> 1) + 4 * (r & 1) + 8 * half); }

DI void pretile_job(int job, const u16* wo, const u16* w1, const u16* w3, const u16* w2, char* ws, LAS char* lds, int lane) {
  const u16* src; int ld, ks, nb; int kind;
  if (job < 1080) { kind = job >= 540; int j = job - 540 * kind; src = kind ? w3 : w1; ld = F; ks = j / 27; nb = j - ks * 27; }
  else if (job < 1620) { kind = 2; int j = job - 1080; src = w2; ld = D; ks = j / 10; nb = j - ks * 10; }
  else { kind = 3; int j = job - 1620; src = wo; ld = D; ks = j / 10; nb = j - ks * 10; }
#pragma unroll
  for (int it = 0; it < 4; ++it) {
    int rho = it * 8 + (lane >> 3), ch = lane & 7; int half = rho >> 4, g = (rho >> 2) & 3, i = rho & 3;
    int k = (kind == 3 ? kmap_o(ks, g, half) : kmap_lin(ks, g, half)) + i;
    u32x4 v = *(const u32x4*)(src + (size_t)k * ld + nb * 64 + ch * 8);
    *(LAS u32x4*)(lds + rho * 144 + ch * 16) = v;
  }
  int g = lane >> 4, q = (lane & 15) >> 2, p = lane & 3;
#pragma unroll
  for (int nt = 0; nt < 4; ++nt) {
    s16x4 a = trd(lds + (4 * g + q) * 144 + nt * 32 + p * 8), b = trd(lds + (16 + 4 * g + q) * 144 + nt * 32 + p * 8);
    bf16x8 f = cat(a, b); int t = nb * 4 + nt; size_t off;
    if (kind < 2) off = WS_W13 + ((size_t)(t * 20 + ks) * 2 + kind) * 1024;
    else if (kind == 2) off = WS_W2 + ((size_t)((t / 5) * 54 + ks) * 5 + t % 5) * 1024;
    else off = WS_WO + ((size_t)((t / 5) * 20 + ks) * 5 + t % 5) * 1024;
    *(bf16x8*)(ws + off + lane * 16) = f;
    if (kind < 2) {
      u32x4 u = __builtin_bit_cast(u32x4, f); float s = 0.f;
#pragma unroll
      for (int e = 0; e < 4; ++e) s += bf_lo(u[e]) + bf_hi(u[e]);
      s += __shfl_xor(s, 16); s += __shfl_xor(s, 32);
      if (lane < 16) ((float*)(ws + WS_CS))[(ks * 2 + kind) * F + t * 16 + lane] = s;
    }
  }
}
__global__ __launch_bounds__(256) void k_pretile(const u16* wo, const u16* w1, const u16* w3, const u16* w2, char* ws) {
  __shared__ __attribute__((aligned(16))) char lds[4 * 4608];
  int w = uni(threadIdx.x >> 6), lane = threadIdx.x & 63; int job = blockIdx.x * 4 + w;
  if (job < 1820) pretile_job(job, wo, w1, w3, w2, ws, (LAS char*)lds + w * 4608, lane);
}

constexpr int K1_LDS_W = 0, K1_LDS_KV = 102400, K1_LDS_CS = K1_LDS_KV + 25600 + 64, K1_LDS_ST = K1_LDS_CS + 4 * 96 * 4, K1_LDS = K1_LDS_ST + 64 * 8;
__global__ __launch_bounds__(256, 1) void k_qkv(const u16* x, const u16* wq, const u16* wk, const u16* wv, char* ws) {
  extern __shared__ __attribute__((aligned(16))) char smem[];
  LAS char* lds = (LAS char*)smem; const unsigned lds0 = (unsigned)(uintptr_t)lds;
  const int tid = threadIdx.x, lane = tid & 63, w = uni(tid >> 6), t16 = lane & 15, g = lane >> 4;
  const int xcd = blockIdx.x & 7, loc = blockIdx.x >> 3, mt = 4 * xcd + (loc >> 3), j = loc & 7;
  const int tok0 = mt * 64 + w * 16;
  bf16x8 xf[20];
  const u16* xp = x + (size_t)(tok0 + t16) * D + g * 8;
#pragma unroll
  for (int s = 0; s < 20; ++s) xf[s] = *(const bf16x8*)(xp + s * 32);
#pragma unroll 5
  for (int it = 0; it < 25; ++it) {
    int ins = it * 4 + w, ci = ins * 64 + lane, row = ci / 10, cc = ci - row * 10, rho = row & 31;
    int k = (row & ~31) + 8 * ((rho & 15) >> 2) + 4 * (rho >> 4) + (rho & 3);
    glds16(wq + (size_t)k * D + j * 80 + cc * 8, uni(lds0 + K1_LDS_W + ins * 1024));
  }
  if (j < 4) {
    const u16* wkv = (j < 2) ? wk : wv;
    for (int ins = w; ins < 25; ins += 4) glds16(wkv + ins * 512 + lane * 8, uni(lds0 + K1_LDS_KV + ins * 1024));
  }
  asm volatile("s_waitcnt vmcnt(0)" ::: "memory");
  __syncthreads();
  f32x4 acc[6], cs[6], asum = {}, asq = {};
#pragma unroll
  for (int i = 0; i < 6; ++i) { acc[i] = f32x4{}; cs[i] = f32x4{}; }
  bf16x8 ones;
#pragma unroll
  for (int i = 0; i < 8; ++i) ones[i] = 0x3f80;
  const int q4 = (lane & 15) >> 2, p4 = lane & 3;
  const LAS char* wb = lds + K1_LDS_W + (4 * g + q4) * 160 + p4 * 8;
  const LAS char* kb = lds + K1_LDS_KV + (8 * g + q4) * 40 + (j & 1) * 32 + p4 * 8;
  const bool isv = (j >> 1) == 1;
#pragma unroll
  for (int s = 0; s < 20; ++s) {
    const bool mine = (s / 5) == w;
#pragma unroll
    for (int nt = 0; nt < 5; ++nt) {
      bf16x8 wf = cat(trd(wb + s * 5120 + nt * 32), trd(wb + s * 5120 + 2560 + nt * 32));
      acc[nt] = MFMA16(wf, xf[s], acc[nt]);
      if (mine) cs[nt] = MFMA16(wf, ones, cs[nt]);
    }
    if (j < 4) {
      bf16x8 wf = cat(trd(kb + s * 1280), trd(kb + s * 1280 + 160));
      if (isv) acc[5] = MFMA16(xf[s], wf, acc[5]); else acc[5] = MFMA16(wf, xf[s], acc[5]);
      if (mine) cs[5] = MFMA16(wf, ones, cs[5]);
    }
    asum = MFMA16(ones, xf[s], asum);
    asq = MFMA16(xf[s], xf[s], asq);
  }
  LAS float* csl = (LAS float*)(lds + K1_LDS_CS);
  if (t16 == 0) {
#pragma unroll
    for (int nt = 0; nt < 6; ++nt) *(LAS f32x4*)(csl + w * 96 + nt * 16 + g * 4) = cs[nt];
  }
  float dsel = (t16 & 3) == 0 ? asq[0] : (t16 & 3) == 1 ? asq[1] : (t16 & 3) == 2 ? asq[2] : asq[3];
  float sq = __shfl(dsel, t16 + 16 * (t16 >> 2));
  float mean = asum[0] * (1.0f / D), var = fmaxf(sq * (1.0f / D) - mean * mean, 0.f), mu, rs;
  ln_consts(mean, var, mu, rs);
  LAS f32x2* stl = (LAS f32x2*)(lds + K1_LDS_ST) + w * 16;
  if (g == 0) stl[t16] = f32x2{mu, rs};
  __syncthreads();
  const float qs = rs * (1.4426950408889634f * 0.22360679774997896f);
  u16* qo = (u16*)(ws + WS_Q) + (size_t)(tok0 + t16) * D + j * 80 + g * 4;
#pragma unroll
  for (int nt = 0; nt < 5; ++nt) {
    f32x4 c = *(LAS f32x4*)(csl + nt * 16 + g * 4) + *(LAS f32x4*)(csl + 96 + nt * 16 + g * 4) + *(LAS f32x4*)(csl + 192 + nt * 16 + g * 4) + *(LAS f32x4*)(csl + 288 + nt * 16 + g * 4);
    f32x4 v = (acc[nt] - mu * c) * qs;
    *(u32x2*)(qo + nt * 16) = u32x2{pk2(v[0], v[1]), pk2(v[2], v[3])};
  }
  if (j < 4) {
    const int T = tok0 >> 5, part = j & 1; char* kv = ws + WS_KV + (size_t)T * 4096;
    if (!isv) {
      f32x4 c = *(LAS f32x4*)(csl + 80 + g * 4) + *(LAS f32x4*)(csl + 96 + 80 + g * 4) + *(LAS f32x4*)(csl + 192 + 80 + g * 4) + *(LAS f32x4*)(csl + 288 + 80 + g * 4);
      f32x4 v = (acc[5] - mu * c) * rs;
      u32x2 o = {pk2(v[0], v[1]), pk2(v[2], v[3])};
      if (part == 1 && g > 0) o = u32x2{0u, 0u};
      const int r = (tok0 & 31) + t16;
      *(u32x2*)(kv + part * 1024 + (g >> 1) * 512 + r * 16 + (g & 1) * 8) = o;
      float n2 = bf_lo(o[0]) * bf_lo(o[0]) + bf_hi(o[0]) * bf_hi(o[0]) + bf_lo(o[1]) * bf_lo(o[1]) + bf_hi(o[1]) * bf_hi(o[1]);
      n2 += __shfl_xor(n2, 16); n2 += __shfl_xor(n2, 32);
#pragma unroll
      for (int m = 1; m < 16; m <<= 1) n2 = fmaxf(n2, __shfl_xor(n2, m));
      if (lane == 0) ((float*)(ws + WS_KN))[part * 128 + mt * 4 + w] = n2;
    } else {
      float c = csl[80 + t16] + csl[96 + 80 + t16] + csl[192 + 80 + t16] + csl[288 + 80 + t16];
      float v[4];
#pragma unroll
      for (int i = 0; i < 4; ++i) { f32x2 st = stl[4 * g + i]; v[i] = (acc[5][i] - st[0] * c) * st[1]; }
      u32x2 o = {pk2(v[0], v[1]), pk2(v[2], v[3])};
      const int d = part * 16 + t16;
      if (d == 20) o = u32x2{0x3f803f80u, 0x3f803f80u}; else if (d > 20) o = u32x2{0u, 0u};
      const int s = (tok0 >> 4) & 1;
      *(u32x2*)(kv + 2048 + s * 1024 + (g & 1) * 512 + d * 16 + (g >> 1) * 8) = o;
    }
  }
}

constexpr int AT_CH = 4, AT_SLOT = AT_CH * 4096, AT_LDS = 3 * AT_SLOT;
template <bool FAST>
DI void attn_loop(const char* kvg, LAS char* lds, unsigned lds0, int w, int lane, bf16x8 q0, bf16x8 q1, f32x16& o) {
  const char* src = kvg + w * 2048 + lane * 16;
  const LAS char* fb = lds + lane * 16;
  float m = -INFINITY;
  const f32x16 zero = {};
  for (int c = 0; c < 16; ++c) {
    asm volatile("s_waitcnt vmcnt(2)" ::: "memory");
    if (c == 15) asm volatile("s_waitcnt vmcnt(0)" ::: "memory");
    __builtin_amdgcn_s_barrier();
    if (c + 2 < 16) { unsigned dst = lds0 + ((c + 2) % 3) * AT_SLOT + w * 2048; glds16(src + (c + 2) * AT_SLOT, uni(dst)); glds16(src + (c + 2) * AT_SLOT + 1024, uni(dst + 1024)); }
    const LAS char* cb = fb + (c % 3) * AT_SLOT;
#pragma unroll
    for (int t = 0; t < AT_CH; ++t) {
      bf16x8 k0 = *(const LAS bf16x8*)(cb + t * 4096), k1 = *(const LAS bf16x8*)(cb + t * 4096 + 1024);
      bf16x8 v0 = *(const LAS bf16x8*)(cb + t * 4096 + 2048), v1 = *(const LAS bf16x8*)(cb + t * 4096 + 3072);
      f32x16 s = MFMA32(k0, q0, zero); s = MFMA32(k1, q1, s);
      if (!FAST) {
        float mx = s[0];
#pragma unroll
        for (int i = 1; i < 16; ++i) mx = fmaxf(mx, s[i]);
        mx = fmaxf(mx, __shfl_xor(mx, 32));
        float mn = fmaxf(m, mx), al = __builtin_amdgcn_exp2f(m - mn); m = mn;
#pragma unroll
        for (int i = 0; i < 16; ++i) { o[i] *= al; s[i] -= mn; }
      }
      u32x4 p0, p1;
#pragma unroll
      for (int i = 0; i < 4; ++i) { p0[i] = pk2(__builtin_amdgcn_exp2f(s[2 * i]), __builtin_amdgcn_exp2f(s[2 * i + 1])); p1[i] = pk2(__builtin_amdgcn_exp2f(s[8 + 2 * i]), __builtin_amdgcn_exp2f(s[9 + 2 * i])); }
      o = MFMA32(v0, __builtin_bit_cast(bf16x8, p0), o); o = MFMA32(v1, __builtin_bit_cast(bf16x8, p1), o);
    }
    asm volatile("s_waitcnt lgkmcnt(0)" ::: "memory");
  }
}
__global__ __launch_bounds__(512, 2) void k_attn(char* ws) {
  extern __shared__ __attribute__((aligned(16))) char smem[];
  LAS char* lds = (LAS char*)smem; const unsigned lds0 = (unsigned)(uintptr_t)lds;
  const int tid = threadIdx.x, lane = tid & 63, w = uni(tid >> 6), r = lane & 31, h = lane >> 5;
  const int xcd = blockIdx.x & 7, loc = blockIdx.x >> 3, mt = 4 * xcd + (loc >> 3), j = loc & 7;
  const int tok = mt * 64 + (w & 1) * 32 + r, head = j * 4 + (w >> 1);
  const char* kvg = ws + WS_KV;
  const u16* qp = (const u16*)(ws + WS_Q) + (size_t)tok * D + head * DH;
  u32x2 qa = *(const u32x2*)(qp + 8 * h), qb = *(const u32x2*)(qp + 8 * h + 4), qc = *(const u32x2*)(qp + 16);
  const float* kn = (const float*)(ws + WS_KN);
  float ka = fmaxf(kn[lane], kn[64 + lane]), kbm = fmaxf(kn[128 + lane], kn[192 + lane]);
  { unsigned dst = lds0 + w * 2048; const char* src = kvg + w * 2048 + lane * 16;
    glds16(src, uni(dst)); glds16(src + 1024, uni(dst + 1024)); glds16(src + AT_SLOT, uni(dst + AT_SLOT)); glds16(src + AT_SLOT + 1024, uni(dst + AT_SLOT + 1024)); }
#pragma unroll
  for (int m = 1; m < 64; m <<= 1) { ka = fmaxf(ka, __shfl_xor(ka, m)); kbm = fmaxf(kbm, __shfl_xor(kbm, m)); }
  float na = bf_lo(qa[0]) * bf_lo(qa[0]) + bf_hi(qa[0]) * bf_hi(qa[0]) + bf_lo(qa[1]) * bf_lo(qa[1]) + bf_hi(qa[1]) * bf_hi(qa[1])
           + bf_lo(qb[0]) * bf_lo(qb[0]) + bf_hi(qb[0]) * bf_hi(qb[0]) + bf_lo(qb[1]) * bf_lo(qb[1]) + bf_hi(qb[1]) * bf_hi(qb[1]);
  na += __shfl_xor(na, 32);
  float nb = bf_lo(qc[0]) * bf_lo(qc[0]) + bf_hi(qc[0]) * bf_hi(qc[0]) + bf_lo(qc[1]) * bf_lo(qc[1]) + bf_hi(qc[1]) * bf_hi(qc[1]);
  float bound = __builtin_sqrtf(na * ka) + __builtin_sqrtf(nb * kbm);
  const bool unsafe = !(bound <= 60.0f);
  if (h) qc = u32x2{0u, 0u};
  bf16x8 q0 = __builtin_bit_cast(bf16x8, u32x4{qa[0], qa[1], qb[0], qb[1]}), q1 = __builtin_bit_cast(bf16x8, u32x4{qc[0], qc[1], 0u, 0u});
  f32x16 o = {};
  if (__any(unsafe)) attn_loop<false>(kvg, lds, lds0, w, lane, q0, q1, o); else attn_loop<true>(kvg, lds, lds0, w, lane, q0, q1, o);
  float l = __shfl(o[8], r + 32), inv = 1.0f / l;
  const int m16 = tok >> 4, p = head >> 1, odd = head & 1;
  char* ob = ws + WS_O + (size_t)m16 * 20480 + (tok & 15) * 16;
  *(u32x4*)(ob + (5 * p + 2 * odd + h) * 256) = u32x4{pk2(o[0] * inv, o[1] * inv), pk2(o[2] * inv, o[3] * inv), pk2(o[4] * inv, o[5] * inv), pk2(o[6] * inv, o[7] * inv)};
  if (h == 0) *(u32x2*)(ob + (5 * p + 4) * 256 + odd * 8) = u32x2{pk2(o[8] * inv, o[9] * inv), pk2(o[10] * inv, o[11] * inv)};
}

template <int NKS, int WIN, bool FINAL>
__global__ __launch_bounds__(256, 1) void k_ksplit(const u16* x, char* ws, u16* out) {
  extern __shared__ __attribute__((aligned(16))) char smem[];
  LAS char* lds = (LAS char*)smem;
  const int tid = threadIdx.x, lane = tid & 63, w = uni(tid >> 6), t16 = lane & 15, g = lane >> 4;
  const int xcd = blockIdx.x & 7, loc = blockIdx.x >> 3, mt = 4 * xcd + (loc >> 3), j = loc & 7;
  constexpr int MAXS = (NKS + 3) / 4;
  const int ks0 = (NKS * w) / 4 + ((NKS % 4) ? ((w & 1) ? 1 : 0) * 0 : 0);
  const int ks1 = (NKS * (w + 1)) / 4, ns = ks1 - ks0;
  const char* A = ws + (FINAL ? WS_HH : WS_O) + ((size_t)(mt * 4) * NKS + ks0) * 1024 + lane * 16;
  const char* B = ws + (FINAL ? WS_W2 : WS_WO) + ((size_t)j * NKS + ks0) * 5120 + lane * 16;
  bf16x8 af[WIN][4], bfr[WIN][5];
  f32x4 acc[4][5];
#pragma unroll
  for (int m = 0; m < 4; ++m)
#pragma unroll
    for (int n = 0; n < 5; ++n) acc[m][n] = f32x4{};
  auto load = [&](int s) { int sc = (s < ns) ? s : ns - 1;
#pragma unroll
    for (int n = 0; n < 5; ++n) bfr[s % WIN][n] = *(const bf16x8*)(B + sc * 5120 + n * 1024);
#pragma unroll
    for (int m = 0; m < 4; ++m) af[s % WIN][m] = *(const bf16x8*)(A + (size_t)m * NKS * 1024 + sc * 1024); };
#pragma unroll
  for (int s = 0; s < WIN && s < MAXS; ++s) load(s);
  const int tok = mt * 64 + w * 16 + t16;
  u32x2 res[5];
#pragma unroll
  for (int n = 0; n < 5; ++n) {
    int t = j * 5 + n;
    if (FINAL) res[n] = *(const u32x2*)(ws + WS_H + ((size_t)(mt * 4 + w) * 20 + (t >> 1)) * 1024 + lane * 16 + (t & 1) * 8);
    else res[n] = *(const u32x2*)(x + (size_t)tok * D + t * 16 + g * 4);
  }
#pragma unroll
  for (int s = 0; s < MAXS; ++s) {
    if (s < ns) {
#pragma unroll
      for (int m = 0; m < 4; ++m)
#pragma unroll
        for (int n = 0; n < 5; ++n) acc[m][n] = MFMA16(bfr[s % WIN][n], af[s % WIN][m], acc[m][n]);
    }
    if (s + WIN < MAXS) load(s + WIN);
  }
#pragma unroll
  for (int m = 0; m < 4; ++m)
#pragma unroll
    for (int n = 0; n < 5; ++n) *(LAS f32x4*)(lds + ((w * 4 + m) * 5 + n) * 1024 + lane * 16) = acc[m][n];
  __syncthreads();
  float v[5][4];
#pragma unroll
  for (int n = 0; n < 5; ++n) {
    f32x4 a = *(LAS f32x4*)(lds + ((0 * 4 + w) * 5 + n) * 1024 + lane * 16) + *(LAS f32x4*)(lds + ((1 * 4 + w) * 5 + n) * 1024 + lane * 16)
            + *(LAS f32x4*)(lds + ((2 * 4 + w) * 5 + n) * 1024 + lane * 16) + *(LAS f32x4*)(lds + ((3 * 4 + w) * 5 + n) * 1024 + lane * 16);
    u32x2 o = {pk2(bf_lo(res[n][0]) + rbf(a[0]), bf_hi(res[n][0]) + rbf(a[1])), pk2(bf_lo(res[n][1]) + rbf(a[2]), bf_hi(res[n][1]) + rbf(a[3]))};
    int t = j * 5 + n;
    if (FINAL) *(u32x2*)(out + (size_t)tok * D + t * 16 + g * 4) = o;
    else { *(u32x2*)(ws + WS_H + ((size_t)(mt * 4 + w) * 20 + (t >> 1)) * 1024 + lane * 16 + (t & 1) * 8) = o;
      v[n][0] = bf_lo(o[0]); v[n][1] = bf_hi(o[0]); v[n][2] = bf_lo(o[1]); v[n][3] = bf_hi(o[1]); }
  }
  if (!FINAL) {
    float sm = 0.f;
#pragma unroll
    for (int n = 0; n < 5; ++n) sm += (v[n][0] + v[n][1]) + (v[n][2] + v[n][3]);
    sm += __shfl_xor(sm, 16); sm += __shfl_xor(sm, 32);
    float mean = sm * (1.0f / 80), m2 = 0.f;
#pragma unroll
    for (int n = 0; n < 5; ++n)
#pragma unroll
      for (int i = 0; i < 4; ++i) { float d = v[n][i] - mean; m2 += d * d; }
    m2 += __shfl_xor(m2, 16); m2 += __shfl_xor(m2, 32);
    if (g == 0) ((f32x2*)(ws + WS_ST))[tok * 8 + j] = f32x2{mean, m2};
  }
}

constexpr int UP_SLOT = 28672, UP_NSLOT = 5, UP_LDS_CS = UP_SLOT * UP_NSLOT, UP_LDS = UP_LDS_CS + 224 * 4;
__global__ __launch_bounds__(256, 1) void k_up(char* ws) {
  extern __shared__ __attribute__((aligned(16))) char smem[];
  LAS char* lds = (LAS char*)smem; const unsigned lds0 = (unsigned)(uintptr_t)lds;
  const int tid = threadIdx.x, lane = tid & 63, w = uni(tid >> 6), t16 = lane & 15, g = lane >> 4;
  const int xcd = blockIdx.x & 7, loc = blockIdx.x >> 3, mb = 2 * xcd + (loc >> 4), fs = loc & 15;
  const int t0 = (fs >> 2) * 27 + (fs & 3) * 7, ntl = (fs & 3) == 3 ? 6 : 7;
  const char* Bg = ws + WS_W13 + lane * 16;
  auto dma = [&](int c) {
#pragma unroll
    for (int i = 0; i < 7; ++i) { int f = i * 4 + w, nt = f >> 2, ks = 2 * c + ((f >> 1) & 1), mat = f & 1; int t = t0 + (nt < ntl ? nt : 0);
      glds16(Bg + ((size_t)(t * 20 + ks) * 2 + mat) * 1024, uni(lds0 + (c % UP_NSLOT) * UP_SLOT + f * 1024)); } };
  bf16x8 af[2][20];
  const char* Ag = ws + WS_H + (size_t)(mb * 8 + w * 2) * 20480 + lane * 16;
#pragma unroll
  for (int s = 0; s < 20; ++s) { af[0][s] = *(const bf16x8*)(Ag + s * 1024); af[1][s] = *(const bf16x8*)(Ag + 20480 + s * 1024); }
  float mu[2], rs[2];
#pragma unroll
  for (int m = 0; m < 2; ++m) {
    const f32x4* sp = (const f32x4*)(ws + WS_ST + (size_t)(mb * 128 + w * 32 + m * 16 + t16) * 64);
    f32x4 a = sp[0], b = sp[1], c = sp[2], d = sp[3];
    float mean = (a[0] + a[2] + b[0] + b[2] + c[0] + c[2] + d[0] + d[2]) * 0.125f;
    float m2 = a[1] + a[3] + b[1] + b[3] + c[1] + c[3] + d[1] + d[3];
    float e0 = a[0] - mean, e1 = a[2] - mean, e2 = b[0] - mean, e3 = b[2] - mean, e4 = c[0] - mean, e5 = c[2] - mean, e6 = d[0] - mean, e7 = d[2] - mean;
    m2 += 80.f * (e0 * e0 + e1 * e1 + e2 * e2 + e3 * e3 + e4 * e4 + e5 * e5 + e6 * e6 + e7 * e7);
    ln_consts(mean, m2 * (1.0f / D), mu[m], rs[m]);
  }
  if (tid < 224) { int mat = tid / 112, n = tid - mat * 112; int col = t0 * 16 + n; col = col < F ? col : F - 1; float s = 0.f;
#pragma unroll
    for (int ks = 0; ks < 20; ++ks) s += ((const float*)(ws + WS_CS))[(ks * 2 + mat) * F + col];
    ((LAS float*)(lds + UP_LDS_CS))[tid] = s; }
  asm volatile("s_waitcnt vmcnt(0)" ::: "memory");
#pragma unroll
  for (int c = 0; c < UP_NSLOT - 1; ++c) dma(c);
  f32x4 acc[2][7][2];
#pragma unroll
  for (int m = 0; m < 2; ++m)
#pragma unroll
    for (int n = 0; n < 7; ++n) { acc[m][n][0] = f32x4{}; acc[m][n][1] = f32x4{}; }
#pragma unroll
  for (int c = 0; c < 10; ++c) {
    if (c <= 6) asm volatile("s_waitcnt vmcnt(21) lgkmcnt(0)" ::: "memory");
    else if (c == 7) asm volatile("s_waitcnt vmcnt(14) lgkmcnt(0)" ::: "memory");
    else if (c == 8) asm volatile("s_waitcnt vmcnt(7) lgkmcnt(0)" ::: "memory");
    else asm volatile("s_waitcnt vmcnt(0) lgkmcnt(0)" ::: "memory");
    __builtin_amdgcn_s_barrier();
    if (c + UP_NSLOT - 1 < 10) dma(c + UP_NSLOT - 1);
    const LAS char* cb = lds + (c % UP_NSLOT) * UP_SLOT + lane * 16;
#pragma unroll
    for (int kk = 0; kk < 2; ++kk)
#pragma unroll
      for (int n = 0; n < 7; ++n)
#pragma unroll
        for (int mat = 0; mat < 2; ++mat) {
          bf16x8 bfr = *(const LAS bf16x8*)(cb + (n * 4 + kk * 2 + mat) * 1024);
          acc[0][n][mat] = MFMA16(bfr, af[0][2 * c + kk], acc[0][n][mat]);
          acc[1][n][mat] = MFMA16(bfr, af[1][2 * c + kk], acc[1][n][mat]);
        }
  }
  const LAS float* csl = (const LAS float*)(lds + UP_LDS_CS);
#pragma unroll
  for (int n = 0; n < 7; ++n) {
    if (n < ntl) {
      f32x4 c1 = *(const LAS f32x4*)(csl + n * 16 + g * 4), c3 = *(const LAS f32x4*)(csl + 112 + n * 16 + g * 4);
      int t = t0 + n;
#pragma unroll
      for (int m = 0; m < 2; ++m) {
        f32x4 gt = (acc[m][n][0] - mu[m] * c1) * rs[m], up = (acc[m][n][1] - mu[m] * c3) * rs[m]; float hv[4];
#pragma unroll
        for (int i = 0; i < 4; ++i) hv[i] = gt[i] * __builtin_amdgcn_rcpf(1.0f + __builtin_amdgcn_exp2f(gt[i] * -1.4426950408889634f)) * up[i];
        *(u32x2*)(ws + WS_HH + ((size_t)(mb * 8 + w * 2 + m) * 54 + (t >> 1)) * 1024 + lane * 16 + (t & 1) * 8) = u32x2{pk2(hv[0], hv[1]), pk2(hv[2], hv[3])};
      }
    }
  }
}

extern "C" void kernel_launch(void* const* d_in, const int* in_sizes, int n_in, void* d_out, int out_size, void* d_ws, size_t ws_size, hipStream_t stream) {
  const u16* x = (const u16*)d_in[0]; const u16* wq = (const u16*)d_in[1]; const u16* wk = (const u16*)d_in[2]; const u16* wv = (const u16*)d_in[3];
  const u16* wo = (const u16*)d_in[4]; const u16* w1 = (const u16*)d_in[6]; const u16* w3 = (const u16*)d_in[7]; const u16* w2 = (const u16*)d_in[8];
  char* ws = (char*)d_ws; u16* out = (u16*)d_out;
  (void)hipFuncSetAttribute((const void*)k_qkv, hipFuncAttributeMaxDynamicSharedMemorySize, K1_LDS);
  (void)hipFuncSetAttribute((const void*)k_up, hipFuncAttributeMaxDynamicSharedMemorySize, UP_LDS);
  (void)hipFuncSetAttribute((const void*)k_ksplit<20, 5, false>, hipFuncAttributeMaxDynamicSharedMemorySize, 81920);
  (void)hipFuncSetAttribute((const void*)k_ksplit<54, 5, true>, hipFuncAttributeMaxDynamicSharedMemorySize, 81920);
  k_pretile<<<455, 256, 0, stream>>>(wo, w1, w3, w2, ws);
  k_qkv<<<256, 256, K1_LDS, stream>>>(x, wq, wk, wv, ws);
  k_attn<<<256, 512, AT_LDS, stream>>>(ws);
  k_ksplit<20, 5, false><<<256, 256, 81920, stream>>>(x, ws, out);
  k_up<<<256, 256, UP_LDS, stream>>>(ws);
  k_ksplit<54, 5, true><<<256, 256, 81920, stream>>>(x, ws, out);
}
```

```cpp
#include <hip/hip_runtime.h>
#include <stdint.h>

constexpr size_t WS_EPOCH = 0;
constexpr size_t WS_KVF = 4096;
constexpr size_t WS_RDY = 32768;
constexpr size_t WS_KV = 131072;
constexpr size_t WS_O = 655360;
constexpr size_t WS_H = 3276800;
constexpr size_t WS_ST = 5898240;
constexpr size_t WS_HH = 6160384;
constexpr size_t WS_WO = 13238272;
constexpr size_t WS_W13 = 14057472;
constexpr size_t WS_W2 = 18591744;
constexpr size_t WS_END = 24997888;
constexpr size_t WS_DBG = 24997888;

__global__ __launch_bounds__(256, 1) void fused_block(const void* x, const void* wq, const void* wk, const void* wv, const void* wo,
                                                      const void* w1, const void* w3, const void* w2, void* out, void* ws) {
  const unsigned wave = __builtin_amdgcn_readfirstlane(threadIdx.x >> 6);
  const unsigned bid = blockIdx.x;
  asm volatile(
".Lmk_start:\n"
"s_load_dwordx16 s[36:51], %0, 0\n"
"s_load_dwordx4 s[52:55], %0, 64\n"
"s_getpc_b64 s[4:5]\n"
".Lmk_pc:\n"
"s_mov_b32 s56, %2\n"
"s_mov_b32 s62, %1\n"
"v_mbcnt_lo_u32_b32 v2, -1, 0\n"
"v_mbcnt_hi_u32_b32 v2, -1, v2\n"
"s_lshr_b32 s63, s62, 3\n"
"s_lshl_b32 s6, s63, 2\n"
"s_add_u32 s6, s6, s56\n"
"v_lshlrev_b32 v25, 7, v2\n"
"s_cmp_ge_u32 s63, 4\n"
"s_cbranch_scc1 .Lp0_nopf\n"
"s_and_b32 s7, s6, 7\n"
"s_lshl_b32 s7, s7, 13\n"
"v_add_u32 v7, s7, v25\n"
"v_min_u32 v7, .Lmk_end-.Lmk_pc-4, v7\n"
"global_load_dword v255, v7, s[4:5]\n"
".Lp0_nopf:\n"
"s_and_b32 s4, s62, 6\n"
"s_lshr_b32 s5, s63, 2\n"
"s_lshl2_add_u32 s57, s4, s5\n"
"s_and_b32 s18, s62, 1\n"
"s_and_b32 s7, s63, 3\n"
"s_add_u32 s5, s5, s7\n"
"s_lshl_b32 s5, s5, 2\n"
"s_mov_b32 s84, 0x21043210\n"
"s_mov_b32 s85, 0x04321043\n"
"s_lshr_b64 s[84:85], s[84:85], s5\n"
"s_lshl2_add_u32 s58, s18, s7\n"
"s_lshl_b32 s4, s57, 2\n"
"s_add_u32 s4, s4, s56\n"
"s_mul_i32 s4, s4, 20480\n"
"v_lshrrev_b32 v5, 2, v2\n"
"v_mul_u32_u24 v254, 1280, v5\n"
"v_and_b32 v9, 3, v2\n"
"v_lshl_add_u32 v5, v9, 4, v254\n"
"v_lshlrev_b32 v0, 4, v2\n"
"s_mul_i32 s86, s56, 5120\n"
"s_add_u32 m0, s86, 122880\n"
"s_lshl_b32 s8, s56, 2\n"
"s_lshr_b32 s8, s84, s8\n"
"s_and_b32 s8, s8, 15\n"
"s_mul_i32 s8, s8, 5120\n"
"s_bfe_u32 s7, s84, 0x40010\n"
"s_mul_i32 s7, s7, 5120\n"
"s_add_u32 s11, s7, 4096\n"
"s_lshl_b32 s10, s56, 10\n"
"s_add_u32 s7, s7, s10\n"
"s_add_u32 s10, s10, 143360\n"
"s_and_b32 s85, s84, 15\n"
"s_lshl_b32 s85, s85, 8\n"
"s_add_u32 s85, s85, s4\n"
"s_mul_i32 s9, s56, 20480\n"
"s_add_u32 s9, s9, 40960\n"
"s_mov_b32 s26, 2621440\n"
"s_mov_b32 s27, 0x20000\n"
"s_mov_b32 s30, 819200\n"
"s_mov_b32 s31, 0x20000\n"
"s_cmp_lt_u32 s58, 4\n"
"s_cselect_b32 s90, 25600, 0\n"
"s_mov_b32 s91, 0x20000\n"
"s_cmp_lt_u32 s58, 2\n"
"s_waitcnt lgkmcnt(0)\n"
"s_cselect_b32 s88, s40, s42\n"
"s_cselect_b32 s89, s41, s43\n"
"s_and_b32 s89, s89, 0xffff\n"
"s_mov_b32 s24, s36\n"
"s_and_b32 s25, s37, 0xffff\n"
"s_mov_b32 s28, s38\n"
"s_and_b32 s29, s39, 0xffff\n"
"buffer_load_dwordx4 v0, s[88:91], s8 offen offset:0 lds\n"
"buffer_load_dwordx4 v0, s[88:91], s8 offen offset:1024 lds\n"
"buffer_load_dwordx4 v0, s[88:91], s8 offen offset:2048 lds\n"
"buffer_load_dwordx4 v0, s[88:91], s8 offen offset:3072 lds\n"
"s_add_u32 m0, m0, 4096\n"
"s_add_u32 s8, s8, 4096\n"
"buffer_load_dwordx4 v0, s[88:91], s8 offen lds\n"
"s_mov_b32 m0, s10\n"
"s_nop 0\n"
"buffer_load_dwordx4 v0, s[88:91], s7 offen lds\n"
"s_mov_b32 m0, 147456\n"
"s_nop 0\n"
"buffer_load_dwordx4 v0, s[88:91], s11 offen lds\n"
"s_mov_b32 m0, s9\n"
"s_nop 0\n"
"buffer_load_dwordx4 v5, s[24:27], s85 offen offset:0 lds\n"
"s_add_u32 m0, m0, 960\n"
"s_add_u32 s54, s54, 0x1ffff\n"
"s_addc_u32 s55, s55, 0\n"
"s_and_b32 s54, s54, 0xfffe0000\n"
"s_cmp_gt_u32 s54, 0xfc000000\n"
"s_cselect_b32 s20, 0x4000000, 0\n"
"s_add_u32 s54, s54, s20\n"
"s_addc_u32 s55, s55, 0\n"
"s_mov_b32 s20, s54\n"
"s_and_b32 s21, s55, 0xffff\n"
"s_mov_b32 s22, 24997888\n"
"s_mov_b32 s23, 0x20000\n"
"v_and_b32 v3, 15, v2\n"
"v_lshrrev_b32 v4, 4, v2\n"
"s_mov_b32 s97, 0\n"
"s_mov_b32 s35, s21\n"
"s_mov_b32 s65, s21\n"
"s_mov_b32 s69, s21\n"
"v_mov_b32 v188, 0x3f803f80\n"
"buffer_load_dwordx4 v5, s[24:27], s85 offen offset:64 lds\n"
"s_add_u32 m0, m0, 960\n"
"v_mov_b32 v189, 0x3f803f80\n"
"v_mov_b32 v190, 0x3f803f80\n"
"v_mov_b32 v191, 0x3f803f80\n"
"v_lshrrev_b32 v7, 2, v3\n"
"v_lshl_add_u32 v248, v4, 3, v7\n"
"v_lshrrev_b32 v8, 2, v2\n"
"v_mul_u32_u24 v8, 160, v8\n"
"v_mul_u32_u24 v248, 40, v248\n"
"v_lshl_add_u32 v248, v9, 3, v248\n"
"v_lshlrev_b32 v249, 6, v3\n"
"v_lshl_add_u32 v249, v4, 4, v249\n"
"v_lshl_add_u32 v8, v9, 3, v8\n"
"s_and_b32 s5, s58, 1\n"
"s_lshl_b32 s5, s5, 5\n"
"s_add_u32 s5, s5, 122880\n"
"v_add_u32 v248, s5, v248\n"
"buffer_load_dwordx4 v5, s[24:27], s85 offen offset:128 lds\n"
"s_add_u32 m0, m0, 960\n"
"v_add_u32 v249, s9, v249\n"
"s_add_u32 s5, s56, 0\n"
"s_and_b32 s5, s5, 3\n"
"s_mul_i32 s7, s5, 1280\n"
"v_add_u32 v223, s7, v248\n"
"s_lshl_b32 s7, s5, 10\n"
"v_add_u32 v227, s7, v249\n"
"s_mul_i32 s7, s5, 5120\n"
"v_add_u32 v204, s7, v8\n"
"s_add_u32 s5, s56, 1\n"
"s_and_b32 s5, s5, 3\n"
"s_mul_i32 s7, s5, 1280\n"
"v_add_u32 v224, s7, v248\n"
"s_lshl_b32 s7, s5, 10\n"
"v_add_u32 v228, s7, v249\n"
"s_mul_i32 s7, s5, 5120\n"
"buffer_load_dwordx4 v5, s[24:27], s85 offen offset:192 lds\n"
"s_add_u32 m0, m0, 1216\n"
"s_bfe_u32 s85, s84, 0x40004\n"
"s_lshl_b32 s85, s85, 8\n"
"s_add_u32 s85, s85, s4\n"
"v_add_u32 v205, s7, v8\n"
"s_add_u32 s5, s56, 2\n"
"s_and_b32 s5, s5, 3\n"
"s_mul_i32 s7, s5, 1280\n"
"v_add_u32 v225, s7, v248\n"
"s_lshl_b32 s7, s5, 10\n"
"v_add_u32 v229, s7, v249\n"
"s_mul_i32 s7, s5, 5120\n"
"v_add_u32 v206, s7, v8\n"
"s_add_u32 s5, s56, 3\n"
"s_and_b32 s5, s5, 3\n"
"s_mul_i32 s7, s5, 1280\n"
"v_add_u32 v226, s7, v248\n"
"s_lshl_b32 s7, s5, 10\n"
"v_add_u32 v230, s7, v249\n"
"s_mul_i32 s7, s5, 5120\n"
"buffer_load_dwordx4 v5, s[24:27], s85 offen offset:0 lds\n"
"s_add_u32 m0, m0, 960\n"
"v_add_u32 v207, s7, v8\n"
"v_mov_b32 v184, 0\n"
"v_mov_b32 v185, 0\n"
"v_mov_b32 v186, 151296\n"
"ds_write_b64 v186, v[184:185]\n"
"v_cmp_eq_u32 s[12:13], 1, v9\n"
"v_cmp_eq_u32 s[14:15], 2, v9\n"
"v_cmp_eq_u32 s[16:17], 3, v9\n"
"v_lshl_add_u32 v242, v7, 4, v3\n"
"v_lshlrev_b32 v242, 2, v242\n"
"s_mul_i32 s5, s58, 160\n"
"s_lshr_b32 s7, s56, 1\n"
"s_mul_i32 s7, s7, -40864\n"
"s_add_u32 s7, s7, s4\n"
"s_add_u32 s7, s7, s5\n"
"v_lshl_add_u32 v254, v9, 3, v254\n"
"buffer_load_dwordx4 v5, s[24:27], s85 offen offset:64 lds\n"
"s_add_u32 m0, m0, 960\n"
"v_add_u32 v24, s7, v254\n"
"v_add_u32 v1, 4096, v0\n"
"v_mul_u32_u24 v7, 205, v2\n"
"s_movk_i32 s7, 1120\n"
"s_movk_i32 s8, 1280\n"
"v_lshrrev_b32 v8, 11, v7\n"
"v_mad_u32_u24 v216, v8, s7, v0\n"
"v_and_b32 v250, 12, v8\n"
"v_mad_i32_i24 v216, v250, s8, v216\n"
"v_add_u32 v7, 13120, v7\n"
"v_lshrrev_b32 v8, 11, v7\n"
"v_mad_u32_u24 v217, v8, s7, v0\n"
"v_and_b32 v250, 12, v8\n"
"v_mad_i32_i24 v217, v250, s8, v217\n"
"v_add_u32 v7, 13120, v7\n"
"v_lshrrev_b32 v8, 11, v7\n"
"buffer_load_dwordx4 v5, s[24:27], s85 offen offset:128 lds\n"
"s_add_u32 m0, m0, 960\n"
"v_mad_u32_u24 v218, v8, s7, v0\n"
"v_and_b32 v250, 12, v8\n"
"v_lshrrev_b32 v8, 4, v8\n"
"v_mad_i32_i24 v250, v8, -12, v250\n"
"v_mad_i32_i24 v218, v250, s8, v218\n"
"v_add_u32 v7, 13120, v7\n"
"v_lshrrev_b32 v8, 11, v7\n"
"v_mad_u32_u24 v219, v8, s7, v0\n"
"v_and_b32 v250, 12, v8\n"
"v_add_u32 v250, -12, v250\n"
"v_mad_i32_i24 v219, v250, s8, v219\n"
"v_add_u32 v7, 13120, v7\n"
"v_lshrrev_b32 v8, 11, v7\n"
"v_mad_u32_u24 v220, v8, s7, v1\n"
"v_and_b32 v250, 12, v8\n"
"v_add_u32 v250, -12, v250\n"
"buffer_load_dwordx4 v5, s[24:27], s85 offen offset:192 lds\n"
"s_add_u32 m0, m0, 1216\n"
"s_bfe_u32 s85, s84, 0x40008\n"
"s_lshl_b32 s85, s85, 8\n"
"s_add_u32 s85, s85, s4\n"
"v_mad_i32_i24 v220, v250, s8, v220\n"
"s_mul_i32 s10, s56, 40960\n"
"s_add_u32 s10, s10, s5\n"
"s_and_b32 s72, s57, 24\n"
"s_add_u32 s72, s72, s57\n"
"s_lshl_b32 s72, s72, 13\n"
"s_add_u32 s72, s72, 131072\n"
"v_add_u32 v247, 151552, v0\n"
"s_and_b32 s73, s58, 1\n"
"s_cselect_b32 s80, 0xffff0000, 0\n"
"s_cselect_b32 s81, -1, 0\n"
"v_cmp_eq_u32 vcc, 1, v4\n"
"v_mov_b32 v7, 0x3f80\n"
"s_cmp_ge_u32 s58, 2\n"
"s_cbranch_scc1 .Lst_v\n"
"v_cndmask_b32 v244, 0, v188, vcc\n"
"v_cndmask_b32 v245, 0, v7, vcc\n"
"v_mov_b32 v246, v0\n"
"s_lshl_b32 s73, s73, 11\n"
"s_cselect_b64 s[74:75], -1, 0\n"
"s_cselect_b32 s76, 30, 10\n"
"s_cselect_b32 s77, 3, 10\n"
"s_add_u32 s72, s72, s73\n"
"s_lshl_b32 s76, 1, s76\n"
"s_add_u32 s73, s72, s76\n"
"s_lshr_b32 s7, s56, 1\n"
"s_lshl_b32 s7, s7, s77\n"
"s_and_b32 s5, s56, 1\n"
"s_lshl_b32 s5, s5, 8\n"
"s_add_u32 s5, s5, s7\n"
"s_add_u32 s5, s5, 151552\n"
"v_and_b32 v7, 1, v4\n"
"v_mul_u32_u24 v7, 248, v7\n"
"v_lshlrev_b32 v8, 8, v4\n"
"v_sub_u32 v7, v8, v7\n"
"v_lshlrev_b32 v8, 9, v4\n"
"v_cndmask_b32 v7, v7, v8, s[74:75]\n"
"v_lshl_add_u32 v7, v3, 4, v7\n"
"v_add_u32 v240, s5, v7\n"
"s_branch .Lst_j\n"
".Lst_v:\n"
"v_cndmask_b32 v244, 0, v7, vcc\n"
"v_mov_b32 v245, 0\n"
"v_lshlrev_b32 v246, 9, v4\n"
"v_lshl_add_u32 v246, v3, 4, v246\n"
"s_lshl_b32 s73, s73, 8\n"
"s_add_u32 s72, s72, s73\n"
"s_add_u32 s72, s72, 3072\n"
"s_add_u32 s73, s72, 2048\n"
"s_lshl_b32 s5, s56, 9\n"
"s_add_u32 s5, s5, 151552\n"
"v_lshl_add_u32 v241, v4, 6, s5\n"
"v_and_b32 v7, 4, v3\n"
"v_lshl_add_u32 v241, v7, 6, v241\n"
"v_and_b32 v7, 8, v3\n"
"v_add_u32 v241, v241, v7\n"
"v_lshl_add_u32 v241, v9, 1, v241\n"
".Lst_j:\n"
"s_cmp_lt_u32 s58, 4\n"
"s_cselect_b32 s96, s56, 0\n"
"buffer_load_dwordx4 v5, s[24:27], s85 offen offset:0 lds\n"
"s_add_u32 m0, m0, 960\n"
"s_nop 0\n"
"buffer_load_dwordx4 v5, s[24:27], s85 offen offset:64 lds\n"
"s_add_u32 m0, m0, 960\n"
"s_mov_b64 exec, 0\n"
"s_cmp_eq_u32 s56, 0\n"
"s_cbranch_scc1 .Lhop0\n"
"s_cmp_eq_u32 s56, 1\n"
"s_cbranch_scc1 .Lhop1\n"
"s_cmp_eq_u32 s56, 2\n"
"s_cbranch_scc1 .Lhop2\n"
"s_branch .Lhop3\n"
".Lhop_ret:\n"
"s_mov_b64 exec, -1\n"
"s_waitcnt vmcnt(10)\n"
"s_waitcnt lgkmcnt(0)\n"
"s_barrier\n"
"s_waitcnt vmcnt(6)\n"
"ds_read_b128 v[32:35], v227 offset:0\n"
"ds_read_b64_tr_b16 a[184:185], v223 offset:0\n"
"ds_read_b64_tr_b16 a[186:187], v223 offset:160\n"
"ds_read_b128 v[36:39], v228 offset:0\n"
"ds_read_b64_tr_b16 a[188:189], v224 offset:0\n"
"ds_read_b64_tr_b16 a[190:191], v224 offset:160\n"
"ds_read_b128 v[40:43], v229 offset:0\n"
"ds_read_b64_tr_b16 a[192:193], v225 offset:0\n"
"ds_read_b64_tr_b16 a[194:195], v225 offset:160\n"
"buffer_load_dwordx4 v5, s[24:27], s85 offen offset:128 lds\n"
"s_add_u32 m0, m0, 960\n"
"s_nop 0\n"
"buffer_load_dwordx4 v5, s[24:27], s85 offen offset:192 lds\n"
"s_add_u32 m0, m0, 1216\n"
"s_waitcnt lgkmcnt(6)\n"
"v_mfma_f32_16x16x32_bf16 v[176:179], v[32:35], v[32:35], 0\n"
"s_bfe_u32 s85, s84, 0x4000c\n"
"ds_read_b128 v[44:47], v230 offset:0\n"
"v_and_b32 v248, 31, v2\n"
"v_mfma_f32_16x16x32_bf16 v[172:175], v[188:191], v[32:35], 0\n"
"s_lshl_b32 s85, s85, 8\n"
"ds_read_b64_tr_b16 a[196:197], v226 offset:0\n"
"v_lshrrev_b32 v249, 5, v2\n"
"v_mfma_f32_16x16x32_bf16 v[180:183], a[184:187], v[32:35], 0\n"
"s_add_u32 s85, s85, s4\n"
"ds_read_b64_tr_b16 a[198:199], v226 offset:160\n"
"v_mul_u32_u24 v248, 48, v248\n"
"v_mfma_f32_16x16x32_bf16 v[184:187], a[184:187], v[188:191], 0\n"
"s_waitcnt lgkmcnt(6)\n"
"buffer_load_dwordx4 v5, s[24:27], s85 offen offset:0 lds\n"
"v_mfma_f32_16x16x32_bf16 v[176:179], v[36:39], v[36:39], v[176:179]\n"
"s_add_u32 m0, m0, 960\n"
"s_waitcnt vmcnt(5)\n"
"ds_read_b128 v[48:51], v227 offset:4096\n"
"v_add_u32 v248, 151552, v248\n"
"v_mfma_f32_16x16x32_bf16 v[172:175], v[188:191], v[36:39], v[172:175]\n"
"buffer_load_dwordx4 v5, s[24:27], s85 offen offset:64 lds\n"
"ds_read_b64_tr_b16 a[200:201], v223 offset:5120\n"
"v_mfma_f32_16x16x32_bf16 v[180:183], a[188:191], v[36:39], v[180:183]\n"
"s_add_u32 m0, m0, 960\n"
"ds_read_b64_tr_b16 a[202:203], v223 offset:5280\n"
"v_lshl_add_u32 v26, v249, 4, v248\n"
"v_mfma_f32_16x16x32_bf16 v[184:187], a[188:191], v[188:191], v[184:187]\n"
"s_waitcnt lgkmcnt(6)\n"
"buffer_load_dwordx4 v5, s[24:27], s85 offen offset:128 lds\n"
"v_mfma_f32_16x16x32_bf16 v[176:179], v[40:43], v[40:43], v[176:179]\n"
"s_add_u32 m0, m0, 960\n"
"ds_read_b128 v[52:55], v228 offset:4096\n"
"v_lshl_add_u32 v27, v249, 3, v248\n"
"v_mfma_f32_16x16x32_bf16 v[172:175], v[188:191], v[40:43], v[172:175]\n"
"buffer_load_dwordx4 v5, s[24:27], s85 offen offset:192 lds\n"
"ds_read_b64_tr_b16 a[204:205], v224 offset:5120\n"
"v_mfma_f32_16x16x32_bf16 v[180:183], a[192:195], v[40:43], v[180:183]\n"
"s_add_u32 m0, m0, 1216\n"
"ds_read_b64_tr_b16 a[206:207], v224 offset:5280\n"
"v_add_u32 v28, 122880, v0\n"
"v_mfma_f32_16x16x32_bf16 v[184:187], a[192:195], v[188:191], v[184:187]\n"
"s_waitcnt lgkmcnt(6)\n"
"s_and_b32 s64, s63, 7\n"
"s_lshl4_add_u32 s64, s56, s64\n"
"v_mfma_f32_16x16x32_bf16 v[176:179], v[44:47], v[44:47], v[176:179]\n"
"s_bfe_u32 s85, s84, 0x40010\n"
"ds_read_b128 v[56:59], v229 offset:4096\n"
"s_lshl_b32 s64, s64, 13\n"
"v_mfma_f32_16x16x32_bf16 v[172:175], v[188:191], v[44:47], v[172:175]\n"
"s_lshl_b32 s85, s85, 8\n"
"ds_read_b64_tr_b16 a[208:209], v225 offset:5120\n"
"s_add_u32 s64, s64, 131072\n"
"v_mfma_f32_16x16x32_bf16 v[180:183], a[196:199], v[44:47], v[180:183]\n"
"s_add_u32 s85, s85, s4\n"
"ds_read_b64_tr_b16 a[210:211], v225 offset:5280\n"
"s_add_u32 s34, s64, s20\n"
"v_mfma_f32_16x16x32_bf16 v[184:187], a[196:199], v[188:191], v[184:187]\n"
"s_waitcnt lgkmcnt(6)\n"
"buffer_load_dwordx4 v5, s[24:27], s85 offen offset:0 lds\n"
"v_mfma_f32_16x16x32_bf16 v[176:179], v[48:51], v[48:51], v[176:179]\n"
"s_add_u32 m0, m0, 960\n"
"ds_read_b128 v[60:63], v230 offset:4096\n"
"s_lshl2_add_u32 s79, s62, s56\n"
"v_mfma_f32_16x16x32_bf16 v[172:175], v[188:191], v[48:51], v[172:175]\n"
"buffer_load_dwordx4 v5, s[24:27], s85 offen offset:64 lds\n"
"ds_read_b64_tr_b16 a[212:213], v226 offset:5120\n"
"v_mfma_f32_16x16x32_bf16 v[180:183], a[200:203], v[48:51], v[180:183]\n"
"s_add_u32 m0, m0, 960\n"
"ds_read_b64_tr_b16 a[214:215], v226 offset:5280\n"
"s_lshl_b32 s79, s79, 12\n"
"v_mfma_f32_16x16x32_bf16 v[184:187], a[200:203], v[188:191], v[184:187]\n"
"s_waitcnt lgkmcnt(6)\n"
"buffer_load_dwordx4 v5, s[24:27], s85 offen offset:128 lds\n"
"v_mfma_f32_16x16x32_bf16 v[176:179], v[52:55], v[52:55], v[176:179]\n"
"s_add_u32 m0, m0, 960\n"
"s_waitcnt vmcnt(7)\n"
"ds_read_b128 v[64:67], v227 offset:8192\n"
"s_add_u32 s79, s79, 20803584\n"
"v_mfma_f32_16x16x32_bf16 v[172:175], v[188:191], v[52:55], v[172:175]\n"
"buffer_load_dwordx4 v5, s[24:27], s85 offen offset:192 lds\n"
"ds_read_b64_tr_b16 a[184:185], v223 offset:10240\n"
"v_mfma_f32_16x16x32_bf16 v[180:183], a[204:207], v[52:55], v[180:183]\n"
"s_cmp_ge_u32 s56, 2\n"
"s_cbranch_scc1 .Ljob_b\n"
"s_cmp_eq_u32 s56, 1\n"
"s_cselect_b32 s88, s48, s46\n"
"s_cselect_b32 s89, s49, s47\n"
"s_mul_i32 s68, s62, 19\n"
"s_lshr_b32 s68, s68, 9\n"
"s_mul_i32 s36, s68, 273024\n"
"s_lshl_b32 s77, s62, 7\n"
"s_add_u32 s36, s36, s77\n"
"s_mul_i32 s70, s68, 27\n"
"s_sub_u32 s70, s62, s70\n"
"s_mul_i32 s38, s70, 167936\n"
"s_add_u32 s38, s38, 14057472\n"
"s_lshr_b32 s77, s68, 1\n"
"s_mul_i32 s77, s77, 248\n"
"s_lshl2_add_u32 s77, s68, s77\n"
"s_lshl3_add_u32 s77, s56, s77\n"
"s_add_u32 s77, s77, 40960\n"
"s_add_u32 s42, s38, s77\n"
"s_lshl3_add_u32 s77, s56, s68\n"
"s_mul_i32 s77, s77, 2560\n"
"s_add_u32 s38, s38, s77\n"
"s_mov_b32 s67, 41984\n"
"s_mov_b32 s68, 3456\n"
"s_mov_b32 s19, 0\n"
"s_mov_b32 s90, 2211840\n"
"s_cmp_ge_u32 s62, 216\n"
"s_branch .Ljob_j\n"
".Ljob_b:\n"
"s_lshl_b32 s64, s56, 8\n"
"s_add_u32 s64, s64, s62\n"
"s_sub_u32 s64, s64, 512\n"
"s_cmp_ge_u32 s64, 270\n"
"s_cselect_b32 s66, 270, 0\n"
"s_cselect_b32 s67, 20, 54\n"
"s_cselect_b32 s88, s44, s50\n"
"s_cselect_b32 s89, s45, s51\n"
"s_cselect_b32 s78, 4289613824, 0\n"
"s_mul_i32 s90, s67, 40960\n"
"s_sub_u32 s66, s64, s66\n"
"s_mul_i32 s68, s66, 205\n"
"s_lshr_b32 s68, s68, 11\n"
"s_mul_i32 s36, s68, 80640\n"
"s_lshl_b32 s77, s66, 7\n"
"s_add_u32 s36, s36, s77\n"
"s_mul_i32 s70, s68, 10\n"
"s_sub_u32 s70, s66, s70\n"
"s_mul_i32 s77, s70, s67\n"
"s_lshl1_add_u32 s77, s77, s68\n"
"s_lshl_b32 s77, s77, 11\n"
"s_add_u32 s77, s77, s78\n"
"s_add_u32 s38, s77, 18591744\n"
"s_lshl_b32 s67, s67, 10\n"
"s_mov_b32 s68, 1280\n"
"s_mov_b32 s19, 1073741824\n"
"s_mov_b32 s42, 1073741824\n"
"s_cmp_ge_u32 s64, 370\n"
".Ljob_j:\n"
"s_cselect_b32 s90, 0, s90\n"
"s_cselect_b32 s38, s79, s38\n"
"s_cselect_b32 s67, 0, s67\n"
"s_cselect_b32 s42, 1073741824, s42\n"
"s_and_b32 s89, s89, 0xffff\n"
"ds_read_b64_tr_b16 a[186:187], v223 offset:10400\n"
"v_lshrrev_b32 v248, 3, v2\n"
"v_mfma_f32_16x16x32_bf16 v[184:187], a[204:207], v[188:191], v[184:187]\n"
"s_waitcnt lgkmcnt(6)\n"
"buffer_load_dword v6, off, s[20:23], 0 sc1\n"
"v_mfma_f32_16x16x32_bf16 v[176:179], v[56:59], v[56:59], v[176:179]\n"
"s_bfe_u32 s11, s84, 0x40000\n"
"ds_read_b128 v[68:71], v228 offset:8192\n"
"v_and_b32 v249, 7, v2\n"
"v_mfma_f32_16x16x32_bf16 v[172:175], v[188:191], v[56:59], v[172:175]\n"
"s_mul_i32 s11, s11, 163840\n"
"ds_read_b64_tr_b16 a[188:189], v224 offset:10240\n"
"v_lshlrev_b32 v249, 4, v249\n"
"v_mfma_f32_16x16x32_bf16 v[180:183], a[208:211], v[56:59], v[180:183]\n"
"s_add_u32 m0, s86, 0\n"
"ds_read_b64_tr_b16 a[190:191], v224 offset:10400\n"
"v_mad_u32_u24 v10, v248, s68, v249\n"
"v_mfma_f32_16x16x32_bf16 v[184:187], a[208:211], v[188:191], v[184:187]\n"
"s_waitcnt lgkmcnt(6)\n"
"v_and_b32 v248, 1, v4\n"
"v_lshrrev_b32 v249, 2, v3\n"
"v_mfma_f32_16x16x32_bf16 v[176:179], v[60:63], v[60:63], v[176:179]\n"
"s_add_u32 s11, s11, s10\n"
"ds_read_b128 v[72:75], v229 offset:8192\n"
"v_lshl_add_u32 v249, v248, 3, v249\n"
"v_mfma_f32_16x16x32_bf16 v[172:175], v[188:191], v[60:63], v[172:175]\n"
"s_lshl3_add_u32 s37, s68, -1024\n"
"ds_read_b64_tr_b16 a[192:193], v225 offset:10240\n"
"v_lshrrev_b32 v15, 1, v4\n"
"v_mfma_f32_16x16x32_bf16 v[180:183], a[212:215], v[60:63], v[180:183]\n"
"s_lshl_b32 s43, s86, 1\n"
"ds_read_b64_tr_b16 a[194:195], v225 offset:10400\n"
"v_lshl_add_u32 v249, v249, 2, v15\n"
"v_mfma_f32_16x16x32_bf16 v[184:187], a[212:215], v[188:191], v[184:187]\n"
"s_waitcnt lgkmcnt(6)\n"
"buffer_load_dwordx4 v216, s[28:31], s11 offen offset:0 lds\n"
"v_mfma_f32_16x16x32_bf16 v[176:179], v[64:67], v[64:67], v[176:179]\n"
"s_add_u32 s38, s38, s20\n"
"ds_read_b128 v[76:79], v230 offset:8192\n"
"v_and_b32 v14, 3, v2\n"
"v_mfma_f32_16x16x32_bf16 v[172:175], v[188:191], v[64:67], v[172:175]\n"
"s_lshl1_add_u32 s40, s67, s38\n"
"ds_read_b64_tr_b16 a[196:197], v226 offset:10240\n"
"v_lshl_add_u32 v249, v249, 2, v14\n"
"v_mfma_f32_16x16x32_bf16 v[180:183], a[184:187], v[64:67], v[180:183]\n"
"s_mov_b32 s39, s21\n"
"ds_read_b64_tr_b16 a[198:199], v226 offset:10400\n"
"v_lshl_add_u32 v13, v249, 3, s43\n"
"v_mfma_f32_16x16x32_bf16 v[184:187], a[184:187], v[188:191], v[184:187]\n"
"s_waitcnt lgkmcnt(6)\n"
"buffer_load_dwordx4 v217, s[28:31], s11 offen offset:1024 lds\n"
"v_mfma_f32_16x16x32_bf16 v[176:179], v[68:71], v[68:71], v[176:179]\n"
"s_waitcnt vmcnt(7)\n"
"ds_read_b128 v[80:83], v227 offset:12288\n"
"v_lshlrev_b32 v14, 4, v3\n"
"v_mfma_f32_16x16x32_bf16 v[172:175], v[188:191], v[68:71], v[172:175]\n"
"s_mov_b32 s41, s21\n"
"ds_read_b64_tr_b16 a[200:201], v223 offset:15360\n"
"v_mad_u32_u24 v11, v15, s67, v14\n"
"v_mfma_f32_16x16x32_bf16 v[180:183], a[188:191], v[68:71], v[180:183]\n"
"s_mul_i32 s71, s56, 768\n"
"ds_read_b64_tr_b16 a[202:203], v223 offset:15520\n"
"v_lshl_add_u32 v11, v248, 8, v11\n"
"v_mfma_f32_16x16x32_bf16 v[184:187], a[188:191], v[188:191], v[184:187]\n"
"s_waitcnt lgkmcnt(6)\n"
"buffer_load_dwordx4 v218, s[28:31], s11 offen offset:2048 lds\n"
"v_mfma_f32_16x16x32_bf16 v[176:179], v[72:75], v[72:75], v[176:179]\n"
"s_add_u32 s71, s71, 151552\n"
"ds_read_b128 v[84:87], v228 offset:12288\n"
"v_lshl_add_u32 v249, v248, 1, v15\n"
"v_mfma_f32_16x16x32_bf16 v[172:175], v[188:191], v[72:75], v[172:175]\n"
"s_lshr_b32 s59, s57, 1\n"
"ds_read_b64_tr_b16 a[204:205], v224 offset:15360\n"
"v_mad_u32_u24 v12, v249, s67, v14\n"
"v_mfma_f32_16x16x32_bf16 v[180:183], a[192:195], v[72:75], v[180:183]\n"
"s_and_b32 s64, s62, 1\n"
"ds_read_b64_tr_b16 a[206:207], v224 offset:15520\n"
"v_mul_u32_u24 v248, 48, v3\n"
"v_mfma_f32_16x16x32_bf16 v[184:187], a[192:195], v[188:191], v[184:187]\n"
"s_waitcnt lgkmcnt(6)\n"
"buffer_load_dwordx4 v219, s[28:31], s11 offen offset:3072 lds\n"
"v_mfma_f32_16x16x32_bf16 v[176:179], v[76:79], v[76:79], v[176:179]\n"
"s_add_u32 m0, m0, 4096\n"
"ds_read_b128 v[88:91], v229 offset:12288\n"
"v_add_u32 v248, s71, v248\n"
"v_mfma_f32_16x16x32_bf16 v[172:175], v[188:191], v[76:79], v[172:175]\n"
"s_lshl_b32 s71, s56, 7\n"
"ds_read_b64_tr_b16 a[208:209], v225 offset:15360\n"
"v_lshl_add_u32 v208, v4, 3, v248\n"
"v_mfma_f32_16x16x32_bf16 v[180:183], a[196:199], v[76:79], v[180:183]\n"
"s_add_u32 s71, s71, 147456\n"
"ds_read_b64_tr_b16 a[210:211], v225 offset:15520\n"
"v_add_u32 v249, 3032, v208\n"
"v_mfma_f32_16x16x32_bf16 v[184:187], a[196:199], v[188:191], v[184:187]\n"
"s_waitcnt lgkmcnt(6)\n"
"buffer_load_dwordx4 v220, s[28:31], s11 offen lds\n"
"v_mfma_f32_16x16x32_bf16 v[176:179], v[80:83], v[80:83], v[176:179]\n"
"s_bfe_u32 s11, s84, 0x40004\n"
"ds_read_b128 v[92:95], v230 offset:12288\n"
"v_cmp_gt_u32 vcc, 1, v4\n"
"v_mfma_f32_16x16x32_bf16 v[172:175], v[188:191], v[80:83], v[172:175]\n"
"s_mul_i32 s11, s11, 163840\n"
"ds_read_b64_tr_b16 a[212:213], v226 offset:15360\n"
"v_cndmask_b32 v209, v249, v208, vcc\n"
"v_mfma_f32_16x16x32_bf16 v[180:183], a[200:203], v[80:83], v[180:183]\n"
"s_add_u32 m0, s86, 20480\n"
"ds_read_b64_tr_b16 a[214:215], v226 offset:15520\n"
"v_cmp_gt_u32 vcc, 2, v4\n"
"v_mfma_f32_16x16x32_bf16 v[184:187], a[200:203], v[188:191], v[184:187]\n"
"s_waitcnt lgkmcnt(6)\n"
"v_cndmask_b32 v210, v249, v208, vcc\n"
"v_cmp_gt_u32 vcc, 3, v4\n"
"v_mfma_f32_16x16x32_bf16 v[176:179], v[84:87], v[84:87], v[176:179]\n"
"s_add_u32 s11, s11, s10\n"
"s_waitcnt vmcnt(6)\n"
"ds_read_b128 v[96:99], v227 offset:16384\n"
"v_cndmask_b32 v211, v249, v208, vcc\n"
"v_mfma_f32_16x16x32_bf16 v[172:175], v[188:191], v[84:87], v[172:175]\n"
"s_and_b32 s60, s63, 7\n"
"ds_read_b64_tr_b16 a[184:185], v223 offset:20480\n"
"v_mul_u32_u24 v249, 3072, v4\n"
"v_mfma_f32_16x16x32_bf16 v[180:183], a[204:207], v[84:87], v[180:183]\n"
"s_lshl3_add_u32 s60, s64, s60\n"
"ds_read_b64_tr_b16 a[186:187], v223 offset:20640\n"
"v_add_u32 v212, v248, v249\n"
"v_mfma_f32_16x16x32_bf16 v[184:187], a[204:207], v[188:191], v[184:187]\n"
"s_waitcnt lgkmcnt(6)\n"
"buffer_load_dwordx4 v216, s[28:31], s11 offen offset:0 lds\n"
"v_mfma_f32_16x16x32_bf16 v[176:179], v[88:91], v[88:91], v[176:179]\n"
"s_mul_i32 s54, s57, 81920\n"
"ds_read_b128 v[100:103], v228 offset:16384\n"
"v_lshl_add_u32 v213, v3, 3, s71\n"
"v_mfma_f32_16x16x32_bf16 v[172:175], v[188:191], v[88:91], v[172:175]\n"
"s_lshl2_add_u32 s71, s56, 151312\n"
"ds_read_b64_tr_b16 a[188:189], v224 offset:20480\n"
"v_lshlrev_b32 v214, 3, v2\n"
"v_mfma_f32_16x16x32_bf16 v[180:183], a[208:211], v[88:91], v[180:183]\n"
"s_add_u32 s54, s54, 655360\n"
"ds_read_b64_tr_b16 a[190:191], v224 offset:20640\n"
"v_add_u32 v214, 147456, v214\n"
"v_mfma_f32_16x16x32_bf16 v[184:187], a[208:211], v[188:191], v[184:187]\n"
"s_waitcnt lgkmcnt(6)\n"
"buffer_load_dwordx4 v217, s[28:31], s11 offen offset:1024 lds\n"
"v_mfma_f32_16x16x32_bf16 v[176:179], v[92:95], v[92:95], v[176:179]\n"
"s_add_u32 s54, s54, s20\n"
"ds_read_b128 v[104:107], v229 offset:16384\n"
"v_mov_b32 v215, s71\n"
"v_mfma_f32_16x16x32_bf16 v[172:175], v[188:191], v[92:95], v[172:175]\n"
"s_mul_i32 s71, s56, 3072\n"
"ds_read_b64_tr_b16 a[192:193], v225 offset:20480\n"
"v_add_u32 v251, s71, v26\n"
"v_mfma_f32_16x16x32_bf16 v[180:183], a[212:215], v[92:95], v[180:183]\n"
"s_add_u32 s55, s54, s86\n"
"ds_read_b64_tr_b16 a[194:195], v225 offset:20640\n"
"v_add_u32 v252, s71, v27\n"
"v_mfma_f32_16x16x32_bf16 v[184:187], a[212:215], v[188:191], v[184:187]\n"
"s_waitcnt lgkmcnt(6)\n"
"buffer_load_dwordx4 v218, s[28:31], s11 offen offset:2048 lds\n"
"v_mfma_f32_16x16x32_bf16 v[176:179], v[96:99], v[96:99], v[176:179]\n"
"s_mul_i32 s64, s58, 2560\n"
"ds_read_b128 v[108:111], v230 offset:16384\n"
"v_and_b32 v16, 0x1ff, v0\n"
"v_mfma_f32_16x16x32_bf16 v[172:175], v[188:191], v[96:99], v[172:175]\n"
"s_lshl2_add_u32 s64, s86, s64\n"
"ds_read_b64_tr_b16 a[196:197], v226 offset:20480\n"
"v_and_b32 v17, 0x7f, v0\n"
"v_mfma_f32_16x16x32_bf16 v[180:183], a[184:187], v[96:99], v[180:183]\n"
"s_add_u32 s44, s54, s64\n"
"ds_read_b64_tr_b16 a[198:199], v226 offset:20640\n"
"v_lshrrev_b32 v248, 5, v2\n"
"v_mfma_f32_16x16x32_bf16 v[184:187], a[184:187], v[188:191], v[184:187]\n"
"s_waitcnt lgkmcnt(6)\n"
"buffer_load_dwordx4 v219, s[28:31], s11 offen offset:3072 lds\n"
"v_mfma_f32_16x16x32_bf16 v[176:179], v[100:103], v[100:103], v[176:179]\n"
"s_add_u32 m0, m0, 4096\n"
"s_add_u32 s45, s44, 2621440\n"
"v_mfma_f32_16x16x32_bf16 v[172:175], v[188:191], v[100:103], v[172:175]\n"
"s_lshl2_add_u32 s64, s57, s56\n"
"v_add_u32 v20, 55296, v0\n"
"v_mfma_f32_16x16x32_bf16 v[180:183], a[188:191], v[100:103], v[180:183]\n"
"s_lshl3_add_u32 s64, s64, s58\n"
"v_add_u32 v21, 110592, v0\n"
"v_mfma_f32_16x16x32_bf16 v[184:187], a[188:191], v[188:191], v[184:187]\n"
"s_waitcnt lgkmcnt(3)\n"
"buffer_load_dwordx4 v220, s[28:31], s11 offen lds\n"
"v_mfma_f32_16x16x32_bf16 v[176:179], v[104:107], v[104:107], v[176:179]\n"
"s_lshl_b32 s46, s64, 8\n"
"v_add_u32 v22, 165888, v0\n"
"v_mfma_f32_16x16x32_bf16 v[172:175], v[188:191], v[104:107], v[172:175]\n"
"s_add_u32 s46, s46, 5898240\n"
"v_add_u32 v23, 221184, v0\n"
"v_mfma_f32_16x16x32_bf16 v[180:183], a[192:195], v[104:107], v[180:183]\n"
"s_add_u32 s46, s46, s20\n"
"v_mov_b32 v14, 0\n"
"v_mfma_f32_16x16x32_bf16 v[184:187], a[192:195], v[188:191], v[184:187]\n"
"s_waitcnt lgkmcnt(0)\n"
"v_mov_b32 v29, 0x3f803f80\n"
"s_lshr_b32 s64, s60, 2\n"
"v_mfma_f32_16x16x32_bf16 v[176:179], v[108:111], v[108:111], v[176:179]\n"
"s_mul_i32 s98, s60, 7\n"
"s_sub_u32 s98, s98, s64\n"
"v_mfma_f32_16x16x32_bf16 v[172:175], v[188:191], v[108:111], v[172:175]\n"
"s_and_b32 s64, s60, 3\n"
"s_cmp_eq_u32 s64, 3\n"
"s_cselect_b32 s99, 1073741824, 0\n"
"v_mfma_f32_16x16x32_bf16 v[180:183], a[196:199], v[108:111], v[180:183]\n"
"s_sub_u32 s99, s99, s20\n"
"s_lshl_b32 s67, s98, 9\n"
"v_mfma_f32_16x16x32_bf16 v[184:187], a[196:199], v[188:191], v[184:187]\n"
"s_add_u32 s67, s67, 6160384\n"
"s_lshl2_add_u32 s64, s59, s56\n"
"v_cndmask_b32 v8, v176, v177, s[12:13]\n"
"v_cndmask_b32 v8, v8, v178, s[14:15]\n"
"v_cndmask_b32 v8, v8, v179, s[16:17]\n"
"ds_bpermute_b32 v8, v242, v8\n"
"v_mul_f32 v9, 0x3acccccd, v172\n"
"v_cvt_pk_bf16_f32 v234, -v9, -v9\n"
"v_and_b32 v233, 0xffff0000, v234\n"
"v_mov_b32 v235, v234\n"
"v_fma_f32 v180, v233, v184, v180\n"
"v_fma_f32 v181, v233, v185, v181\n"
"v_fma_f32 v182, v233, v186, v182\n"
"v_fma_f32 v183, v233, v187, v183\n"
"s_waitcnt lgkmcnt(0)\n"
"v_mul_f32 v8, 0x3acccccd, v8\n"
"v_fma_f32 v8, -v9, v9, v8\n"
"v_max_f32 v8, 0, v8\n"
"v_cvt_pk_bf16_f32 v8, v8, v8\n"
"v_and_b32 v8, 0xffff0000, v8\n"
"v_add_f32 v8, 0x3727c5ac, v8\n"
"v_cvt_pk_bf16_f32 v8, v8, v8\n"
"v_and_b32 v8, 0xffff0000, v8\n"
"v_sqrt_f32 v8, v8\n"
"s_cmp_ge_u32 s58, 2\n"
"v_cvt_pk_bf16_f32 v8, v8, v8\n"
"v_and_b32 v8, 0xffff0000, v8\n"
"v_rcp_f32 v236, v8\n"
"s_nop 0\n"
"v_pk_mul_f32 v[180:181], v[180:181], v[236:237] op_sel_hi:[1,0]\n"
"v_pk_mul_f32 v[182:183], v[182:183], v[236:237] op_sel_hi:[1,0]\n"
"v_cvt_pk_bf16_f32 v8, v180, v181\n"
"v_cvt_pk_bf16_f32 v9, v182, v183\n"
"v_cndmask_b32 v8, v8, v244, s[80:81]\n"
"v_cndmask_b32 v9, v9, v245, s[80:81]\n"
"s_cbranch_scc1 .Lkv_v\n"
"ds_write_b64 v240, v[8:9]\n"
"s_branch .Lkv_staged\n"
".Lkv_v:\n"
"ds_write_b16 v241, v8\n"
"ds_write_b16_d16_hi v241, v8 offset:16\n"
"ds_write_b16 v241, v9 offset:32\n"
"ds_write_b16_d16_hi v241, v9 offset:48\n"
".Lkv_staged:\n"
"s_cmp_lg_u32 s96, 3\n"
"s_waitcnt lgkmcnt(0)\n"
"s_waitcnt vmcnt(5)\n"
"s_barrier\n"
"s_cbranch_scc1 .Lkv_done\n"
"ds_read_b128 v[172:175], v247\n"
"ds_read_b128 v[176:179], v247 offset:1024\n"
"s_waitcnt lgkmcnt(1)\n"
"buffer_store_dwordx4 v[172:175], v246, s[20:23], s72 offen sc1\n"
"s_waitcnt lgkmcnt(0)\n"
"buffer_store_dwordx4 v[176:179], v246, s[20:23], s73 offen sc1\n"
"v_mov_b32 v181, 0\n"
"v_mov_b32 v182, 0\n"
"v_mov_b32 v183, 0\n"
"v_dot2c_f32_bf16 v181, v172, v172\n"
"v_dot2c_f32_bf16 v182, v174, v174\n"
"v_dot2c_f32_bf16 v183, v176, v176\n"
"v_dot2c_f32_bf16 v181, v173, v173\n"
"v_dot2c_f32_bf16 v182, v175, v175\n"
"v_dot2c_f32_bf16 v183, v177, v177\n"
"v_dot2c_f32_bf16 v183, v178, v178\n"
"v_dot2c_f32_bf16 v183, v179, v179\n"
".Lkv_done:\n"
"s_bfe_u32 s11, s84, 0x40008\n"
"s_mul_i32 s11, s11, 163840\n"
"s_add_u32 m0, s86, 40960\n"
"s_add_u32 s11, s11, s10\n"
"buffer_load_dwordx4 v216, s[28:31], s11 offen offset:0 lds\n"
"ds_read_b64_tr_b16 a[184:185], v204 offset:0\n"
"ds_read_b64_tr_b16 a[186:187], v204 offset:2560\n"
"ds_read_b64_tr_b16 a[188:189], v204 offset:32\n"
"ds_read_b64_tr_b16 a[190:191], v204 offset:2592\n"
"ds_read_b64_tr_b16 a[192:193], v204 offset:64\n"
"ds_read_b64_tr_b16 a[194:195], v204 offset:2624\n"
"ds_read_b64_tr_b16 a[196:197], v204 offset:96\n"
"ds_read_b64_tr_b16 a[198:199], v204 offset:2656\n"
"ds_read_b64_tr_b16 a[200:201], v204 offset:128\n"
"ds_read_b64_tr_b16 a[202:203], v204 offset:2688\n"
"ds_read_b64_tr_b16 a[204:205], v205 offset:0\n"
"ds_read_b64_tr_b16 a[206:207], v205 offset:2560\n"
"ds_read_b64_tr_b16 a[208:209], v205 offset:32\n"
"ds_read_b64_tr_b16 a[210:211], v205 offset:2592\n"
"s_waitcnt lgkmcnt(10)\n"
"v_mfma_f32_16x16x32_bf16 v[112:115], a[184:187], v[32:35], 0\n"
"ds_read_b64_tr_b16 a[212:213], v205 offset:64\n"
"v_mfma_f32_16x16x32_bf16 v[152:155], v[188:191], a[184:187], 0\n"
"s_mul_i32 s47, s64, 110592\n"
"ds_read_b64_tr_b16 a[214:215], v205 offset:2624\n"
"buffer_load_dwordx4 v217, s[28:31], s11 offen offset:1024 lds\n"
"v_mfma_f32_16x16x32_bf16 v[116:119], a[188:191], v[32:35], 0\n"
"s_waitcnt lgkmcnt(8)\n"
"ds_read_b64_tr_b16 a[184:185], v205 offset:96\n"
"v_mfma_f32_16x16x32_bf16 v[156:159], v[188:191], a[188:191], 0\n"
"s_add_u32 s47, s47, s67\n"
"ds_read_b64_tr_b16 a[186:187], v205 offset:2656\n"
"v_readfirstlane_b32 s5, v6\n"
"v_add_f32 v180, v181, v182\n"
"v_cndmask_b32 v181, v180, v181, s[74:75]\n"
"v_mfma_f32_16x16x32_bf16 v[120:123], a[192:195], v[32:35], 0\n"
"s_add_u32 s47, s47, s20\n"
"ds_read_b64_tr_b16 a[188:189], v205 offset:128\n"
"v_mfma_f32_16x16x32_bf16 v[160:163], v[188:191], a[192:195], 0\n"
"s_lshl_b32 s49, s62, 7\n"
"ds_read_b64_tr_b16 a[190:191], v205 offset:2688\n"
"v_cndmask_b32 v182, v183, v182, s[74:75]\n"
"s_mov_b32 s74, 0\n"
"v_cndmask_b32 v181, v181, 0, s[74:75]\n"
"v_mfma_f32_16x16x32_bf16 v[124:127], a[196:199], v[32:35], 0\n"
"s_waitcnt lgkmcnt(8)\n"
"ds_read_b64_tr_b16 a[192:193], v206 offset:0\n"
"v_mfma_f32_16x16x32_bf16 v[164:167], v[188:191], a[196:199], 0\n"
"s_add_u32 s49, s49, 32768\n"
"ds_read_b64_tr_b16 a[194:195], v206 offset:2560\n"
"buffer_load_dwordx4 v218, s[28:31], s11 offen offset:2048 lds\n"
"v_mfma_f32_16x16x32_bf16 v[128:131], a[200:203], v[32:35], 0\n"
"s_cmp_eq_u32 s56, 0\n"
"s_cselect_b32 s49, s49, 1073741824\n"
"ds_read_b64_tr_b16 a[196:197], v206 offset:32\n"
"v_mfma_f32_16x16x32_bf16 v[168:171], v[188:191], a[200:203], 0\n"
"s_lshl_b32 s50, s56, 13\n"
"ds_read_b64_tr_b16 a[198:199], v206 offset:2592\n"
"v_cndmask_b32 v182, v182, 0, s[74:75]\n"
"s_mul_i32 s61, s5, 0x41c64e6d\n"
"s_add_u32 s61, s61, 12345\n"
"v_mfma_f32_16x16x32_bf16 v[112:115], a[204:207], v[36:39], v[112:115]\n"
"s_waitcnt lgkmcnt(8)\n"
"ds_read_b64_tr_b16 a[200:201], v206 offset:64\n"
"ds_read_b64_tr_b16 a[202:203], v206 offset:2624\n"
"v_permlane32_swap_b32 v181, v182\n"
"v_add_f32 v181, v181, v182\n"
"s_lshl_b32 s76, s57, 2\n"
"v_mfma_f32_16x16x32_bf16 v[116:119], a[208:211], v[36:39], v[116:119]\n"
"s_add_u32 s76, s76, s58\n"
"ds_read_b64_tr_b16 a[204:205], v206 offset:96\n"
"ds_read_b64_tr_b16 a[206:207], v206 offset:2656\n"
"buffer_load_dwordx4 v219, s[28:31], s11 offen offset:3072 lds\n"
"v_mfma_f32_16x16x32_bf16 v[120:123], a[212:215], v[36:39], v[120:123]\n"
"s_waitcnt lgkmcnt(8)\n"
"ds_read_b64_tr_b16 a[208:209], v206 offset:128\n"
"ds_read_b64_tr_b16 a[210:211], v206 offset:2688\n"
"v_max_f32_dpp v181, v181, v181 row_ror:1 row_mask:0xf bank_mask:0xf\n"
"s_nop 1\n"
"v_max_f32_dpp v181, v181, v181 row_ror:2 row_mask:0xf bank_mask:0xf\n"
"s_nop 1\n"
"v_max_f32_dpp v181, v181, v181 row_ror:4 row_mask:0xf bank_mask:0xf\n"
"v_mfma_f32_16x16x32_bf16 v[124:127], a[184:187], v[36:39], v[124:127]\n"
"s_add_u32 m0, m0, 4096\n"
"ds_read_b64_tr_b16 a[212:213], v207 offset:0\n"
"ds_read_b64_tr_b16 a[214:215], v207 offset:2560\n"
"v_max_f32_dpp v181, v181, v181 row_ror:8 row_mask:0xf bank_mask:0xf\n"
"s_nop 1\n"
"v_max_f32_dpp v181, v181, v181 row_bcast:15 row_mask:0xa bank_mask:0xf\n"
"s_nop 1\n"
"v_max_f32_dpp v181, v181, v181 row_bcast:31 row_mask:0xc bank_mask:0xf\n"
"v_mfma_f32_16x16x32_bf16 v[128:131], a[188:191], v[36:39], v[128:131]\n"
"s_waitcnt lgkmcnt(8)\n"
"ds_read_b64_tr_b16 a[184:185], v207 offset:32\n"
"ds_read_b64_tr_b16 a[186:187], v207 offset:2592\n"
"buffer_load_dwordx4 v220, s[28:31], s11 offen lds\n"
"v_mfma_f32_16x16x32_bf16 v[112:115], a[192:195], v[40:43], v[112:115]\n"
"s_bfe_u32 s11, s84, 0x4000c\n"
"ds_read_b64_tr_b16 a[188:189], v207 offset:64\n"
"ds_read_b64_tr_b16 a[190:191], v207 offset:2624\n"
"s_lshl_b32 s76, s76, 7\n"
"v_readlane_b32 s5, v181, 63\n"
"s_add_u32 s76, s76, 4096\n"
"v_mfma_f32_16x16x32_bf16 v[116:119], a[196:199], v[40:43], v[116:119]\n"
"s_waitcnt lgkmcnt(8)\n"
"ds_read_b64_tr_b16 a[192:193], v207 offset:96\n"
"ds_read_b64_tr_b16 a[194:195], v207 offset:2656\n"
"v_mov_b32 v180, s61\n"
"v_mov_b32 v181, s5\n"
"s_lshl_b32 s18, s56, 12\n"
"v_mfma_f32_16x16x32_bf16 v[120:123], a[200:203], v[40:43], v[120:123]\n"
"s_mul_i32 s11, s11, 163840\n"
"ds_read_b64_tr_b16 a[196:197], v207 offset:128\n"
"ds_read_b64_tr_b16 a[198:199], v207 offset:2688\n"
"s_add_u32 s18, s18, 4096\n"
"v_mov_b32 v30, s61\n"
"v_mfma_f32_16x16x32_bf16 v[124:127], a[204:207], v[40:43], v[124:127]\n"
"s_waitcnt vmcnt(5) lgkmcnt(8)\n"
"s_barrier\n"
"ds_read_b64_tr_b16 a[200:201], v204 offset:20480\n"
"ds_read_b64_tr_b16 a[202:203], v204 offset:23040\n"
"v_cmp_eq_u32 s[12:13], 0, v4\n"
"v_cmp_gt_u32 s[14:15], 2, v4\n"
"v_mfma_f32_16x16x32_bf16 v[128:131], a[208:211], v[40:43], v[128:131]\n"
"s_add_u32 m0, s86, 61440\n"
"ds_read_b64_tr_b16 a[204:205], v204 offset:20512\n"
"ds_read_b64_tr_b16 a[206:207], v204 offset:23072\n"
"v_cmp_gt_u32 s[16:17], 3, v4\n"
"v_mul_f32 v238, 0x3ea52b5c, v236\n"
"s_cmp_lg_u32 s96, 3\n"
"s_cbranch_scc1 .Lkv_flagged\n"
"s_mov_b64 exec, 1\n"
"buffer_store_dwordx2 v[180:181], off, s[20:23], s76 sc1\n"
"s_mov_b64 exec, -1\n"
".Lkv_flagged:\n"
"v_mfma_f32_16x16x32_bf16 v[112:115], a[212:215], v[44:47], v[112:115]\n"
"s_waitcnt lgkmcnt(8)\n"
"ds_read_b64_tr_b16 a[208:209], v204 offset:20544\n"
"ds_read_b64_tr_b16 a[210:211], v204 offset:23104\n"
"v_mov_b32 v184, 0\n"
"v_mov_b32 v185, 0\n"
"v_mfma_f32_16x16x32_bf16 v[116:119], a[184:187], v[44:47], v[116:119]\n"
"s_add_u32 s11, s11, s10\n"
"ds_read_b64_tr_b16 a[212:213], v204 offset:20576\n"
"ds_read_b64_tr_b16 a[214:215], v204 offset:23136\n"
"v_mov_b32 v9, 151296\n"
"v_mfma_f32_16x16x32_bf16 v[120:123], a[188:191], v[44:47], v[120:123]\n"
"s_waitcnt lgkmcnt(8)\n"
"ds_read_b64_tr_b16 a[184:185], v204 offset:20608\n"
"ds_read_b64_tr_b16 a[186:187], v204 offset:23168\n"
"v_mfma_f32_16x16x32_bf16 v[124:127], a[192:195], v[44:47], v[124:127]\n"
"s_add_u32 s50, s50, 32768\n"
"ds_read_b64_tr_b16 a[188:189], v205 offset:20480\n"
"ds_read_b64_tr_b16 a[190:191], v205 offset:23040\n"
"buffer_load_dwordx4 v216, s[28:31], s11 offen offset:0 lds\n"
"v_mfma_f32_16x16x32_bf16 v[128:131], a[196:199], v[44:47], v[128:131]\n"
"s_waitcnt lgkmcnt(8)\n"
"ds_read_b64_tr_b16 a[192:193], v205 offset:20512\n"
"ds_read_b64_tr_b16 a[194:195], v205 offset:23072\n"
"v_mfma_f32_16x16x32_bf16 v[112:115], a[200:203], v[48:51], v[112:115]\n"
"s_or_b32 s64, s62, s56\n"
"s_cselect_b32 s51, 1073741824, 0\n"
"ds_read_b64_tr_b16 a[196:197], v205 offset:20544\n"
"v_mfma_f32_16x16x32_bf16 v[152:155], v[188:191], a[200:203], v[152:155]\n"
"s_add_u32 s64, s56, 3\n"
"ds_read_b64_tr_b16 a[198:199], v205 offset:23104\n"
"v_mfma_f32_16x16x32_bf16 v[116:119], a[204:207], v[48:51], v[116:119]\n"
"s_waitcnt lgkmcnt(8)\n"
"ds_read_b64_tr_b16 a[200:201], v205 offset:20576\n"
"v_mfma_f32_16x16x32_bf16 v[156:159], v[188:191], a[204:207], v[156:159]\n"
"s_and_b32 s64, s64, 3\n"
"ds_read_b64_tr_b16 a[202:203], v205 offset:23136\n"
"buffer_load_dwordx4 v217, s[28:31], s11 offen offset:1024 lds\n"
"v_mfma_f32_16x16x32_bf16 v[120:123], a[208:211], v[48:51], v[120:123]\n"
"s_lshl2_add_u32 s64, s58, s64\n"
"ds_read_b64_tr_b16 a[204:205], v205 offset:20608\n"
"v_mfma_f32_16x16x32_bf16 v[160:163], v[188:191], a[208:211], v[160:163]\n"
"s_mul_i32 s64, s64, 5\n"
"ds_read_b64_tr_b16 a[206:207], v205 offset:23168\n"
"v_add_u32 v249, s64, v248\n"
"v_mfma_f32_16x16x32_bf16 v[124:127], a[212:215], v[48:51], v[124:127]\n"
"s_waitcnt lgkmcnt(8)\n"
"ds_read_b64_tr_b16 a[208:209], v206 offset:20480\n"
"v_lshrrev_b32 v15, 1, v249\n"
"v_mfma_f32_16x16x32_bf16 v[164:167], v[188:191], a[212:215], v[164:167]\n"
"ds_read_b64_tr_b16 a[210:211], v206 offset:23040\n"
"v_lshlrev_b32 v15, 8, v15\n"
"v_mfma_f32_16x16x32_bf16 v[128:131], a[184:187], v[48:51], v[128:131]\n"
"ds_read_b64_tr_b16 a[212:213], v206 offset:20512\n"
"v_and_b32 v249, 1, v249\n"
"v_mfma_f32_16x16x32_bf16 v[168:171], v[188:191], a[184:187], v[168:171]\n"
"ds_read_b64_tr_b16 a[214:215], v206 offset:23072\n"
"v_lshl_add_u32 v15, v249, 3, v15\n"
"buffer_load_dwordx4 v218, s[28:31], s11 offen offset:2048 lds\n"
"v_mfma_f32_16x16x32_bf16 v[112:115], a[188:191], v[52:55], v[112:115]\n"
"s_waitcnt lgkmcnt(8)\n"
"ds_read_b64_tr_b16 a[184:185], v206 offset:20544\n"
"ds_read_b64_tr_b16 a[186:187], v206 offset:23104\n"
"v_mfma_f32_16x16x32_bf16 v[116:119], a[192:195], v[52:55], v[116:119]\n"
"ds_read_b64_tr_b16 a[188:189], v206 offset:20576\n"
"ds_read_b64_tr_b16 a[190:191], v206 offset:23136\n"
"v_mfma_f32_16x16x32_bf16 v[120:123], a[196:199], v[52:55], v[120:123]\n"
"s_waitcnt lgkmcnt(8)\n"
"ds_read_b64_tr_b16 a[192:193], v206 offset:20608\n"
"ds_read_b64_tr_b16 a[194:195], v206 offset:23168\n"
"buffer_load_dwordx4 v219, s[28:31], s11 offen offset:3072 lds\n"
"v_mfma_f32_16x16x32_bf16 v[124:127], a[200:203], v[52:55], v[124:127]\n"
"s_add_u32 m0, m0, 4096\n"
"ds_read_b64_tr_b16 a[196:197], v207 offset:20480\n"
"ds_read_b64_tr_b16 a[198:199], v207 offset:23040\n"
"v_mfma_f32_16x16x32_bf16 v[128:131], a[204:207], v[52:55], v[128:131]\n"
"s_waitcnt lgkmcnt(8)\n"
"ds_read_b64_tr_b16 a[200:201], v207 offset:20512\n"
"ds_read_b64_tr_b16 a[202:203], v207 offset:23072\n"
"v_mfma_f32_16x16x32_bf16 v[112:115], a[208:211], v[56:59], v[112:115]\n"
"ds_read_b64_tr_b16 a[204:205], v207 offset:20544\n"
"ds_read_b64_tr_b16 a[206:207], v207 offset:23104\n"
"buffer_load_dwordx4 v220, s[28:31], s11 offen lds\n"
"v_mfma_f32_16x16x32_bf16 v[116:119], a[212:215], v[56:59], v[116:119]\n"
"s_waitcnt lgkmcnt(8)\n"
"ds_read_b64_tr_b16 a[208:209], v207 offset:20576\n"
"ds_read_b64_tr_b16 a[210:211], v207 offset:23136\n"
"v_mfma_f32_16x16x32_bf16 v[120:123], a[184:187], v[56:59], v[120:123]\n"
"s_bfe_u32 s11, s84, 0x40010\n"
"ds_read_b64_tr_b16 a[212:213], v207 offset:20608\n"
"ds_read_b64_tr_b16 a[214:215], v207 offset:23168\n"
"v_mfma_f32_16x16x32_bf16 v[124:127], a[188:191], v[56:59], v[124:127]\n"
"s_waitcnt vmcnt(5) lgkmcnt(8)\n"
"s_barrier\n"
"ds_read_b64_tr_b16 a[184:185], v204 offset:40960\n"
"ds_read_b64_tr_b16 a[186:187], v204 offset:43520\n"
"v_mfma_f32_16x16x32_bf16 v[128:131], a[192:195], v[56:59], v[128:131]\n"
"s_mul_i32 s11, s11, 163840\n"
"ds_read_b64_tr_b16 a[188:189], v204 offset:40992\n"
"ds_read_b64_tr_b16 a[190:191], v204 offset:43552\n"
"v_mfma_f32_16x16x32_bf16 v[112:115], a[196:199], v[60:63], v[112:115]\n"
"s_waitcnt lgkmcnt(8)\n"
"ds_read_b64_tr_b16 a[192:193], v204 offset:41024\n"
"ds_read_b64_tr_b16 a[194:195], v204 offset:43584\n"
"v_mfma_f32_16x16x32_bf16 v[116:119], a[200:203], v[60:63], v[116:119]\n"
"s_add_u32 m0, s86, 81920\n"
"ds_read_b64_tr_b16 a[196:197], v204 offset:41056\n"
"ds_read_b64_tr_b16 a[198:199], v204 offset:43616\n"
"v_mfma_f32_16x16x32_bf16 v[120:123], a[204:207], v[60:63], v[120:123]\n"
"s_waitcnt lgkmcnt(8)\n"
"ds_read_b64_tr_b16 a[200:201], v204 offset:41088\n"
"ds_read_b64_tr_b16 a[202:203], v204 offset:43648\n"
"v_mfma_f32_16x16x32_bf16 v[124:127], a[208:211], v[60:63], v[124:127]\n"
"s_add_u32 s11, s11, s10\n"
"ds_read_b64_tr_b16 a[204:205], v205 offset:40960\n"
"ds_read_b64_tr_b16 a[206:207], v205 offset:43520\n"
"v_mfma_f32_16x16x32_bf16 v[128:131], a[212:215], v[60:63], v[128:131]\n"
"s_waitcnt lgkmcnt(8)\n"
"ds_read_b64_tr_b16 a[208:209], v205 offset:40992\n"
"ds_read_b64_tr_b16 a[210:211], v205 offset:43552\n"
"buffer_load_dwordx4 v216, s[28:31], s11 offen offset:0 lds\n"
"v_mfma_f32_16x16x32_bf16 v[112:115], a[184:187], v[64:67], v[112:115]\n"
"ds_read_b64_tr_b16 a[212:213], v205 offset:41024\n"
"v_lshl_add_u32 v15, v3, 4, v15\n"
"v_mfma_f32_16x16x32_bf16 v[152:155], v[188:191], a[184:187], v[152:155]\n"
"ds_read_b64_tr_b16 a[214:215], v205 offset:43584\n"
"v_and_b32 v249, 1, v4\n"
"v_mfma_f32_16x16x32_bf16 v[116:119], a[188:191], v[64:67], v[116:119]\n"
"s_waitcnt lgkmcnt(8)\n"
"ds_read_b64_tr_b16 a[184:185], v205 offset:41056\n"
"v_mul_u32_u24 v249, 20480, v249\n"
"v_mfma_f32_16x16x32_bf16 v[156:159], v[188:191], a[188:191], v[156:159]\n"
"ds_read_b64_tr_b16 a[186:187], v205 offset:43616\n"
"v_add_u32 v18, v15, v249\n"
"v_mfma_f32_16x16x32_bf16 v[120:123], a[192:195], v[64:67], v[120:123]\n"
"ds_read_b64_tr_b16 a[188:189], v205 offset:41088\n"
"v_lshl_add_u32 v19, v248, 30, v18\n"
"v_mfma_f32_16x16x32_bf16 v[160:163], v[188:191], a[192:195], v[160:163]\n"
"ds_read_b64_tr_b16 a[190:191], v205 offset:43648\n"
"v_mov_b32 v15, 0\n"
"buffer_load_dwordx4 v217, s[28:31], s11 offen offset:1024 lds\n"
"v_mfma_f32_16x16x32_bf16 v[124:127], a[196:199], v[64:67], v[124:127]\n"
"s_waitcnt lgkmcnt(8)\n"
"ds_read_b64_tr_b16 a[192:193], v206 offset:40960\n"
"v_mfma_f32_16x16x32_bf16 v[164:167], v[188:191], a[196:199], v[164:167]\n"
"ds_read_b64_tr_b16 a[194:195], v206 offset:43520\n"
"v_mfma_f32_16x16x32_bf16 v[128:131], a[200:203], v[64:67], v[128:131]\n"
"ds_read_b64_tr_b16 a[196:197], v206 offset:40992\n"
"v_mfma_f32_16x16x32_bf16 v[168:171], v[188:191], a[200:203], v[168:171]\n"
"ds_read_b64_tr_b16 a[198:199], v206 offset:43552\n"
"v_mfma_f32_16x16x32_bf16 v[112:115], a[204:207], v[68:71], v[112:115]\n"
"s_waitcnt lgkmcnt(8)\n"
"ds_read_b64_tr_b16 a[200:201], v206 offset:41024\n"
"ds_read_b64_tr_b16 a[202:203], v206 offset:43584\n"
"buffer_load_dwordx4 v218, s[28:31], s11 offen offset:2048 lds\n"
"v_mfma_f32_16x16x32_bf16 v[116:119], a[208:211], v[68:71], v[116:119]\n"
"ds_read_b64_tr_b16 a[204:205], v206 offset:41056\n"
"ds_read_b64_tr_b16 a[206:207], v206 offset:43616\n"
"v_mfma_f32_16x16x32_bf16 v[120:123], a[212:215], v[68:71], v[120:123]\n"
"s_waitcnt lgkmcnt(8)\n"
"ds_read_b64_tr_b16 a[208:209], v206 offset:41088\n"
"ds_read_b64_tr_b16 a[210:211], v206 offset:43648\n"
"v_mfma_f32_16x16x32_bf16 v[124:127], a[184:187], v[68:71], v[124:127]\n"
"ds_read_b64_tr_b16 a[212:213], v207 offset:40960\n"
"ds_read_b64_tr_b16 a[214:215], v207 offset:43520\n"
"buffer_load_dwordx4 v219, s[28:31], s11 offen offset:3072 lds\n"
"v_mfma_f32_16x16x32_bf16 v[128:131], a[188:191], v[68:71], v[128:131]\n"
"s_waitcnt lgkmcnt(8)\n"
"ds_read_b64_tr_b16 a[184:185], v207 offset:40992\n"
"ds_read_b64_tr_b16 a[186:187], v207 offset:43552\n"
"v_mfma_f32_16x16x32_bf16 v[112:115], a[192:195], v[72:75], v[112:115]\n"
"s_add_u32 m0, m0, 4096\n"
"ds_read_b64_tr_b16 a[188:189], v207 offset:41024\n"
"ds_read_b64_tr_b16 a[190:191], v207 offset:43584\n"
"v_mfma_f32_16x16x32_bf16 v[116:119], a[196:199], v[72:75], v[116:119]\n"
"s_waitcnt lgkmcnt(8)\n"
"ds_read_b64_tr_b16 a[192:193], v207 offset:41056\n"
"ds_read_b64_tr_b16 a[194:195], v207 offset:43616\n"
"buffer_load_dwordx4 v220, s[28:31], s11 offen lds\n"
"v_mfma_f32_16x16x32_bf16 v[120:123], a[200:203], v[72:75], v[120:123]\n"
"ds_read_b64_tr_b16 a[196:197], v207 offset:41088\n"
"ds_read_b64_tr_b16 a[198:199], v207 offset:43648\n"
"v_mfma_f32_16x16x32_bf16 v[124:127], a[204:207], v[72:75], v[124:127]\n"
"s_waitcnt vmcnt(5) lgkmcnt(8)\n"
"s_barrier\n"
"v_add_u32 v204, 61440, v204\n"
"ds_read_b64_tr_b16 a[200:201], v204 offset:0\n"
"ds_read_b64_tr_b16 a[202:203], v204 offset:2560\n"
"v_mfma_f32_16x16x32_bf16 v[128:131], a[208:211], v[72:75], v[128:131]\n"
"ds_read_b64_tr_b16 a[204:205], v204 offset:32\n"
"ds_read_b64_tr_b16 a[206:207], v204 offset:2592\n"
"v_mfma_f32_16x16x32_bf16 v[112:115], a[212:215], v[76:79], v[112:115]\n"
"s_waitcnt lgkmcnt(8)\n"
"ds_read_b64_tr_b16 a[208:209], v204 offset:64\n"
"ds_read_b64_tr_b16 a[210:211], v204 offset:2624\n"
"v_mfma_f32_16x16x32_bf16 v[116:119], a[184:187], v[76:79], v[116:119]\n"
"ds_read_b64_tr_b16 a[212:213], v204 offset:96\n"
"ds_read_b64_tr_b16 a[214:215], v204 offset:2656\n"
"v_mfma_f32_16x16x32_bf16 v[120:123], a[188:191], v[76:79], v[120:123]\n"
"s_waitcnt lgkmcnt(8)\n"
"ds_read_b64_tr_b16 a[184:185], v204 offset:128\n"
"ds_read_b64_tr_b16 a[186:187], v204 offset:2688\n"
"v_mfma_f32_16x16x32_bf16 v[124:127], a[192:195], v[76:79], v[124:127]\n"
"v_add_u32 v205, 61440, v205\n"
"ds_read_b64_tr_b16 a[188:189], v205 offset:0\n"
"ds_read_b64_tr_b16 a[190:191], v205 offset:2560\n"
"v_mfma_f32_16x16x32_bf16 v[128:131], a[196:199], v[76:79], v[128:131]\n"
"s_waitcnt lgkmcnt(8)\n"
"ds_read_b64_tr_b16 a[192:193], v205 offset:32\n"
"ds_read_b64_tr_b16 a[194:195], v205 offset:2592\n"
"v_mfma_f32_16x16x32_bf16 v[112:115], a[200:203], v[80:83], v[112:115]\n"
"ds_read_b64_tr_b16 a[196:197], v205 offset:64\n"
"v_mfma_f32_16x16x32_bf16 v[152:155], v[188:191], a[200:203], v[152:155]\n"
"ds_read_b64_tr_b16 a[198:199], v205 offset:2624\n"
"v_mfma_f32_16x16x32_bf16 v[116:119], a[204:207], v[80:83], v[116:119]\n"
"s_waitcnt lgkmcnt(8)\n"
"ds_read_b64_tr_b16 a[200:201], v205 offset:96\n"
"v_mfma_f32_16x16x32_bf16 v[156:159], v[188:191], a[204:207], v[156:159]\n"
"ds_read_b64_tr_b16 a[202:203], v205 offset:2656\n"
"v_mfma_f32_16x16x32_bf16 v[120:123], a[208:211], v[80:83], v[120:123]\n"
"ds_read_b64_tr_b16 a[204:205], v205 offset:128\n"
"v_mfma_f32_16x16x32_bf16 v[160:163], v[188:191], a[208:211], v[160:163]\n"
"ds_read_b64_tr_b16 a[206:207], v205 offset:2688\n"
"v_mfma_f32_16x16x32_bf16 v[124:127], a[212:215], v[80:83], v[124:127]\n"
"s_waitcnt lgkmcnt(8)\n"
"v_add_u32 v206, 61440, v206\n"
"ds_read_b64_tr_b16 a[208:209], v206 offset:0\n"
"v_mfma_f32_16x16x32_bf16 v[164:167], v[188:191], a[212:215], v[164:167]\n"
"ds_read_b64_tr_b16 a[210:211], v206 offset:2560\n"
"v_mfma_f32_16x16x32_bf16 v[128:131], a[184:187], v[80:83], v[128:131]\n"
"ds_read_b64_tr_b16 a[212:213], v206 offset:32\n"
"v_mfma_f32_16x16x32_bf16 v[168:171], v[188:191], a[184:187], v[168:171]\n"
"ds_read_b64_tr_b16 a[214:215], v206 offset:2592\n"
"s_mov_b32 exec_hi, 0\n"
"buffer_load_dwordx2 v[226:227], v25, s[20:23], s18 offen sc1\n"
"s_mov_b32 exec_hi, -1\n"
"v_mfma_f32_16x16x32_bf16 v[112:115], a[188:191], v[84:87], v[112:115]\n"
"s_waitcnt lgkmcnt(8)\n"
"ds_read_b64_tr_b16 a[184:185], v206 offset:64\n"
"ds_read_b64_tr_b16 a[186:187], v206 offset:2624\n"
"v_mfma_f32_16x16x32_bf16 v[116:119], a[192:195], v[84:87], v[116:119]\n"
"ds_read_b64_tr_b16 a[188:189], v206 offset:96\n"
"ds_read_b64_tr_b16 a[190:191], v206 offset:2656\n"
"v_mfma_f32_16x16x32_bf16 v[120:123], a[196:199], v[84:87], v[120:123]\n"
"s_waitcnt lgkmcnt(8)\n"
"ds_read_b64_tr_b16 a[192:193], v206 offset:128\n"
"ds_read_b64_tr_b16 a[194:195], v206 offset:2688\n"
"v_mfma_f32_16x16x32_bf16 v[124:127], a[200:203], v[84:87], v[124:127]\n"
"v_add_u32 v207, 61440, v207\n"
"ds_read_b64_tr_b16 a[196:197], v207 offset:0\n"
"ds_read_b64_tr_b16 a[198:199], v207 offset:2560\n"
"v_mfma_f32_16x16x32_bf16 v[128:131], a[204:207], v[84:87], v[128:131]\n"
"s_waitcnt lgkmcnt(8)\n"
"ds_read_b64_tr_b16 a[200:201], v207 offset:32\n"
"ds_read_b64_tr_b16 a[202:203], v207 offset:2592\n"
"v_mfma_f32_16x16x32_bf16 v[112:115], a[208:211], v[88:91], v[112:115]\n"
"ds_read_b64_tr_b16 a[204:205], v207 offset:64\n"
"ds_read_b64_tr_b16 a[206:207], v207 offset:2624\n"
"v_mfma_f32_16x16x32_bf16 v[116:119], a[212:215], v[88:91], v[116:119]\n"
"s_waitcnt lgkmcnt(8)\n"
"ds_read_b64_tr_b16 a[208:209], v207 offset:96\n"
"ds_read_b64_tr_b16 a[210:211], v207 offset:2656\n"
"v_mfma_f32_16x16x32_bf16 v[120:123], a[184:187], v[88:91], v[120:123]\n"
"ds_read_b64_tr_b16 a[212:213], v207 offset:128\n"
"ds_read_b64_tr_b16 a[214:215], v207 offset:2688\n"
"v_mfma_f32_16x16x32_bf16 v[124:127], a[188:191], v[88:91], v[124:127]\n"
"s_waitcnt vmcnt(1) lgkmcnt(8)\n"
"s_barrier\n"
"ds_read_b64_tr_b16 a[184:185], v204 offset:20480\n"
"ds_read_b64_tr_b16 a[186:187], v204 offset:23040\n"
"v_mfma_f32_16x16x32_bf16 v[128:131], a[192:195], v[88:91], v[128:131]\n"
"ds_read_b64_tr_b16 a[188:189], v204 offset:20512\n"
"ds_read_b64_tr_b16 a[190:191], v204 offset:23072\n"
"v_mfma_f32_16x16x32_bf16 v[112:115], a[196:199], v[92:95], v[112:115]\n"
"s_waitcnt lgkmcnt(8)\n"
"ds_read_b64_tr_b16 a[192:193], v204 offset:20544\n"
"ds_read_b64_tr_b16 a[194:195], v204 offset:23104\n"
"v_mfma_f32_16x16x32_bf16 v[116:119], a[200:203], v[92:95], v[116:119]\n"
"ds_read_b64_tr_b16 a[196:197], v204 offset:20576\n"
"ds_read_b64_tr_b16 a[198:199], v204 offset:23136\n"
"v_mfma_f32_16x16x32_bf16 v[120:123], a[204:207], v[92:95], v[120:123]\n"
"s_waitcnt lgkmcnt(8)\n"
"ds_read_b64_tr_b16 a[200:201], v204 offset:20608\n"
"ds_read_b64_tr_b16 a[202:203], v204 offset:23168\n"
"v_mfma_f32_16x16x32_bf16 v[124:127], a[208:211], v[92:95], v[124:127]\n"
"ds_read_b64_tr_b16 a[204:205], v205 offset:20480\n"
"ds_read_b64_tr_b16 a[206:207], v205 offset:23040\n"
"v_mfma_f32_16x16x32_bf16 v[128:131], a[212:215], v[92:95], v[128:131]\n"
"s_waitcnt lgkmcnt(8)\n"
"ds_read_b64_tr_b16 a[208:209], v205 offset:20512\n"
"ds_read_b64_tr_b16 a[210:211], v205 offset:23072\n"
"v_mfma_f32_16x16x32_bf16 v[112:115], a[184:187], v[96:99], v[112:115]\n"
"ds_read_b64_tr_b16 a[212:213], v205 offset:20544\n"
"v_mfma_f32_16x16x32_bf16 v[152:155], v[188:191], a[184:187], v[152:155]\n"
"ds_read_b64_tr_b16 a[214:215], v205 offset:23104\n"
"v_mfma_f32_16x16x32_bf16 v[116:119], a[188:191], v[96:99], v[116:119]\n"
"s_waitcnt lgkmcnt(8)\n"
"ds_read_b64_tr_b16 a[184:185], v205 offset:20576\n"
"v_mfma_f32_16x16x32_bf16 v[156:159], v[188:191], a[188:191], v[156:159]\n"
"ds_read_b64_tr_b16 a[186:187], v205 offset:23136\n"
"v_mfma_f32_16x16x32_bf16 v[120:123], a[192:195], v[96:99], v[120:123]\n"
"ds_read_b64_tr_b16 a[188:189], v205 offset:20608\n"
"v_mfma_f32_16x16x32_bf16 v[160:163], v[188:191], a[192:195], v[160:163]\n"
"ds_read_b64_tr_b16 a[190:191], v205 offset:23168\n"
"v_mfma_f32_16x16x32_bf16 v[124:127], a[196:199], v[96:99], v[124:127]\n"
"s_waitcnt lgkmcnt(8)\n"
"ds_read_b64_tr_b16 a[192:193], v206 offset:20480\n"
"v_mfma_f32_16x16x32_bf16 v[164:167], v[188:191], a[196:199], v[164:167]\n"
"ds_read_b64_tr_b16 a[194:195], v206 offset:23040\n"
"v_mfma_f32_16x16x32_bf16 v[128:131], a[200:203], v[96:99], v[128:131]\n"
"ds_read_b64_tr_b16 a[196:197], v206 offset:20512\n"
"v_mfma_f32_16x16x32_bf16 v[168:171], v[188:191], a[200:203], v[168:171]\n"
"ds_read_b64_tr_b16 a[198:199], v206 offset:23072\n"
"v_mfma_f32_16x16x32_bf16 v[112:115], a[204:207], v[100:103], v[112:115]\n"
"s_waitcnt lgkmcnt(8)\n"
"ds_read_b64_tr_b16 a[200:201], v206 offset:20544\n"
"ds_read_b64_tr_b16 a[202:203], v206 offset:23104\n"
"v_mfma_f32_16x16x32_bf16 v[116:119], a[208:211], v[100:103], v[116:119]\n"
"ds_read_b64_tr_b16 a[204:205], v206 offset:20576\n"
"ds_read_b64_tr_b16 a[206:207], v206 offset:23136\n"
"s_waitcnt vmcnt(0)\n"
".Lkf_chk:\n"
"v_cmp_ne_u32 vcc, s61, v226\n"
"s_nop 1\n"
"s_cmp_lg_u32 vcc_lo, 0\n"
"s_cbranch_scc1 .Lkf_re\n"
".Lkf_ok:\n"
"v_add_f32_dpp v227, v227, v227 quad_perm:[1,0,3,2] row_mask:0xf bank_mask:0xf\n"
"s_mov_b64 exec, 0x11111111\n"
"ds_max_u32 v9, v227\n"
"s_mov_b64 exec, -1\n"
"ds_write_b64 v212, v[184:185] offset:40\n"
"v_mfma_f32_16x16x32_bf16 v[120:123], a[212:215], v[100:103], v[120:123]\n"
"s_waitcnt lgkmcnt(10)\n"
"ds_read_b64_tr_b16 a[208:209], v206 offset:20608\n"
"ds_read_b64_tr_b16 a[210:211], v206 offset:23168\n"
"v_cvt_pk_bf16_f32 v7, v152, v152\n"
"v_and_b32 v7, 0xffff0000, v7\n"
"v_sub_f32 v7, v152, v7\n"
"v_mfma_f32_16x16x32_bf16 v[124:127], a[184:187], v[100:103], v[124:127]\n"
"ds_read_b64_tr_b16 a[212:213], v207 offset:20480\n"
"ds_read_b64_tr_b16 a[214:215], v207 offset:23040\n"
"global_load_dwordx4 a[0:3], v0, s[34:35] offset:0 sc1\n"
"v_mfma_f32_16x16x32_bf16 v[128:131], a[188:191], v[100:103], v[128:131]\n"
"s_waitcnt lgkmcnt(10)\n"
"ds_read_b64_tr_b16 a[184:185], v207 offset:20512\n"
"ds_read_b64_tr_b16 a[186:187], v207 offset:23072\n"
"v_cvt_pk_bf16_f32 v152, v152, v7\n"
"v_mov_b32 v153, 0\n"
"ds_write_b64 v213, v[152:153] offset:0\n"
"v_mfma_f32_16x16x32_bf16 v[112:115], a[192:195], v[104:107], v[112:115]\n"
"ds_read_b64_tr_b16 a[188:189], v207 offset:20544\n"
"ds_read_b64_tr_b16 a[190:191], v207 offset:23104\n"
"global_load_dwordx4 a[8:11], v0, s[34:35] offset:2048 sc1\n"
"v_mfma_f32_16x16x32_bf16 v[116:119], a[196:199], v[104:107], v[116:119]\n"
"s_waitcnt lgkmcnt(11)\n"
"ds_read_b64_tr_b16 a[192:193], v207 offset:20576\n"
"ds_read_b64_tr_b16 a[194:195], v207 offset:23136\n"
"v_cvt_pk_bf16_f32 v7, v156, v156\n"
"v_and_b32 v7, 0xffff0000, v7\n"
"v_sub_f32 v7, v156, v7\n"
"v_mfma_f32_16x16x32_bf16 v[120:123], a[200:203], v[104:107], v[120:123]\n"
"ds_read_b64_tr_b16 a[196:197], v207 offset:20608\n"
"ds_read_b64_tr_b16 a[198:199], v207 offset:23168\n"
"global_load_dwordx4 a[12:15], v0, s[34:35] offset:3072 sc1\n"
"v_mfma_f32_16x16x32_bf16 v[124:127], a[204:207], v[104:107], v[124:127]\n"
"s_waitcnt lgkmcnt(9)\n"
"v_cvt_pk_bf16_f32 v156, v156, v7\n"
"v_mov_b32 v157, 0\n"
"ds_write_b64 v213, v[156:157] offset:512\n"
"v_mfma_f32_16x16x32_bf16 v[128:131], a[208:211], v[104:107], v[128:131]\n"
"global_load_dwordx4 a[4:7], v0, s[34:35] offset:1024 sc1\n"
"v_mfma_f32_16x16x32_bf16 v[112:115], a[212:215], v[108:111], v[112:115]\n"
"s_waitcnt lgkmcnt(5)\n"
"v_cvt_pk_bf16_f32 v7, v160, v160\n"
"v_and_b32 v7, 0xffff0000, v7\n"
"v_sub_f32 v7, v160, v7\n"
"v_mfma_f32_16x16x32_bf16 v[116:119], a[184:187], v[108:111], v[116:119]\n"
"global_load_dwordx4 a[16:19], v1, s[34:35] offset:0 sc1\n"
"v_mfma_f32_16x16x32_bf16 v[120:123], a[188:191], v[108:111], v[120:123]\n"
".Lhop0:\n"
"s_cbranch_execz .Lhop4\n"
"s_waitcnt lgkmcnt(1)\n"
"v_cvt_pk_bf16_f32 v160, v160, v7\n"
"v_mov_b32 v161, 0\n"
"ds_write_b64 v213, v[160:161] offset:1024\n"
"v_cvt_pk_bf16_f32 v7, v164, v164\n"
"v_and_b32 v7, 0xffff0000, v7\n"
"v_sub_f32 v7, v164, v7\n"
"v_cvt_pk_bf16_f32 v164, v164, v7\n"
"v_mov_b32 v165, 0\n"
"ds_write_b64 v213, v[164:165] offset:1536\n"
"v_cvt_pk_bf16_f32 v7, v168, v168\n"
"v_and_b32 v7, 0xffff0000, v7\n"
"v_sub_f32 v7, v168, v7\n"
"v_cvt_pk_bf16_f32 v168, v168, v7\n"
"v_mov_b32 v169, 0\n"
"ds_write_b64 v213, v[168:169] offset:2048\n"
"s_waitcnt lgkmcnt(0)\n"
"s_barrier\n"
"ds_read_b64 v[152:153], v214 offset:0\n"
"ds_read_b64 v[156:157], v214 offset:512\n"
"ds_read_b64 v[160:161], v214 offset:1024\n"
"ds_read_b64 v[164:165], v214 offset:1536\n"
"ds_read_b64 v[168:169], v214 offset:2048\n"
"ds_read_b32 v200, v9\n"
"v_mfma_f32_16x16x32_bf16 v[124:127], a[192:195], v[108:111], v[124:127]\n"
"global_load_dwordx4 a[20:23], v1, s[34:35] offset:1024 sc1\n"
"v_mfma_f32_16x16x32_bf16 v[128:131], a[196:199], v[108:111], v[128:131]\n"
"global_load_dwordx4 a[24:27], v1, s[34:35] offset:2048 sc1\n"
"v_mov_b32 v192, 0\n"
"v_mov_b32 v193, 0\n"
"v_mov_b32 v194, 0\n"
"v_mov_b32 v195, 0\n"
"v_mov_b32 v196, 0\n"
"s_waitcnt lgkmcnt(5)\n"
"v_mfma_f32_16x16x16_bf16 v[112:115], v[152:153], v[234:235], v[112:115]\n"
"s_waitcnt lgkmcnt(4)\n"
"s_add_u32 s34, s34, 8192\n"
"s_bitset0_b32 s34, 16\n"
"global_load_dwordx4 a[28:31], v0, s[34:35] offset:0 sc1\n"
"v_mfma_f32_16x16x16_bf16 v[116:119], v[156:157], v[234:235], v[116:119]\n"
"s_waitcnt lgkmcnt(3)\n"
"v_mfma_f32_16x16x16_bf16 v[120:123], v[160:161], v[234:235], v[120:123]\n"
"s_waitcnt lgkmcnt(2)\n"
"v_mfma_f32_16x16x16_bf16 v[124:127], v[164:165], v[234:235], v[124:127]\n"
"s_waitcnt lgkmcnt(1)\n"
"v_mfma_f32_16x16x16_bf16 v[128:131], v[168:169], v[234:235], v[128:131]\n"
"v_pk_mul_f32 v[112:113], v[112:113], v[238:239] op_sel_hi:[1,0]\n"
"global_load_dwordx4 a[36:39], v0, s[34:35] offset:2048 sc1\n"
"v_pk_mul_f32 v[114:115], v[114:115], v[238:239] op_sel_hi:[1,0]\n"
"v_cvt_pk_bf16_f32 v112, v112, v113\n"
"v_cvt_pk_bf16_f32 v113, v114, v115\n"
"ds_write_b64 v208, v[112:113] offset:0\n"
"v_pk_mul_f32 v[116:117], v[116:117], v[238:239] op_sel_hi:[1,0]\n"
"v_pk_mul_f32 v[118:119], v[118:119], v[238:239] op_sel_hi:[1,0]\n"
"v_cvt_pk_bf16_f32 v116, v116, v117\n"
"v_cvt_pk_bf16_f32 v117, v118, v119\n"
"global_load_dwordx4 a[40:43], v0, s[34:35] offset:3072 sc1\n"
"ds_write_b64 v209, v[116:117] offset:32\n"
"v_pk_mul_f32 v[120:121], v[120:121], v[238:239] op_sel_hi:[1,0]\n"
"v_pk_mul_f32 v[122:123], v[122:123], v[238:239] op_sel_hi:[1,0]\n"
"v_cvt_pk_bf16_f32 v120, v120, v121\n"
"v_cvt_pk_bf16_f32 v121, v122, v123\n"
"ds_write_b64 v210, v[120:121] offset:3096\n"
"v_pk_mul_f32 v[124:125], v[124:125], v[238:239] op_sel_hi:[1,0]\n"
"v_pk_mul_f32 v[126:127], v[126:127], v[238:239] op_sel_hi:[1,0]\n"
"global_load_dwordx4 a[32:35], v0, s[34:35] offset:1024 sc1\n"
"v_cvt_pk_bf16_f32 v124, v124, v125\n"
"v_cvt_pk_bf16_f32 v125, v126, v127\n"
"ds_write_b64 v211, v[124:125] offset:6160\n"
"v_pk_mul_f32 v[128:129], v[128:129], v[238:239] op_sel_hi:[1,0]\n"
"v_pk_mul_f32 v[130:131], v[130:131], v[238:239] op_sel_hi:[1,0]\n"
"v_cvt_pk_bf16_f32 v128, v128, v129\n"
"v_cvt_pk_bf16_f32 v129, v130, v131\n"
"ds_write_b64 v208, v[128:129] offset:9224\n"
"global_load_dwordx4 a[44:47], v1, s[34:35] offset:0 sc1\n"
"v_dot2c_f32_bf16 v192, v112, v112\n"
"v_dot2c_f32_bf16 v193, v116, v116\n"
"v_dot2c_f32_bf16 v194, v120, v120\n"
"v_dot2c_f32_bf16 v195, v124, v124\n"
"v_dot2c_f32_bf16 v196, v128, v128\n"
"v_dot2c_f32_bf16 v192, v113, v113\n"
"v_dot2c_f32_bf16 v193, v117, v117\n"
"v_dot2c_f32_bf16 v194, v121, v121\n"
"global_load_dwordx4 a[48:51], v1, s[34:35] offset:1024 sc1\n"
"v_dot2c_f32_bf16 v195, v125, v125\n"
"v_dot2c_f32_bf16 v196, v129, v129\n"
"s_waitcnt lgkmcnt(0)\n"
"s_barrier\n"
"ds_read_b128 v[104:107], v251\n"
"ds_read_b64 v[108:109], v252 offset:32\n"
"ds_read_b128 v[110:113], v251 offset:1536\n"
"ds_read_b64 v[114:115], v252 offset:1568\n"
"global_load_dwordx4 a[52:55], v1, s[34:35] offset:2048 sc1\n"
"v_cndmask_b32 v197, 0, v193, s[12:13]\n"
"v_cndmask_b32 v198, 0, v194, s[14:15]\n"
"v_cndmask_b32 v199, 0, v195, s[16:17]\n"
"v_add_f32 v192, v192, v197\n"
"v_sub_f32 v193, v193, v197\n"
"v_sub_f32 v194, v194, v198\n"
"v_sub_f32 v195, v195, v199\n"
"v_add_f32 v193, v193, v198\n"
"s_add_u32 s34, s34, 8192\n"
"s_bitset0_b32 s34, 16\n"
"global_load_dwordx4 a[56:59], v0, s[34:35] offset:0 sc1\n"
"v_add_f32 v194, v194, v199\n"
"v_add_f32 v195, v195, v196\n"
"v_permlane16_swap_b32 v192, v193\n"
"s_nop 0\n"
"v_permlane16_swap_b32 v194, v195\n"
"v_add_f32 v192, v192, v193\n"
"v_add_f32 v194, v194, v195\n"
"s_nop 1\n"
"v_permlane32_swap_b32 v192, v194\n"
"v_add_f32 v192, v192, v194\n"
"global_load_dwordx4 a[64:67], v0, s[34:35] offset:2048 sc1\n"
"v_mul_f32 v192, v192, v200\n"
"v_cmp_nge_f32 vcc, 0x45610000, v192\n"
"v_mov_b32 v116, 0xffbfffbf\n"
"v_mov_b32 v117, 0xffbfffbf\n"
"v_mov_b32 v118, 0xffbfffbf\n"
"v_mov_b32 v119, 0xffbfffbf\n"
"s_or_b32 s4, vcc_lo, vcc_hi\n"
"v_mov_b32 v8, s4\n"
"global_load_dwordx4 a[68:71], v0, s[34:35] offset:3072 sc1\n"
"ds_write_b32 v215, v8\n"
"global_load_dwordx4 a[60:63], v0, s[34:35] offset:1024 sc1\n"
"global_load_dwordx4 a[72:75], v1, s[34:35] offset:0 sc1\n"
"global_load_dwordx4 a[76:79], v1, s[34:35] offset:1024 sc1\n"
"global_load_dwordx4 a[80:83], v1, s[34:35] offset:2048 sc1\n"
".Lslow_done:\n"
"s_waitcnt vmcnt(19) lgkmcnt(3)\n"
"v_mfma_f32_32x32x16_bf16 v[32:47], a[0:3], v[104:107], 0\n"
"v_mfma_f32_32x32x8_bf16 v[32:47], a[8:9], v[108:109], v[32:47]\n"
"s_waitcnt lgkmcnt(0)\n"
"s_barrier\n"
"ds_read_b128 v[120:123], v9 offset:16\n"
"s_waitcnt vmcnt(17)\n"
"v_mfma_f32_32x32x16_bf16 v[48:63], a[4:7], v[104:107], 0\n"
"s_nop 6\n"
"v_exp_f32 v32, v32\n"
"v_exp_f32 v33, v33\n"
"v_exp_f32 v34, v34\n"
"v_exp_f32 v35, v35\n"
"v_cvt_pk_bf16_f32 v96, v32, v33\n"
"v_cvt_pk_bf16_f32 v97, v34, v35\n"
"v_exp_f32 v36, v36\n"
"v_exp_f32 v37, v37\n"
"v_exp_f32 v38, v38\n"
"v_exp_f32 v39, v39\n"
"v_cvt_pk_bf16_f32 v98, v36, v37\n"
"v_cvt_pk_bf16_f32 v99, v38, v39\n"
"v_mfma_f32_32x32x8_bf16 v[48:63], a[10:11], v[108:109], v[48:63]\n"
"s_waitcnt lgkmcnt(0)\n"
"v_or3_b32 v120, v120, v121, v122\n"
"v_or_b32 v120, v120, v123\n"
"v_cmp_ne_u32 vcc, 0, v120\n"
"v_exp_f32 v40, v40\n"
"v_exp_f32 v41, v41\n"
"v_exp_f32 v42, v42\n"
"v_exp_f32 v43, v43\n"
"v_cvt_pk_bf16_f32 v100, v40, v41\n"
"v_cvt_pk_bf16_f32 v101, v42, v43\n"
"s_waitcnt vmcnt(16)\n"
"v_mfma_f32_32x32x16_bf16 v[64:79], a[12:15], v[96:99], 0\n"
"s_cbranch_vccnz .Lslow\n"
"v_exp_f32 v44, v44\n"
"v_exp_f32 v45, v45\n"
"v_exp_f32 v46, v46\n"
"v_exp_f32 v47, v47\n"
"v_cvt_pk_bf16_f32 v102, v44, v45\n"
"v_cvt_pk_bf16_f32 v103, v46, v47\n"
"v_mfma_f32_32x32x16_bf16 v[32:47], a[0:3], v[110:113], 0\n"
"s_add_u32 s34, s34, 8192\n"
"v_exp_f32 v48, v48\n"
"v_exp_f32 v49, v49\n"
"v_exp_f32 v50, v50\n"
"v_exp_f32 v51, v51\n"
"v_cvt_pk_bf16_f32 v96, v48, v49\n"
"v_cvt_pk_bf16_f32 v97, v50, v51\n"
"v_mfma_f32_32x32x16_bf16 v[64:79], a[16:19], v[100:103], v[64:79]\n"
"s_bitset0_b32 s34, 16\n"
"v_exp_f32 v52, v52\n"
"v_exp_f32 v53, v53\n"
"v_exp_f32 v54, v54\n"
"v_exp_f32 v55, v55\n"
"v_cvt_pk_bf16_f32 v98, v52, v53\n"
"v_cvt_pk_bf16_f32 v99, v54, v55\n"
"v_mfma_f32_32x32x8_bf16 v[32:47], a[8:9], v[114:115], v[32:47]\n"
"v_exp_f32 v56, v56\n"
"v_exp_f32 v57, v57\n"
"v_exp_f32 v58, v58\n"
"v_exp_f32 v59, v59\n"
"v_cvt_pk_bf16_f32 v100, v56, v57\n"
"v_cvt_pk_bf16_f32 v101, v58, v59\n"
"s_waitcnt vmcnt(14)\n"
"v_mfma_f32_32x32x16_bf16 v[64:79], a[20:23], v[96:99], v[64:79]\n"
"global_load_dwordx4 a[84:87], v0, s[34:35] offset:0 sc1\n"
"v_exp_f32 v60, v60\n"
"v_exp_f32 v61, v61\n"
"v_exp_f32 v62, v62\n"
"v_exp_f32 v63, v63\n"
"v_cvt_pk_bf16_f32 v102, v60, v61\n"
"v_cvt_pk_bf16_f32 v103, v62, v63\n"
"s_waitcnt vmcnt(8)\n"
"v_mfma_f32_32x32x16_bf16 v[48:63], a[28:31], v[104:107], 0\n"
"global_load_dwordx4 a[92:95], v0, s[34:35] offset:2048 sc1\n"
"v_exp_f32 v32, v32\n"
"v_exp_f32 v33, v33\n"
"v_exp_f32 v34, v34\n"
"v_exp_f32 v35, v35\n"
"v_cvt_pk_bf16_f32 v96, v32, v33\n"
"v_cvt_pk_bf16_f32 v97, v34, v35\n"
"v_mfma_f32_32x32x16_bf16 v[64:79], a[24:27], v[100:103], v[64:79]\n"
"global_load_dwordx4 a[96:99], v0, s[34:35] offset:3072 sc1\n"
"v_exp_f32 v36, v36\n"
"v_exp_f32 v37, v37\n"
"v_exp_f32 v38, v38\n"
"v_exp_f32 v39, v39\n"
"v_cvt_pk_bf16_f32 v98, v36, v37\n"
"v_cvt_pk_bf16_f32 v99, v38, v39\n"
"v_mfma_f32_32x32x8_bf16 v[48:63], a[36:37], v[108:109], v[48:63]\n"
"v_exp_f32 v40, v40\n"
"v_exp_f32 v41, v41\n"
"v_exp_f32 v42, v42\n"
"v_exp_f32 v43, v43\n"
"v_cvt_pk_bf16_f32 v100, v40, v41\n"
"v_cvt_pk_bf16_f32 v101, v42, v43\n"
"v_mfma_f32_32x32x16_bf16 v[80:95], a[12:15], v[96:99], 0\n"
"global_load_dwordx4 a[88:91], v0, s[34:35] offset:1024 sc1\n"
"v_exp_f32 v44, v44\n"
"v_exp_f32 v45, v45\n"
"v_exp_f32 v46, v46\n"
"v_exp_f32 v47, v47\n"
"v_cvt_pk_bf16_f32 v102, v44, v45\n"
"v_cvt_pk_bf16_f32 v103, v46, v47\n"
"v_mfma_f32_32x32x16_bf16 v[32:47], a[32:35], v[104:107], 0\n"
"global_load_dwordx4 a[100:103], v1, s[34:35] offset:0 sc1\n"
"v_exp_f32 v48, v48\n"
"v_exp_f32 v49, v49\n"
"v_exp_f32 v50, v50\n"
"v_exp_f32 v51, v51\n"
"v_cvt_pk_bf16_f32 v96, v48, v49\n"
"v_cvt_pk_bf16_f32 v97, v50, v51\n"
"v_mfma_f32_32x32x16_bf16 v[80:95], a[16:19], v[100:103], v[80:95]\n"
"global_load_dwordx4 a[104:107], v1, s[34:35] offset:1024 sc1\n"
"v_exp_f32 v52, v52\n"
"v_exp_f32 v53, v53\n"
"v_exp_f32 v54, v54\n"
"v_exp_f32 v55, v55\n"
"v_cvt_pk_bf16_f32 v98, v52, v53\n"
"v_cvt_pk_bf16_f32 v99, v54, v55\n"
"v_mfma_f32_32x32x8_bf16 v[32:47], a[38:39], v[108:109], v[32:47]\n"
"v_exp_f32 v56, v56\n"
"v_exp_f32 v57, v57\n"
"v_exp_f32 v58, v58\n"
"v_exp_f32 v59, v59\n"
"v_cvt_pk_bf16_f32 v100, v56, v57\n"
"v_cvt_pk_bf16_f32 v101, v58, v59\n"
"v_mfma_f32_32x32x16_bf16 v[64:79], a[40:43], v[96:99], v[64:79]\n"
"global_load_dwordx4 a[108:111], v1, s[34:35] offset:2048 sc1\n"
"v_exp_f32 v60, v60\n"
"v_exp_f32 v61, v61\n"
"v_exp_f32 v62, v62\n"
"v_exp_f32 v63, v63\n"
"v_cvt_pk_bf16_f32 v102, v60, v61\n"
"v_cvt_pk_bf16_f32 v103, v62, v63\n"
"v_mfma_f32_32x32x16_bf16 v[48:63], a[4:7], v[110:113], 0\n"
"s_add_u32 s34, s34, 8192\n"
"v_exp_f32 v32, v32\n"
"v_exp_f32 v33, v33\n"
"v_exp_f32 v34, v34\n"
"v_exp_f32 v35, v35\n"
"v_cvt_pk_bf16_f32 v96, v32, v33\n"
"v_cvt_pk_bf16_f32 v97, v34, v35\n"
"v_mfma_f32_32x32x16_bf16 v[64:79], a[44:47], v[100:103], v[64:79]\n"
"s_bitset0_b32 s34, 16\n"
"v_exp_f32 v36, v36\n"
"v_exp_f32 v37, v37\n"
"v_exp_f32 v38, v38\n"
"v_exp_f32 v39, v39\n"
"v_cvt_pk_bf16_f32 v98, v36, v37\n"
"v_cvt_pk_bf16_f32 v99, v38, v39\n"
"v_mfma_f32_32x32x8_bf16 v[48:63], a[10:11], v[114:115], v[48:63]\n"
"v_exp_f32 v40, v40\n"
"v_exp_f32 v41, v41\n"
"v_exp_f32 v42, v42\n"
"v_exp_f32 v43, v43\n"
"v_cvt_pk_bf16_f32 v100, v40, v41\n"
"v_cvt_pk_bf16_f32 v101, v42, v43\n"
"v_mfma_f32_32x32x16_bf16 v[64:79], a[48:51], v[96:99], v[64:79]\n"
"global_load_dwordx4 a[112:115], v0, s[34:35] offset:0 sc1\n"
"v_exp_f32 v44, v44\n"
"v_exp_f32 v45, v45\n"
"v_exp_f32 v46, v46\n"
"v_exp_f32 v47, v47\n"
"v_cvt_pk_bf16_f32 v102, v44, v45\n"
"v_cvt_pk_bf16_f32 v103, v46, v47\n"
"s_waitcnt vmcnt(8)\n"
"v_mfma_f32_32x32x16_bf16 v[32:47], a[56:59], v[104:107], 0\n"
"global_load_dwordx4 a[120:123], v0, s[34:35] offset:2048 sc1\n"
"v_exp_f32 v48, v48\n"
"v_exp_f32 v49, v49\n"
"v_exp_f32 v50, v50\n"
"v_exp_f32 v51, v51\n"
"v_cvt_pk_bf16_f32 v96, v48, v49\n"
"v_cvt_pk_bf16_f32 v97, v50, v51\n"
"v_mfma_f32_32x32x16_bf16 v[64:79], a[52:55], v[100:103], v[64:79]\n"
"global_load_dwordx4 a[124:127], v0, s[34:35] offset:3072 sc1\n"
"v_exp_f32 v52, v52\n"
"v_exp_f32 v53, v53\n"
"v_exp_f32 v54, v54\n"
"v_exp_f32 v55, v55\n"
"v_cvt_pk_bf16_f32 v98, v52, v53\n"
"v_cvt_pk_bf16_f32 v99, v54, v55\n"
"v_mfma_f32_32x32x8_bf16 v[32:47], a[64:65], v[108:109], v[32:47]\n"
"v_exp_f32 v56, v56\n"
"v_exp_f32 v57, v57\n"
"v_exp_f32 v58, v58\n"
"v_exp_f32 v59, v59\n"
"v_cvt_pk_bf16_f32 v100, v56, v57\n"
"v_cvt_pk_bf16_f32 v101, v58, v59\n"
"v_mfma_f32_32x32x16_bf16 v[80:95], a[20:23], v[96:99], v[80:95]\n"
"global_load_dwordx4 a[116:119], v0, s[34:35] offset:1024 sc1\n"
"v_exp_f32 v60, v60\n"
"v_exp_f32 v61, v61\n"
"v_exp_f32 v62, v62\n"
"v_exp_f32 v63, v63\n"
"v_cvt_pk_bf16_f32 v102, v60, v61\n"
"v_cvt_pk_bf16_f32 v103, v62, v63\n"
"v_mfma_f32_32x32x16_bf16 v[48:63], a[60:63], v[104:107], 0\n"
"global_load_dwordx4 a[128:131], v1, s[34:35] offset:0 sc1\n"
"v_exp_f32 v32, v32\n"
"v_exp_f32 v33, v33\n"
"v_exp_f32 v34, v34\n"
"v_exp_f32 v35, v35\n"
"v_cvt_pk_bf16_f32 v96, v32, v33\n"
"v_cvt_pk_bf16_f32 v97, v34, v35\n"
"v_mfma_f32_32x32x16_bf16 v[80:95], a[24:27], v[100:103], v[80:95]\n"
"global_load_dwordx4 a[132:135], v1, s[34:35] offset:1024 sc1\n"
"v_exp_f32 v36, v36\n"
"v_exp_f32 v37, v37\n"
"v_exp_f32 v38, v38\n"
"v_exp_f32 v39, v39\n"
"v_cvt_pk_bf16_f32 v98, v36, v37\n"
"v_cvt_pk_bf16_f32 v99, v38, v39\n"
"v_mfma_f32_32x32x8_bf16 v[48:63], a[66:67], v[108:109], v[48:63]\n"
"v_exp_f32 v40, v40\n"
"v_exp_f32 v41, v41\n"
"v_exp_f32 v42, v42\n"
"v_exp_f32 v43, v43\n"
"v_cvt_pk_bf16_f32 v100, v40, v41\n"
"v_cvt_pk_bf16_f32 v101, v42, v43\n"
"v_mfma_f32_32x32x16_bf16 v[64:79], a[68:71], v[96:99], v[64:79]\n"
"global_load_dwordx4 a[136:139], v1, s[34:35] offset:2048 sc1\n"
"v_exp_f32 v44, v44\n"
"v_exp_f32 v45, v45\n"
"v_exp_f32 v46, v46\n"
"v_exp_f32 v47, v47\n"
"v_cvt_pk_bf16_f32 v102, v44, v45\n"
"v_cvt_pk_bf16_f32 v103, v46, v47\n"
"v_mfma_f32_32x32x16_bf16 v[32:47], a[28:31], v[110:113], 0\n"
"s_add_u32 s34, s34, 8192\n"
"v_exp_f32 v48, v48\n"
"v_exp_f32 v49, v49\n"
"v_exp_f32 v50, v50\n"
"v_exp_f32 v51, v51\n"
"v_cvt_pk_bf16_f32 v96, v48, v49\n"
"v_cvt_pk_bf16_f32 v97, v50, v51\n"
"v_mfma_f32_32x32x16_bf16 v[64:79], a[72:75], v[100:103], v[64:79]\n"
"s_bitset0_b32 s34, 16\n"
"v_exp_f32 v52, v52\n"
"v_exp_f32 v53, v53\n"
"v_exp_f32 v54, v54\n"
"v_exp_f32 v55, v55\n"
"v_cvt_pk_bf16_f32 v98, v52, v53\n"
"v_cvt_pk_bf16_f32 v99, v54, v55\n"
"v_mfma_f32_32x32x8_bf16 v[32:47], a[36:37], v[114:115], v[32:47]\n"
"v_exp_f32 v56, v56\n"
"v_exp_f32 v57, v57\n"
"v_exp_f32 v58, v58\n"
"v_exp_f32 v59, v59\n"
"v_cvt_pk_bf16_f32 v100, v56, v57\n"
"v_cvt_pk_bf16_f32 v101, v58, v59\n"
"v_mfma_f32_32x32x16_bf16 v[64:79], a[76:79], v[96:99], v[64:79]\n"
"global_load_dwordx4 a[140:143], v0, s[34:35] offset:0 sc1\n"
"v_exp_f32 v60, v60\n"
"v_exp_f32 v61, v61\n"
"v_exp_f32 v62, v62\n"
"v_exp_f32 v63, v63\n"
"v_cvt_pk_bf16_f32 v102, v60, v61\n"
"v_cvt_pk_bf16_f32 v103, v62, v63\n"
"s_waitcnt vmcnt(8)\n"
"v_mfma_f32_32x32x16_bf16 v[48:63], a[84:87], v[104:107], 0\n"
"global_load_dwordx4 a[148:151], v0, s[34:35] offset:2048 sc1\n"
"v_exp_f32 v32, v32\n"
"v_exp_f32 v33, v33\n"
"v_exp_f32 v34, v34\n"
"v_exp_f32 v35, v35\n"
"v_cvt_pk_bf16_f32 v96, v32, v33\n"
"v_cvt_pk_bf16_f32 v97, v34, v35\n"
"v_mfma_f32_32x32x16_bf16 v[64:79], a[80:83], v[100:103], v[64:79]\n"
"global_load_dwordx4 a[152:155], v0, s[34:35] offset:3072 sc1\n"
"v_exp_f32 v36, v36\n"
"v_exp_f32 v37, v37\n"
"v_exp_f32 v38, v38\n"
"v_exp_f32 v39, v39\n"
"v_cvt_pk_bf16_f32 v98, v36, v37\n"
"v_cvt_pk_bf16_f32 v99, v38, v39\n"
"v_mfma_f32_32x32x8_bf16 v[48:63], a[92:93], v[108:109], v[48:63]\n"
"v_exp_f32 v40, v40\n"
"v_exp_f32 v41, v41\n"
"v_exp_f32 v42, v42\n"
"v_exp_f32 v43, v43\n"
"v_cvt_pk_bf16_f32 v100, v40, v41\n"
"v_cvt_pk_bf16_f32 v101, v42, v43\n"
"v_mfma_f32_32x32x16_bf16 v[80:95], a[40:43], v[96:99], v[80:95]\n"
"global_load_dwordx4 a[144:147], v0, s[34:35] offset:1024 sc1\n"
"v_exp_f32 v44, v44\n"
"v_exp_f32 v45, v45\n"
"v_exp_f32 v46, v46\n"
"v_exp_f32 v47, v47\n"
"v_cvt_pk_bf16_f32 v102, v44, v45\n"
"v_cvt_pk_bf16_f32 v103, v46, v47\n"
"v_mfma_f32_32x32x16_bf16 v[32:47], a[88:91], v[104:107], 0\n"
"global_load_dwordx4 a[156:159], v1, s[34:35] offset:0 sc1\n"
"v_exp_f32 v48, v48\n"
"v_exp_f32 v49, v49\n"
"v_exp_f32 v50, v50\n"
"v_exp_f32 v51, v51\n"
"v_cvt_pk_bf16_f32 v96, v48, v49\n"
"v_cvt_pk_bf16_f32 v97, v50, v51\n"
"v_mfma_f32_32x32x16_bf16 v[80:95], a[44:47], v[100:103], v[80:95]\n"
"global_load_dwordx4 a[160:163], v1, s[34:35] offset:1024 sc1\n"
"v_exp_f32 v52, v52\n"
"v_exp_f32 v53, v53\n"
"v_exp_f32 v54, v54\n"
"v_exp_f32 v55, v55\n"
"v_cvt_pk_bf16_f32 v98, v52, v53\n"
"v_cvt_pk_bf16_f32 v99, v54, v55\n"
"v_mfma_f32_32x32x8_bf16 v[32:47], a[94:95], v[108:109], v[32:47]\n"
"v_exp_f32 v56, v56\n"
"v_exp_f32 v57, v57\n"
"v_exp_f32 v58, v58\n"
"v_exp_f32 v59, v59\n"
"v_cvt_pk_bf16_f32 v100, v56, v57\n"
"v_cvt_pk_bf16_f32 v101, v58, v59\n"
"v_mfma_f32_32x32x16_bf16 v[64:79], a[96:99], v[96:99], v[64:79]\n"
"global_load_dwordx4 a[164:167], v1, s[34:35] offset:2048 sc1\n"
"v_exp_f32 v60, v60\n"
"v_exp_f32 v61, v61\n"
"v_exp_f32 v62, v62\n"
"v_exp_f32 v63, v63\n"
"v_cvt_pk_bf16_f32 v102, v60, v61\n"
"v_cvt_pk_bf16_f32 v103, v62, v63\n"
"v_mfma_f32_32x32x16_bf16 v[48:63], a[32:35], v[110:113], 0\n"
"s_add_u32 s34, s34, 8192\n"
"v_exp_f32 v32, v32\n"
"v_exp_f32 v33, v33\n"
"v_exp_f32 v34, v34\n"
"v_exp_f32 v35, v35\n"
"v_cvt_pk_bf16_f32 v96, v32, v33\n"
"v_cvt_pk_bf16_f32 v97, v34, v35\n"
"v_mfma_f32_32x32x16_bf16 v[64:79], a[100:103], v[100:103], v[64:79]\n"
"s_bitset0_b32 s34, 16\n"
"v_exp_f32 v36, v36\n"
"v_exp_f32 v37, v37\n"
"v_exp_f32 v38, v38\n"
"v_exp_f32 v39, v39\n"
"v_cvt_pk_bf16_f32 v98, v36, v37\n"
"v_cvt_pk_bf16_f32 v99, v38, v39\n"
"v_mfma_f32_32x32x8_bf16 v[48:63], a[38:39], v[114:115], v[48:63]\n"
"v_exp_f32 v40, v40\n"
"v_exp_f32 v41, v41\n"
"v_exp_f32 v42, v42\n"
"v_exp_f32 v43, v43\n"
"v_cvt_pk_bf16_f32 v100, v40, v41\n"
"v_cvt_pk_bf16_f32 v101, v42, v43\n"
"v_mfma_f32_32x32x16_bf16 v[64:79], a[104:107], v[96:99], v[64:79]\n"
"global_load_dwordx4 a[168:171], v0, s[34:35] offset:0 sc1\n"
"v_exp_f32 v44, v44\n"
"v_exp_f32 v45, v45\n"
"v_exp_f32 v46, v46\n"
"v_exp_f32 v47, v47\n"
"v_cvt_pk_bf16_f32 v102, v44, v45\n"
"v_cvt_pk_bf16_f32 v103, v46, v47\n"
"s_waitcnt vmcnt(8)\n"
"v_mfma_f32_32x32x16_bf16 v[32:47], a[112:115], v[104:107], 0\n"
"global_load_dwordx4 a[176:179], v0, s[34:35] offset:2048 sc1\n"
"v_exp_f32 v48, v48\n"
"v_exp_f32 v49, v49\n"
"v_exp_f32 v50, v50\n"
"v_exp_f32 v51, v51\n"
"v_cvt_pk_bf16_f32 v96, v48, v49\n"
"v_cvt_pk_bf16_f32 v97, v50, v51\n"
"v_mfma_f32_32x32x16_bf16 v[64:79], a[108:111], v[100:103], v[64:79]\n"
"global_load_dwordx4 a[180:183], v0, s[34:35] offset:3072 sc1\n"
"v_exp_f32 v52, v52\n"
"v_exp_f32 v53, v53\n"
"v_exp_f32 v54, v54\n"
"v_exp_f32 v55, v55\n"
"v_cvt_pk_bf16_f32 v98, v52, v53\n"
"v_cvt_pk_bf16_f32 v99, v54, v55\n"
"v_mfma_f32_32x32x8_bf16 v[32:47], a[120:121], v[108:109], v[32:47]\n"
"v_exp_f32 v56, v56\n"
"v_exp_f32 v57, v57\n"
"v_exp_f32 v58, v58\n"
"v_exp_f32 v59, v59\n"
"v_cvt_pk_bf16_f32 v100, v56, v57\n"
"v_cvt_pk_bf16_f32 v101, v58, v59\n"
"v_mfma_f32_32x32x16_bf16 v[80:95], a[48:51], v[96:99], v[80:95]\n"
"global_load_dwordx4 a[172:175], v0, s[34:35] offset:1024 sc1\n"
"v_exp_f32 v60, v60\n"
"v_exp_f32 v61, v61\n"
"v_exp_f32 v62, v62\n"
"v_exp_f32 v63, v63\n"
"v_cvt_pk_bf16_f32 v102, v60, v61\n"
"v_cvt_pk_bf16_f32 v103, v62, v63\n"
"v_mfma_f32_32x32x16_bf16 v[48:63], a[116:119], v[104:107], 0\n"
"global_load_dwordx4 a[184:187], v1, s[34:35] offset:0 sc1\n"
"v_exp_f32 v32, v32\n"
"v_exp_f32 v33, v33\n"
"v_exp_f32 v34, v34\n"
"v_exp_f32 v35, v35\n"
"v_cvt_pk_bf16_f32 v96, v32, v33\n"
"v_cvt_pk_bf16_f32 v97, v34, v35\n"
"v_mfma_f32_32x32x16_bf16 v[80:95], a[52:55], v[100:103], v[80:95]\n"
"global_load_dwordx4 a[188:191], v1, s[34:35] offset:1024 sc1\n"
"v_exp_f32 v36, v36\n"
"v_exp_f32 v37, v37\n"
"v_exp_f32 v38, v38\n"
"v_exp_f32 v39, v39\n"
"v_cvt_pk_bf16_f32 v98, v36, v37\n"
"v_cvt_pk_bf16_f32 v99, v38, v39\n"
"v_mfma_f32_32x32x8_bf16 v[48:63], a[122:123], v[108:109], v[48:63]\n"
"v_exp_f32 v40, v40\n"
"v_exp_f32 v41, v41\n"
"v_exp_f32 v42, v42\n"
"v_exp_f32 v43, v43\n"
"v_cvt_pk_bf16_f32 v100, v40, v41\n"
"v_cvt_pk_bf16_f32 v101, v42, v43\n"
"v_mfma_f32_32x32x16_bf16 v[64:79], a[124:127], v[96:99], v[64:79]\n"
"global_load_dwordx4 a[192:195], v1, s[34:35] offset:2048 sc1\n"
"v_exp_f32 v44, v44\n"
"v_exp_f32 v45, v45\n"
"v_exp_f32 v46, v46\n"
"v_exp_f32 v47, v47\n"
"v_cvt_pk_bf16_f32 v102, v44, v45\n"
"v_cvt_pk_bf16_f32 v103, v46, v47\n"
"v_mfma_f32_32x32x16_bf16 v[32:47], a[56:59], v[110:113], 0\n"
"s_add_u32 s34, s34, 8192\n"
"v_exp_f32 v48, v48\n"
"v_exp_f32 v49, v49\n"
"v_exp_f32 v50, v50\n"
"v_exp_f32 v51, v51\n"
"v_cvt_pk_bf16_f32 v96, v48, v49\n"
"v_cvt_pk_bf16_f32 v97, v50, v51\n"
"v_mfma_f32_32x32x16_bf16 v[64:79], a[128:131], v[100:103], v[64:79]\n"
"s_bitset0_b32 s34, 16\n"
"v_exp_f32 v52, v52\n"
"v_exp_f32 v53, v53\n"
"v_exp_f32 v54, v54\n"
"v_exp_f32 v55, v55\n"
"v_cvt_pk_bf16_f32 v98, v52, v53\n"
"v_cvt_pk_bf16_f32 v99, v54, v55\n"
"v_mfma_f32_32x32x8_bf16 v[32:47], a[64:65], v[114:115], v[32:47]\n"
"v_exp_f32 v56, v56\n"
"v_exp_f32 v57, v57\n"
"v_exp_f32 v58, v58\n"
"v_exp_f32 v59, v59\n"
"v_cvt_pk_bf16_f32 v100, v56, v57\n"
"v_cvt_pk_bf16_f32 v101, v58, v59\n"
"v_mfma_f32_32x32x16_bf16 v[64:79], a[132:135], v[96:99], v[64:79]\n"
"global_load_dwordx4 a[196:199], v0, s[34:35] offset:0 sc1\n"
"v_exp_f32 v60, v60\n"
"v_exp_f32 v61, v61\n"
"v_exp_f32 v62, v62\n"
"v_exp_f32 v63, v63\n"
"v_cvt_pk_bf16_f32 v102, v60, v61\n"
"v_cvt_pk_bf16_f32 v103, v62, v63\n"
"s_waitcnt vmcnt(8)\n"
"v_mfma_f32_32x32x16_bf16 v[48:63], a[140:143], v[104:107], 0\n"
"global_load_dwordx4 a[204:207], v0, s[34:35] offset:2048 sc1\n"
"v_exp_f32 v32, v32\n"
"v_exp_f32 v33, v33\n"
"v_exp_f32 v34, v34\n"
"v_exp_f32 v35, v35\n"
"v_cvt_pk_bf16_f32 v96, v32, v33\n"
"v_cvt_pk_bf16_f32 v97, v34, v35\n"
"v_mfma_f32_32x32x16_bf16 v[64:79], a[136:139], v[100:103], v[64:79]\n"
"global_load_dwordx4 a[208:211], v0, s[34:35] offset:3072 sc1\n"
"v_exp_f32 v36, v36\n"
"v_exp_f32 v37, v37\n"
"v_exp_f32 v38, v38\n"
"v_exp_f32 v39, v39\n"
"v_cvt_pk_bf16_f32 v98, v36, v37\n"
"v_cvt_pk_bf16_f32 v99, v38, v39\n"
"v_mfma_f32_32x32x8_bf16 v[48:63], a[148:149], v[108:109], v[48:63]\n"
"v_exp_f32 v40, v40\n"
"v_exp_f32 v41, v41\n"
"v_exp_f32 v42, v42\n"
"v_exp_f32 v43, v43\n"
"v_cvt_pk_bf16_f32 v100, v40, v41\n"
"v_cvt_pk_bf16_f32 v101, v42, v43\n"
"v_mfma_f32_32x32x16_bf16 v[80:95], a[68:71], v[96:99], v[80:95]\n"
"global_load_dwordx4 a[200:203], v0, s[34:35] offset:1024 sc1\n"
"v_exp_f32 v44, v44\n"
"v_exp_f32 v45, v45\n"
"v_exp_f32 v46, v46\n"
"v_exp_f32 v47, v47\n"
"v_cvt_pk_bf16_f32 v102, v44, v45\n"
"v_cvt_pk_bf16_f32 v103, v46, v47\n"
"v_mfma_f32_32x32x16_bf16 v[32:47], a[144:147], v[104:107], 0\n"
"global_load_dwordx4 a[212:215], v1, s[34:35] offset:0 sc1\n"
"v_exp_f32 v48, v48\n"
"v_exp_f32 v49, v49\n"
"v_exp_f32 v50, v50\n"
"v_exp_f32 v51, v51\n"
"v_cvt_pk_bf16_f32 v96, v48, v49\n"
"v_cvt_pk_bf16_f32 v97, v50, v51\n"
"v_mfma_f32_32x32x16_bf16 v[80:95], a[72:75], v[100:103], v[80:95]\n"
"global_load_dwordx4 a[216:219], v1, s[34:35] offset:1024 sc1\n"
"v_exp_f32 v52, v52\n"
"v_exp_f32 v53, v53\n"
"v_exp_f32 v54, v54\n"
"v_exp_f32 v55, v55\n"
"v_cvt_pk_bf16_f32 v98, v52, v53\n"
"v_cvt_pk_bf16_f32 v99, v54, v55\n"
"v_mfma_f32_32x32x8_bf16 v[32:47], a[150:151], v[108:109], v[32:47]\n"
"v_exp_f32 v56, v56\n"
"v_exp_f32 v57, v57\n"
"v_exp_f32 v58, v58\n"
"v_exp_f32 v59, v59\n"
"v_cvt_pk_bf16_f32 v100, v56, v57\n"
"v_cvt_pk_bf16_f32 v101, v58, v59\n"
"v_mfma_f32_32x32x16_bf16 v[64:79], a[152:155], v[96:99], v[64:79]\n"
"global_load_dwordx4 a[220:223], v1, s[34:35] offset:2048 sc1\n"
"v_exp_f32 v60, v60\n"
"v_exp_f32 v61, v61\n"
"v_exp_f32 v62, v62\n"
"v_exp_f32 v63, v63\n"
"v_cvt_pk_bf16_f32 v102, v60, v61\n"
"v_cvt_pk_bf16_f32 v103, v62, v63\n"
"v_mfma_f32_32x32x16_bf16 v[48:63], a[60:63], v[110:113], 0\n"
"s_mov_b32 m0, s43\n"
"v_exp_f32 v32, v32\n"
"v_exp_f32 v33, v33\n"
"v_exp_f32 v34, v34\n"
"v_exp_f32 v35, v35\n"
"v_cvt_pk_bf16_f32 v96, v32, v33\n"
"v_cvt_pk_bf16_f32 v97, v34, v35\n"
"v_mfma_f32_32x32x16_bf16 v[64:79], a[156:159], v[100:103], v[64:79]\n"
"buffer_load_dwordx4 v10, s[88:91], s36 offen offset:0 lds\n"
"v_exp_f32 v36, v36\n"
"v_exp_f32 v37, v37\n"
"v_exp_f32 v38, v38\n"
"v_exp_f32 v39, v39\n"
"v_cvt_pk_bf16_f32 v98, v36, v37\n"
"v_cvt_pk_bf16_f32 v99, v38, v39\n"
"v_mfma_f32_32x32x8_bf16 v[48:63], a[66:67], v[114:115], v[48:63]\n"
"v_exp_f32 v40, v40\n"
"v_exp_f32 v41, v41\n"
"v_exp_f32 v42, v42\n"
"v_exp_f32 v43, v43\n"
"v_cvt_pk_bf16_f32 v100, v40, v41\n"
"v_cvt_pk_bf16_f32 v101, v42, v43\n"
"v_mfma_f32_32x32x16_bf16 v[64:79], a[160:163], v[96:99], v[64:79]\n"
"s_add_u32 s36, s36, s37\n"
"v_exp_f32 v44, v44\n"
"v_exp_f32 v45, v45\n"
"v_exp_f32 v46, v46\n"
"v_exp_f32 v47, v47\n"
"v_cvt_pk_bf16_f32 v102, v44, v45\n"
"v_cvt_pk_bf16_f32 v103, v46, v47\n"
"s_waitcnt vmcnt(8)\n"
"v_mfma_f32_32x32x16_bf16 v[32:47], a[168:171], v[104:107], 0\n"
"s_mov_b32 s29, s21\n"
"v_exp_f32 v48, v48\n"
"v_exp_f32 v49, v49\n"
"v_exp_f32 v50, v50\n"
"v_exp_f32 v51, v51\n"
"v_cvt_pk_bf16_f32 v96, v48, v49\n"
"v_cvt_pk_bf16_f32 v97, v50, v51\n"
"v_mfma_f32_32x32x16_bf16 v[64:79], a[164:167], v[100:103], v[64:79]\n"
"s_mov_b32 s28, s44\n"
"v_exp_f32 v52, v52\n"
"v_exp_f32 v53, v53\n"
"v_exp_f32 v54, v54\n"
"v_exp_f32 v55, v55\n"
"v_cvt_pk_bf16_f32 v98, v52, v53\n"
"v_cvt_pk_bf16_f32 v99, v54, v55\n"
"v_mfma_f32_32x32x8_bf16 v[32:47], a[176:177], v[108:109], v[32:47]\n"
".Lhop1:\n"
"s_cbranch_execz .Lhop5\n"
"v_exp_f32 v56, v56\n"
"v_exp_f32 v57, v57\n"
"v_exp_f32 v58, v58\n"
"v_exp_f32 v59, v59\n"
"v_cvt_pk_bf16_f32 v100, v56, v57\n"
"v_cvt_pk_bf16_f32 v101, v58, v59\n"
"v_mfma_f32_32x32x16_bf16 v[80:95], a[76:79], v[96:99], v[80:95]\n"
"global_store_dwordx4 v0, v[116:119], s[28:29] offset:0 sc1\n"
"v_exp_f32 v60, v60\n"
"v_exp_f32 v61, v61\n"
"v_exp_f32 v62, v62\n"
"v_exp_f32 v63, v63\n"
"v_cvt_pk_bf16_f32 v102, v60, v61\n"
"v_cvt_pk_bf16_f32 v103, v62, v63\n"
"v_mfma_f32_32x32x16_bf16 v[48:63], a[172:175], v[104:107], 0\n"
"s_mul_i32 s48, s58, 102400\n"
"v_exp_f32 v32, v32\n"
"v_exp_f32 v33, v33\n"
"v_exp_f32 v34, v34\n"
"v_exp_f32 v35, v35\n"
"v_cvt_pk_bf16_f32 v96, v32, v33\n"
"v_cvt_pk_bf16_f32 v97, v34, v35\n"
"v_mfma_f32_32x32x16_bf16 v[80:95], a[80:83], v[100:103], v[80:95]\n"
"buffer_load_dwordx4 v10, s[88:91], s36 offen offset:1024 lds\n"
"v_exp_f32 v36, v36\n"
"v_exp_f32 v37, v37\n"
"v_exp_f32 v38, v38\n"
"v_exp_f32 v39, v39\n"
"v_cvt_pk_bf16_f32 v98, v36, v37\n"
"v_cvt_pk_bf16_f32 v99, v38, v39\n"
"v_mfma_f32_32x32x8_bf16 v[48:63], a[178:179], v[108:109], v[48:63]\n"
"v_exp_f32 v40, v40\n"
"v_exp_f32 v41, v41\n"
"v_exp_f32 v42, v42\n"
"v_exp_f32 v43, v43\n"
"v_cvt_pk_bf16_f32 v100, v40, v41\n"
"v_cvt_pk_bf16_f32 v101, v42, v43\n"
"v_mfma_f32_32x32x16_bf16 v[64:79], a[180:183], v[96:99], v[64:79]\n"
"s_add_u32 s36, s36, s37\n"
"v_exp_f32 v44, v44\n"
"v_exp_f32 v45, v45\n"
"v_exp_f32 v46, v46\n"
"v_exp_f32 v47, v47\n"
"v_cvt_pk_bf16_f32 v102, v44, v45\n"
"v_cvt_pk_bf16_f32 v103, v46, v47\n"
"v_mfma_f32_32x32x16_bf16 v[32:47], a[84:87], v[110:113], 0\n"
"s_add_u32 s48, s48, s86\n"
"v_exp_f32 v48, v48\n"
"v_exp_f32 v49, v49\n"
"v_exp_f32 v50, v50\n"
"v_exp_f32 v51, v51\n"
"v_cvt_pk_bf16_f32 v96, v48, v49\n"
"v_cvt_pk_bf16_f32 v97, v50, v51\n"
"v_mfma_f32_32x32x16_bf16 v[64:79], a[184:187], v[100:103], v[64:79]\n"
"global_store_dwordx4 v0, v[116:119], s[28:29] offset:1024 sc1\n"
"v_exp_f32 v52, v52\n"
"v_exp_f32 v53, v53\n"
"v_exp_f32 v54, v54\n"
"v_exp_f32 v55, v55\n"
"v_cvt_pk_bf16_f32 v98, v52, v53\n"
"v_cvt_pk_bf16_f32 v99, v54, v55\n"
"v_mfma_f32_32x32x8_bf16 v[32:47], a[92:93], v[114:115], v[32:47]\n"
"v_exp_f32 v56, v56\n"
"v_exp_f32 v57, v57\n"
"v_exp_f32 v58, v58\n"
"v_exp_f32 v59, v59\n"
"v_cvt_pk_bf16_f32 v100, v56, v57\n"
"v_cvt_pk_bf16_f32 v101, v58, v59\n"
"v_mfma_f32_32x32x16_bf16 v[64:79], a[188:191], v[96:99], v[64:79]\n"
"s_add_u32 s48, s48, 13238272\n"
"v_exp_f32 v60, v60\n"
"v_exp_f32 v61, v61\n"
"v_exp_f32 v62, v62\n"
"v_exp_f32 v63, v63\n"
"v_cvt_pk_bf16_f32 v102, v60, v61\n"
"v_cvt_pk_bf16_f32 v103, v62, v63\n"
"s_waitcnt vmcnt(4)\n"
"v_mfma_f32_32x32x16_bf16 v[48:63], a[196:199], v[104:107], 0\n"
"s_add_u32 s63, s56, 0\n"
"v_exp_f32 v32, v32\n"
"v_exp_f32 v33, v33\n"
"v_exp_f32 v34, v34\n"
"v_exp_f32 v35, v35\n"
"v_cvt_pk_bf16_f32 v96, v32, v33\n"
"v_cvt_pk_bf16_f32 v97, v34, v35\n"
"v_mfma_f32_32x32x16_bf16 v[64:79], a[192:195], v[100:103], v[64:79]\n"
"buffer_load_dwordx4 v10, s[88:91], s36 offen offset:2048 lds\n"
"v_exp_f32 v36, v36\n"
"v_exp_f32 v37, v37\n"
"v_exp_f32 v38, v38\n"
"v_exp_f32 v39, v39\n"
"v_cvt_pk_bf16_f32 v98, v36, v37\n"
"v_cvt_pk_bf16_f32 v99, v38, v39\n"
"v_mfma_f32_32x32x8_bf16 v[48:63], a[204:205], v[108:109], v[48:63]\n"
"v_exp_f32 v40, v40\n"
"v_exp_f32 v41, v41\n"
"v_exp_f32 v42, v42\n"
"v_exp_f32 v43, v43\n"
"v_cvt_pk_bf16_f32 v100, v40, v41\n"
"v_cvt_pk_bf16_f32 v101, v42, v43\n"
"v_mfma_f32_32x32x16_bf16 v[80:95], a[96:99], v[96:99], v[80:95]\n"
"global_store_dwordx4 v16, v[116:119], s[28:29] offset:2048 sc1\n"
"v_exp_f32 v44, v44\n"
"v_exp_f32 v45, v45\n"
"v_exp_f32 v46, v46\n"
"v_exp_f32 v47, v47\n"
"v_cvt_pk_bf16_f32 v102, v44, v45\n"
"v_cvt_pk_bf16_f32 v103, v46, v47\n"
"v_mfma_f32_32x32x16_bf16 v[32:47], a[200:203], v[104:107], 0\n"
"s_and_b32 s63, s63, 3\n"
"v_exp_f32 v48, v48\n"
"v_exp_f32 v49, v49\n"
"v_exp_f32 v50, v50\n"
"v_exp_f32 v51, v51\n"
"v_cvt_pk_bf16_f32 v96, v48, v49\n"
"v_cvt_pk_bf16_f32 v97, v50, v51\n"
"v_mfma_f32_32x32x16_bf16 v[80:95], a[100:103], v[100:103], v[80:95]\n"
"s_mul_i32 s63, s63, 3072\n"
"v_lshl_add_u32 v124, s63, 1, v28\n"
"v_exp_f32 v52, v52\n"
"v_exp_f32 v53, v53\n"
"v_exp_f32 v54, v54\n"
"v_exp_f32 v55, v55\n"
"v_cvt_pk_bf16_f32 v98, v52, v53\n"
"v_cvt_pk_bf16_f32 v99, v54, v55\n"
"v_mfma_f32_32x32x8_bf16 v[32:47], a[206:207], v[108:109], v[32:47]\n"
"v_exp_f32 v56, v56\n"
"v_exp_f32 v57, v57\n"
"v_exp_f32 v58, v58\n"
"v_exp_f32 v59, v59\n"
"v_cvt_pk_bf16_f32 v100, v56, v57\n"
"v_cvt_pk_bf16_f32 v101, v58, v59\n"
"v_mfma_f32_32x32x16_bf16 v[64:79], a[208:211], v[96:99], v[64:79]\n"
"s_add_u32 s36, s36, s37\n"
"v_exp_f32 v60, v60\n"
"v_exp_f32 v61, v61\n"
"v_exp_f32 v62, v62\n"
"v_exp_f32 v63, v63\n"
"v_cvt_pk_bf16_f32 v102, v60, v61\n"
"v_cvt_pk_bf16_f32 v103, v62, v63\n"
"v_mfma_f32_32x32x16_bf16 v[48:63], a[88:91], v[110:113], 0\n"
"s_mov_b32 s28, s45\n"
"v_exp_f32 v32, v32\n"
"v_exp_f32 v33, v33\n"
"v_exp_f32 v34, v34\n"
"v_exp_f32 v35, v35\n"
"v_cvt_pk_bf16_f32 v96, v32, v33\n"
"v_cvt_pk_bf16_f32 v97, v34, v35\n"
"v_mfma_f32_32x32x16_bf16 v[64:79], a[212:215], v[100:103], v[64:79]\n"
"global_store_dwordx4 v0, v[116:119], s[28:29] offset:0 sc1\n"
"v_exp_f32 v36, v36\n"
"v_exp_f32 v37, v37\n"
"v_exp_f32 v38, v38\n"
"v_exp_f32 v39, v39\n"
"v_cvt_pk_bf16_f32 v98, v36, v37\n"
"v_cvt_pk_bf16_f32 v99, v38, v39\n"
"v_mfma_f32_32x32x8_bf16 v[48:63], a[94:95], v[114:115], v[48:63]\n"
"v_exp_f32 v40, v40\n"
"v_exp_f32 v41, v41\n"
"v_exp_f32 v42, v42\n"
"v_exp_f32 v43, v43\n"
"v_cvt_pk_bf16_f32 v100, v40, v41\n"
"v_cvt_pk_bf16_f32 v101, v42, v43\n"
"v_mfma_f32_32x32x16_bf16 v[64:79], a[216:219], v[96:99], v[64:79]\n"
"buffer_load_dwordx4 v10, s[88:91], s36 offen offset:3072 lds\n"
"v_exp_f32 v44, v44\n"
"v_exp_f32 v45, v45\n"
"v_exp_f32 v46, v46\n"
"v_exp_f32 v47, v47\n"
"v_cvt_pk_bf16_f32 v102, v44, v45\n"
"v_cvt_pk_bf16_f32 v103, v46, v47\n"
"v_mfma_f32_32x32x16_bf16 v[32:47], a[112:115], v[110:113], 0\n"
"s_add_u32 s36, s36, s37\n"
"v_exp_f32 v48, v48\n"
"v_exp_f32 v49, v49\n"
"v_exp_f32 v50, v50\n"
"v_exp_f32 v51, v51\n"
"v_cvt_pk_bf16_f32 v96, v48, v49\n"
"v_cvt_pk_bf16_f32 v97, v50, v51\n"
"v_mfma_f32_32x32x16_bf16 v[64:79], a[220:223], v[100:103], v[64:79]\n"
"s_add_u32 s36, s36, 4096\n"
"v_exp_f32 v52, v52\n"
"v_exp_f32 v53, v53\n"
"v_exp_f32 v54, v54\n"
"v_exp_f32 v55, v55\n"
"v_cvt_pk_bf16_f32 v98, v52, v53\n"
"v_cvt_pk_bf16_f32 v99, v54, v55\n"
"v_mfma_f32_32x32x8_bf16 v[32:47], a[120:121], v[114:115], v[32:47]\n"
"v_exp_f32 v56, v56\n"
"v_exp_f32 v57, v57\n"
"v_exp_f32 v58, v58\n"
"v_exp_f32 v59, v59\n"
"v_cvt_pk_bf16_f32 v100, v56, v57\n"
"v_cvt_pk_bf16_f32 v101, v58, v59\n"
"v_mfma_f32_32x32x16_bf16 v[80:95], a[104:107], v[96:99], v[80:95]\n"
"global_store_dwordx4 v0, v[116:119], s[28:29] offset:1024 sc1\n"
"v_exp_f32 v60, v60\n"
"v_exp_f32 v61, v61\n"
"v_exp_f32 v62, v62\n"
"v_exp_f32 v63, v63\n"
"v_cvt_pk_bf16_f32 v102, v60, v61\n"
"v_cvt_pk_bf16_f32 v103, v62, v63\n"
"v_mfma_f32_32x32x16_bf16 v[48:63], a[116:119], v[110:113], 0\n"
"ds_write_b128 v124, v[64:67] offset:0\n"
"v_exp_f32 v32, v32\n"
"v_exp_f32 v33, v33\n"
"v_exp_f32 v34, v34\n"
"v_exp_f32 v35, v35\n"
"v_cvt_pk_bf16_f32 v96, v32, v33\n"
"v_cvt_pk_bf16_f32 v97, v34, v35\n"
"v_mfma_f32_32x32x16_bf16 v[80:95], a[108:111], v[100:103], v[80:95]\n"
"ds_write_b128 v124, v[68:71] offset:1024\n"
"v_exp_f32 v36, v36\n"
"v_exp_f32 v37, v37\n"
"v_exp_f32 v38, v38\n"
"v_exp_f32 v39, v39\n"
"v_cvt_pk_bf16_f32 v98, v36, v37\n"
"v_cvt_pk_bf16_f32 v99, v38, v39\n"
"v_mfma_f32_32x32x8_bf16 v[48:63], a[122:123], v[114:115], v[48:63]\n"
"v_exp_f32 v40, v40\n"
"v_exp_f32 v41, v41\n"
"v_exp_f32 v42, v42\n"
"v_exp_f32 v43, v43\n"
"v_cvt_pk_bf16_f32 v100, v40, v41\n"
"v_cvt_pk_bf16_f32 v101, v42, v43\n"
"v_mfma_f32_32x32x16_bf16 v[80:95], a[124:127], v[96:99], v[80:95]\n"
"ds_write_b128 v124, v[72:75] offset:2048\n"
"v_exp_f32 v44, v44\n"
"v_exp_f32 v45, v45\n"
"v_exp_f32 v46, v46\n"
"v_exp_f32 v47, v47\n"
"v_cvt_pk_bf16_f32 v102, v44, v45\n"
"v_cvt_pk_bf16_f32 v103, v46, v47\n"
"v_mfma_f32_32x32x16_bf16 v[32:47], a[140:143], v[110:113], 0\n"
"s_add_u32 s63, s56, 1\n"
"v_exp_f32 v48, v48\n"
"v_exp_f32 v49, v49\n"
"v_exp_f32 v50, v50\n"
"v_exp_f32 v51, v51\n"
"v_cvt_pk_bf16_f32 v96, v48, v49\n"
"v_cvt_pk_bf16_f32 v97, v50, v51\n"
"v_mfma_f32_32x32x16_bf16 v[80:95], a[128:131], v[100:103], v[80:95]\n"
"global_store_dwordx4 v16, v[116:119], s[28:29] offset:2048 sc1\n"
"v_exp_f32 v52, v52\n"
"v_exp_f32 v53, v53\n"
"v_exp_f32 v54, v54\n"
"v_exp_f32 v55, v55\n"
"v_cvt_pk_bf16_f32 v98, v52, v53\n"
"v_cvt_pk_bf16_f32 v99, v54, v55\n"
"v_mfma_f32_32x32x8_bf16 v[32:47], a[148:149], v[114:115], v[32:47]\n"
"v_exp_f32 v56, v56\n"
"v_exp_f32 v57, v57\n"
"v_exp_f32 v58, v58\n"
"v_exp_f32 v59, v59\n"
"v_cvt_pk_bf16_f32 v100, v56, v57\n"
"v_cvt_pk_bf16_f32 v101, v58, v59\n"
"v_mfma_f32_32x32x16_bf16 v[80:95], a[132:135], v[96:99], v[80:95]\n"
"s_and_b32 s63, s63, 3\n"
"v_exp_f32 v60, v60\n"
"v_exp_f32 v61, v61\n"
"v_exp_f32 v62, v62\n"
"v_exp_f32 v63, v63\n"
"v_cvt_pk_bf16_f32 v102, v60, v61\n"
"v_cvt_pk_bf16_f32 v103, v62, v63\n"
"v_mfma_f32_32x32x16_bf16 v[48:63], a[144:147], v[110:113], 0\n"
"s_mul_i32 s63, s63, 3072\n"
"v_lshl_add_u32 v125, s63, 1, v28\n"
"v_add_u32 v126, s63, v26\n"
"v_add_u32 v127, s63, v27\n"
"v_exp_f32 v32, v32\n"
"v_exp_f32 v33, v33\n"
"v_exp_f32 v34, v34\n"
"v_exp_f32 v35, v35\n"
"v_cvt_pk_bf16_f32 v96, v32, v33\n"
"v_cvt_pk_bf16_f32 v97, v34, v35\n"
"v_mfma_f32_32x32x16_bf16 v[80:95], a[136:139], v[100:103], v[80:95]\n"
"s_waitcnt lgkmcnt(0)\n"
"v_exp_f32 v36, v36\n"
"v_exp_f32 v37, v37\n"
"v_exp_f32 v38, v38\n"
"v_exp_f32 v39, v39\n"
"v_cvt_pk_bf16_f32 v98, v36, v37\n"
"v_cvt_pk_bf16_f32 v99, v38, v39\n"
"v_mfma_f32_32x32x8_bf16 v[48:63], a[150:151], v[114:115], v[48:63]\n"
"v_exp_f32 v40, v40\n"
"v_exp_f32 v41, v41\n"
"v_exp_f32 v42, v42\n"
"v_exp_f32 v43, v43\n"
"v_cvt_pk_bf16_f32 v100, v40, v41\n"
"v_cvt_pk_bf16_f32 v101, v42, v43\n"
"v_mfma_f32_32x32x16_bf16 v[80:95], a[152:155], v[96:99], v[80:95]\n"
"s_add_u32 m0, m0, 4096\n"
"v_exp_f32 v44, v44\n"
"v_exp_f32 v45, v45\n"
"v_exp_f32 v46, v46\n"
"v_exp_f32 v47, v47\n"
"v_cvt_pk_bf16_f32 v102, v44, v45\n"
"v_cvt_pk_bf16_f32 v103, v46, v47\n"
"v_mfma_f32_32x32x16_bf16 v[32:47], a[168:171], v[110:113], 0\n"
"s_barrier\n"
"v_exp_f32 v48, v48\n"
"v_exp_f32 v49, v49\n"
"v_exp_f32 v50, v50\n"
"v_exp_f32 v51, v51\n"
"v_cvt_pk_bf16_f32 v96, v48, v49\n"
"v_cvt_pk_bf16_f32 v97, v50, v51\n"
"v_mfma_f32_32x32x16_bf16 v[80:95], a[156:159], v[100:103], v[80:95]\n"
"buffer_load_dwordx4 v10, s[88:91], s36 offen offset:0 lds\n"
"v_exp_f32 v52, v52\n"
"v_exp_f32 v53, v53\n"
"v_exp_f32 v54, v54\n"
"v_exp_f32 v55, v55\n"
"v_cvt_pk_bf16_f32 v98, v52, v53\n"
"v_cvt_pk_bf16_f32 v99, v54, v55\n"
"v_mfma_f32_32x32x8_bf16 v[32:47], a[176:177], v[114:115], v[32:47]\n"
"ds_read_b128 v[64:67], v125 offset:0\n"
"v_exp_f32 v56, v56\n"
"v_exp_f32 v57, v57\n"
"v_exp_f32 v58, v58\n"
"v_exp_f32 v59, v59\n"
"v_cvt_pk_bf16_f32 v100, v56, v57\n"
"v_cvt_pk_bf16_f32 v101, v58, v59\n"
"v_mfma_f32_32x32x16_bf16 v[80:95], a[160:163], v[96:99], v[80:95]\n"
"s_add_u32 s36, s36, s37\n"
"ds_read_b128 v[68:71], v125 offset:1024\n"
"v_exp_f32 v60, v60\n"
"v_exp_f32 v61, v61\n"
"v_exp_f32 v62, v62\n"
"v_exp_f32 v63, v63\n"
"v_cvt_pk_bf16_f32 v102, v60, v61\n"
"v_cvt_pk_bf16_f32 v103, v62, v63\n"
"v_mfma_f32_32x32x16_bf16 v[48:63], a[172:175], v[110:113], 0\n"
"s_mov_b32 s28, s46\n"
"ds_read_b128 v[72:75], v125 offset:2048\n"
"v_exp_f32 v32, v32\n"
"v_exp_f32 v33, v33\n"
"v_exp_f32 v34, v34\n"
"v_exp_f32 v35, v35\n"
"v_cvt_pk_bf16_f32 v96, v32, v33\n"
"v_cvt_pk_bf16_f32 v97, v34, v35\n"
"v_mfma_f32_32x32x16_bf16 v[80:95], a[164:167], v[100:103], v[80:95]\n"
"global_store_dwordx4 v17, v[116:119], s[28:29] offset:0 sc1\n"
"v_exp_f32 v36, v36\n"
"v_exp_f32 v37, v37\n"
"v_exp_f32 v38, v38\n"
"v_exp_f32 v39, v39\n"
"v_cvt_pk_bf16_f32 v98, v36, v37\n"
"v_cvt_pk_bf16_f32 v99, v38, v39\n"
"v_mfma_f32_32x32x8_bf16 v[48:63], a[178:179], v[114:115], v[48:63]\n"
"ds_read_b128 v[104:107], v126 offset:0\n"
"v_exp_f32 v40, v40\n"
"v_exp_f32 v41, v41\n"
"v_exp_f32 v42, v42\n"
"v_exp_f32 v43, v43\n"
"v_cvt_pk_bf16_f32 v100, v40, v41\n"
"v_cvt_pk_bf16_f32 v101, v42, v43\n"
"v_mfma_f32_32x32x16_bf16 v[80:95], a[180:183], v[96:99], v[80:95]\n"
"s_add_u32 s48, s48, s20\n"
"ds_read_b64 v[108:109], v127 offset:32\n"
"v_exp_f32 v44, v44\n"
"v_exp_f32 v45, v45\n"
"v_exp_f32 v46, v46\n"
"v_exp_f32 v47, v47\n"
"v_cvt_pk_bf16_f32 v102, v44, v45\n"
"v_cvt_pk_bf16_f32 v103, v46, v47\n"
"v_mfma_f32_32x32x16_bf16 v[32:47], a[196:199], v[110:113], 0\n"
"s_mul_i32 s87, s98, 41984\n"
"v_exp_f32 v48, v48\n"
"v_exp_f32 v49, v49\n"
"v_exp_f32 v50, v50\n"
"v_exp_f32 v51, v51\n"
"v_cvt_pk_bf16_f32 v96, v48, v49\n"
"v_cvt_pk_bf16_f32 v97, v50, v51\n"
"v_mfma_f32_32x32x16_bf16 v[80:95], a[184:187], v[100:103], v[80:95]\n"
"buffer_load_dwordx4 v10, s[88:91], s36 offen offset:1024 lds\n"
"v_exp_f32 v52, v52\n"
"v_exp_f32 v53, v53\n"
"v_exp_f32 v54, v54\n"
"v_exp_f32 v55, v55\n"
"v_cvt_pk_bf16_f32 v98, v52, v53\n"
"v_cvt_pk_bf16_f32 v99, v54, v55\n"
"v_mfma_f32_32x32x8_bf16 v[32:47], a[204:205], v[114:115], v[32:47]\n"
"v_exp_f32 v56, v56\n"
"v_exp_f32 v57, v57\n"
"v_exp_f32 v58, v58\n"
"v_exp_f32 v59, v59\n"
"v_cvt_pk_bf16_f32 v100, v56, v57\n"
"v_cvt_pk_bf16_f32 v101, v58, v59\n"
"v_mfma_f32_32x32x16_bf16 v[80:95], a[188:191], v[96:99], v[80:95]\n"
"global_store_dwordx4 v17, v[116:119], s[28:29] offset:128 sc1\n"
"v_exp_f32 v60, v60\n"
"v_exp_f32 v61, v61\n"
"v_exp_f32 v62, v62\n"
"v_exp_f32 v63, v63\n"
"v_cvt_pk_bf16_f32 v102, v60, v61\n"
"v_cvt_pk_bf16_f32 v103, v62, v63\n"
"v_mfma_f32_32x32x16_bf16 v[48:63], a[200:203], v[110:113], 0\n"
"s_waitcnt lgkmcnt(0)\n"
"v_exp_f32 v32, v32\n"
"v_exp_f32 v33, v33\n"
"v_exp_f32 v34, v34\n"
"v_exp_f32 v35, v35\n"
"v_cvt_pk_bf16_f32 v96, v32, v33\n"
"v_cvt_pk_bf16_f32 v97, v34, v35\n"
"v_mfma_f32_32x32x16_bf16 v[80:95], a[192:195], v[100:103], v[80:95]\n"
"s_add_u32 s36, s36, s37\n"
"v_exp_f32 v36, v36\n"
"v_exp_f32 v37, v37\n"
"v_exp_f32 v38, v38\n"
"v_exp_f32 v39, v39\n"
"v_cvt_pk_bf16_f32 v98, v36, v37\n"
"v_cvt_pk_bf16_f32 v99, v38, v39\n"
"v_mfma_f32_32x32x8_bf16 v[48:63], a[206:207], v[114:115], v[48:63]\n"
"v_exp_f32 v40, v40\n"
"v_exp_f32 v41, v41\n"
"v_exp_f32 v42, v42\n"
"v_exp_f32 v43, v43\n"
"v_cvt_pk_bf16_f32 v100, v40, v41\n"
"v_cvt_pk_bf16_f32 v101, v42, v43\n"
"v_mfma_f32_32x32x16_bf16 v[80:95], a[208:211], v[96:99], v[80:95]\n"
"s_add_u32 s28, s47, 0\n"
"v_exp_f32 v44, v44\n"
"v_exp_f32 v45, v45\n"
"v_exp_f32 v46, v46\n"
"v_exp_f32 v47, v47\n"
"v_cvt_pk_bf16_f32 v102, v44, v45\n"
"v_cvt_pk_bf16_f32 v103, v46, v47\n"
"v_mfma_f32_32x32x16_bf16 v[32:47], a[0:3], v[104:107], 0\n"
"s_add_u32 s87, s87, 14057472\n"
"v_exp_f32 v48, v48\n"
"v_exp_f32 v49, v49\n"
"v_exp_f32 v50, v50\n"
"v_exp_f32 v51, v51\n"
"v_cvt_pk_bf16_f32 v96, v48, v49\n"
"v_cvt_pk_bf16_f32 v97, v50, v51\n"
"v_mfma_f32_32x32x16_bf16 v[80:95], a[212:215], v[100:103], v[80:95]\n"
"global_store_dwordx4 v0, v[116:119], s[28:29] offset:0 sc1\n"
"v_exp_f32 v52, v52\n"
"v_exp_f32 v53, v53\n"
"v_exp_f32 v54, v54\n"
"v_exp_f32 v55, v55\n"
"v_cvt_pk_bf16_f32 v98, v52, v53\n"
"v_cvt_pk_bf16_f32 v99, v54, v55\n"
"v_mfma_f32_32x32x8_bf16 v[32:47], a[8:9], v[108:109], v[32:47]\n"
"v_exp_f32 v56, v56\n"
"v_exp_f32 v57, v57\n"
"v_exp_f32 v58, v58\n"
"v_exp_f32 v59, v59\n"
"v_cvt_pk_bf16_f32 v100, v56, v57\n"
"v_cvt_pk_bf16_f32 v101, v58, v59\n"
"v_mfma_f32_32x32x16_bf16 v[80:95], a[216:219], v[96:99], v[80:95]\n"
"buffer_load_dwordx4 v10, s[88:91], s36 offen offset:2048 lds\n"
"v_exp_f32 v60, v60\n"
"v_exp_f32 v61, v61\n"
"v_exp_f32 v62, v62\n"
"v_exp_f32 v63, v63\n"
"v_cvt_pk_bf16_f32 v102, v60, v61\n"
"v_cvt_pk_bf16_f32 v103, v62, v63\n"
"v_mfma_f32_32x32x16_bf16 v[48:63], a[4:7], v[104:107], 0\n"
"s_add_u32 s36, s36, s37\n"
"v_exp_f32 v32, v32\n"
"v_exp_f32 v33, v33\n"
"v_exp_f32 v34, v34\n"
"v_exp_f32 v35, v35\n"
"v_cvt_pk_bf16_f32 v96, v32, v33\n"
"v_cvt_pk_bf16_f32 v97, v34, v35\n"
"v_mfma_f32_32x32x16_bf16 v[80:95], a[220:223], v[100:103], v[80:95]\n"
"s_add_u32 s87, s87, s20\n"
"v_exp_f32 v36, v36\n"
"v_exp_f32 v37, v37\n"
"v_exp_f32 v38, v38\n"
"v_exp_f32 v39, v39\n"
"v_cvt_pk_bf16_f32 v98, v36, v37\n"
"v_cvt_pk_bf16_f32 v99, v38, v39\n"
"v_mfma_f32_32x32x8_bf16 v[48:63], a[10:11], v[108:109], v[48:63]\n"
"v_exp_f32 v40, v40\n"
"v_exp_f32 v41, v41\n"
"v_exp_f32 v42, v42\n"
"v_exp_f32 v43, v43\n"
"v_cvt_pk_bf16_f32 v100, v40, v41\n"
"v_cvt_pk_bf16_f32 v101, v42, v43\n"
"v_mfma_f32_32x32x16_bf16 v[64:79], a[12:15], v[96:99], v[64:79]\n"
"global_store_dwordx4 v0, v[116:119], s[28:29] offset:1024 sc1\n"
"v_exp_f32 v44, v44\n"
"v_exp_f32 v45, v45\n"
"v_exp_f32 v46, v46\n"
"v_exp_f32 v47, v47\n"
"v_cvt_pk_bf16_f32 v102, v44, v45\n"
"v_cvt_pk_bf16_f32 v103, v46, v47\n"
"v_mfma_f32_32x32x16_bf16 v[32:47], a[28:31], v[104:107], 0\n"
"ds_write_b128 v124, v[80:83] offset:3072\n"
"v_exp_f32 v48, v48\n"
"v_exp_f32 v49, v49\n"
"v_exp_f32 v50, v50\n"
"v_exp_f32 v51, v51\n"
"v_cvt_pk_bf16_f32 v96, v48, v49\n"
"v_cvt_pk_bf16_f32 v97, v50, v51\n"
"v_mfma_f32_32x32x16_bf16 v[64:79], a[16:19], v[100:103], v[64:79]\n"
"ds_write_b128 v124, v[84:87] offset:4096\n"
"v_exp_f32 v52, v52\n"
"v_exp_f32 v53, v53\n"
"v_exp_f32 v54, v54\n"
"v_exp_f32 v55, v55\n"
"v_cvt_pk_bf16_f32 v98, v52, v53\n"
"v_cvt_pk_bf16_f32 v99, v54, v55\n"
"v_mfma_f32_32x32x8_bf16 v[32:47], a[36:37], v[108:109], v[32:47]\n"
"v_exp_f32 v56, v56\n"
"v_exp_f32 v57, v57\n"
"v_exp_f32 v58, v58\n"
"v_exp_f32 v59, v59\n"
"v_cvt_pk_bf16_f32 v100, v56, v57\n"
"v_cvt_pk_bf16_f32 v101, v58, v59\n"
"v_mfma_f32_32x32x16_bf16 v[64:79], a[20:23], v[96:99], v[64:79]\n"
"ds_write_b128 v124, v[88:91] offset:5120\n"
"v_exp_f32 v60, v60\n"
"v_exp_f32 v61, v61\n"
"v_exp_f32 v62, v62\n"
"v_exp_f32 v63, v63\n"
"v_cvt_pk_bf16_f32 v102, v60, v61\n"
"v_cvt_pk_bf16_f32 v103, v62, v63\n"
"v_mfma_f32_32x32x16_bf16 v[48:63], a[32:35], v[104:107], 0\n"
"buffer_load_dwordx4 v10, s[88:91], s36 offen offset:3072 lds\n"
"v_exp_f32 v32, v32\n"
"v_exp_f32 v33, v33\n"
"v_exp_f32 v34, v34\n"
"v_exp_f32 v35, v35\n"
"v_cvt_pk_bf16_f32 v96, v32, v33\n"
"v_cvt_pk_bf16_f32 v97, v34, v35\n"
"v_mfma_f32_32x32x16_bf16 v[64:79], a[24:27], v[100:103], v[64:79]\n"
"s_add_u32 s36, s36, s37\n"
"v_exp_f32 v36, v36\n"
"v_exp_f32 v37, v37\n"
"v_exp_f32 v38, v38\n"
"v_exp_f32 v39, v39\n"
"v_cvt_pk_bf16_f32 v98, v36, v37\n"
"v_cvt_pk_bf16_f32 v99, v38, v39\n"
"v_mfma_f32_32x32x8_bf16 v[48:63], a[38:39], v[108:109], v[48:63]\n"
"v_exp_f32 v40, v40\n"
"v_exp_f32 v41, v41\n"
"v_exp_f32 v42, v42\n"
"v_exp_f32 v43, v43\n"
"v_cvt_pk_bf16_f32 v100, v40, v41\n"
"v_cvt_pk_bf16_f32 v101, v42, v43\n"
"v_mfma_f32_32x32x16_bf16 v[64:79], a[40:43], v[96:99], v[64:79]\n"
"global_store_dwordx4 v0, v[116:119], s[28:29] offset:2048 sc1\n"
"v_exp_f32 v44, v44\n"
"v_exp_f32 v45, v45\n"
"v_exp_f32 v46, v46\n"
"v_exp_f32 v47, v47\n"
"v_cvt_pk_bf16_f32 v102, v44, v45\n"
"v_cvt_pk_bf16_f32 v103, v46, v47\n"
"v_mfma_f32_32x32x16_bf16 v[32:47], a[56:59], v[104:107], 0\n"
"s_add_u32 s36, s36, 4096\n"
"v_exp_f32 v48, v48\n"
"v_exp_f32 v49, v49\n"
"v_exp_f32 v50, v50\n"
"v_exp_f32 v51, v51\n"
"v_cvt_pk_bf16_f32 v96, v48, v49\n"
"v_cvt_pk_bf16_f32 v97, v50, v51\n"
"v_mfma_f32_32x32x16_bf16 v[64:79], a[44:47], v[100:103], v[64:79]\n"
"s_add_u32 m0, m0, 4096\n"
"v_exp_f32 v52, v52\n"
"v_exp_f32 v53, v53\n"
"v_exp_f32 v54, v54\n"
"v_exp_f32 v55, v55\n"
"v_cvt_pk_bf16_f32 v98, v52, v53\n"
"v_cvt_pk_bf16_f32 v99, v54, v55\n"
"v_mfma_f32_32x32x8_bf16 v[32:47], a[64:65], v[108:109], v[32:47]\n"
"v_exp_f32 v56, v56\n"
"v_exp_f32 v57, v57\n"
"v_exp_f32 v58, v58\n"
"v_exp_f32 v59, v59\n"
"v_cvt_pk_bf16_f32 v100, v56, v57\n"
"v_cvt_pk_bf16_f32 v101, v58, v59\n"
"v_mfma_f32_32x32x16_bf16 v[64:79], a[48:51], v[96:99], v[64:79]\n"
"s_add_u32 s36, s36, s19\n"
"v_exp_f32 v60, v60\n"
"v_exp_f32 v61, v61\n"
"v_exp_f32 v62, v62\n"
"v_exp_f32 v63, v63\n"
"v_cvt_pk_bf16_f32 v102, v60, v61\n"
"v_cvt_pk_bf16_f32 v103, v62, v63\n"
"v_mfma_f32_32x32x16_bf16 v[48:63], a[60:63], v[104:107], 0\n"
"buffer_load_dwordx4 v10, s[88:91], s36 offen offset:0 lds\n"
"v_exp_f32 v32, v32\n"
"v_exp_f32 v33, v33\n"
"v_exp_f32 v34, v34\n"
"v_exp_f32 v35, v35\n"
"v_cvt_pk_bf16_f32 v96, v32, v33\n"
"v_cvt_pk_bf16_f32 v97, v34, v35\n"
"v_mfma_f32_32x32x16_bf16 v[64:79], a[52:55], v[100:103], v[64:79]\n"
"s_add_u32 s36, s36, s37\n"
"v_exp_f32 v36, v36\n"
"v_exp_f32 v37, v37\n"
"v_exp_f32 v38, v38\n"
"v_exp_f32 v39, v39\n"
"v_cvt_pk_bf16_f32 v98, v36, v37\n"
"v_cvt_pk_bf16_f32 v99, v38, v39\n"
"v_mfma_f32_32x32x8_bf16 v[48:63], a[66:67], v[108:109], v[48:63]\n"
"v_exp_f32 v40, v40\n"
"v_exp_f32 v41, v41\n"
"v_exp_f32 v42, v42\n"
"v_exp_f32 v43, v43\n"
"v_cvt_pk_bf16_f32 v100, v40, v41\n"
"v_cvt_pk_bf16_f32 v101, v42, v43\n"
"v_mfma_f32_32x32x16_bf16 v[64:79], a[68:71], v[96:99], v[64:79]\n"
"s_add_u32 s11, s28, s99\n"
"v_exp_f32 v44, v44\n"
"v_exp_f32 v45, v45\n"
"v_exp_f32 v46, v46\n"
"v_exp_f32 v47, v47\n"
"v_cvt_pk_bf16_f32 v102, v44, v45\n"
"v_cvt_pk_bf16_f32 v103, v46, v47\n"
"v_mfma_f32_32x32x16_bf16 v[32:47], a[84:87], v[104:107], 0\n"
"buffer_store_dwordx4 v[116:119], v16, s[20:23], s11 offen offset:3072 sc1\n"
"v_exp_f32 v48, v48\n"
"v_exp_f32 v49, v49\n"
"v_exp_f32 v50, v50\n"
"v_exp_f32 v51, v51\n"
"v_cvt_pk_bf16_f32 v96, v48, v49\n"
"v_cvt_pk_bf16_f32 v97, v50, v51\n"
"v_mfma_f32_32x32x16_bf16 v[64:79], a[72:75], v[100:103], v[64:79]\n"
"s_waitcnt lgkmcnt(0)\n"
"v_exp_f32 v52, v52\n"
"v_exp_f32 v53, v53\n"
"v_exp_f32 v54, v54\n"
"v_exp_f32 v55, v55\n"
"v_cvt_pk_bf16_f32 v98, v52, v53\n"
"v_cvt_pk_bf16_f32 v99, v54, v55\n"
"v_mfma_f32_32x32x8_bf16 v[32:47], a[92:93], v[108:109], v[32:47]\n"
"v_exp_f32 v56, v56\n"
"v_exp_f32 v57, v57\n"
"v_exp_f32 v58, v58\n"
"v_exp_f32 v59, v59\n"
"v_cvt_pk_bf16_f32 v100, v56, v57\n"
"v_cvt_pk_bf16_f32 v101, v58, v59\n"
"v_mfma_f32_32x32x16_bf16 v[64:79], a[76:79], v[96:99], v[64:79]\n"
"s_add_u32 s28, s47, 55296\n"
"v_exp_f32 v60, v60\n"
"v_exp_f32 v61, v61\n"
"v_exp_f32 v62, v62\n"
"v_exp_f32 v63, v63\n"
"v_cvt_pk_bf16_f32 v102, v60, v61\n"
"v_cvt_pk_bf16_f32 v103, v62, v63\n"
"v_mfma_f32_32x32x16_bf16 v[48:63], a[88:91], v[104:107], 0\n"
"s_barrier\n"
"v_exp_f32 v32, v32\n"
"v_exp_f32 v33, v33\n"
"v_exp_f32 v34, v34\n"
"v_exp_f32 v35, v35\n"
"v_cvt_pk_bf16_f32 v96, v32, v33\n"
"v_cvt_pk_bf16_f32 v97, v34, v35\n"
"v_mfma_f32_32x32x16_bf16 v[64:79], a[80:83], v[100:103], v[64:79]\n"
"buffer_load_dwordx4 v10, s[88:91], s36 offen offset:1024 lds\n"
"v_exp_f32 v36, v36\n"
"v_exp_f32 v37, v37\n"
"v_exp_f32 v38, v38\n"
"v_exp_f32 v39, v39\n"
"v_cvt_pk_bf16_f32 v98, v36, v37\n"
"v_cvt_pk_bf16_f32 v99, v38, v39\n"
"v_mfma_f32_32x32x8_bf16 v[48:63], a[94:95], v[108:109], v[48:63]\n"
"ds_read_b128 v[80:83], v125 offset:3072\n"
"v_exp_f32 v40, v40\n"
"v_exp_f32 v41, v41\n"
"v_exp_f32 v42, v42\n"
"v_exp_f32 v43, v43\n"
"v_cvt_pk_bf16_f32 v100, v40, v41\n"
"v_cvt_pk_bf16_f32 v101, v42, v43\n"
"v_mfma_f32_32x32x16_bf16 v[64:79], a[96:99], v[96:99], v[64:79]\n"
"global_store_dwordx4 v0, v[116:119], s[28:29] offset:0 sc1\n"
"v_exp_f32 v44, v44\n"
"v_exp_f32 v45, v45\n"
"v_exp_f32 v46, v46\n"
"v_exp_f32 v47, v47\n"
"v_cvt_pk_bf16_f32 v102, v44, v45\n"
"v_cvt_pk_bf16_f32 v103, v46, v47\n"
"v_mfma_f32_32x32x16_bf16 v[32:47], a[112:115], v[104:107], 0\n"
"s_lshr_b32 s6, s56, 1\n"
"ds_read_b128 v[84:87], v125 offset:4096\n"
"v_exp_f32 v48, v48\n"
"v_exp_f32 v49, v49\n"
"v_exp_f32 v50, v50\n"
"v_exp_f32 v51, v51\n"
"v_cvt_pk_bf16_f32 v96, v48, v49\n"
"v_cvt_pk_bf16_f32 v97, v50, v51\n"
"v_mfma_f32_32x32x16_bf16 v[64:79], a[100:103], v[100:103], v[64:79]\n"
"s_mul_i32 s6, s6, 10240\n"
"ds_read_b128 v[88:91], v125 offset:5120\n"
"v_exp_f32 v52, v52\n"
"v_exp_f32 v53, v53\n"
"v_exp_f32 v54, v54\n"
"v_exp_f32 v55, v55\n"
"v_cvt_pk_bf16_f32 v98, v52, v53\n"
"v_cvt_pk_bf16_f32 v99, v54, v55\n"
"v_mfma_f32_32x32x8_bf16 v[32:47], a[120:121], v[108:109], v[32:47]\n"
"ds_read_b128 v[110:113], v126 offset:1536\n"
"v_exp_f32 v56, v56\n"
"v_exp_f32 v57, v57\n"
"v_exp_f32 v58, v58\n"
"v_exp_f32 v59, v59\n"
"v_cvt_pk_bf16_f32 v100, v56, v57\n"
"v_cvt_pk_bf16_f32 v101, v58, v59\n"
"v_mfma_f32_32x32x16_bf16 v[64:79], a[104:107], v[96:99], v[64:79]\n"
"s_add_u32 s6, s6, s86\n"
"ds_read_b64 v[114:115], v127 offset:1568\n"
"v_exp_f32 v60, v60\n"
"v_exp_f32 v61, v61\n"
"v_exp_f32 v62, v62\n"
"v_exp_f32 v63, v63\n"
"v_cvt_pk_bf16_f32 v102, v60, v61\n"
"v_cvt_pk_bf16_f32 v103, v62, v63\n"
".Lhop2:\n"
"s_cbranch_execz .Lhop6\n"
"v_mfma_f32_32x32x16_bf16 v[48:63], a[116:119], v[104:107], 0\n"
"s_add_u32 s60, s87, s6\n"
"v_exp_f32 v32, v32\n"
"v_exp_f32 v33, v33\n"
"v_exp_f32 v34, v34\n"
"v_exp_f32 v35, v35\n"
"v_cvt_pk_bf16_f32 v96, v32, v33\n"
"v_cvt_pk_bf16_f32 v97, v34, v35\n"
"v_mfma_f32_32x32x16_bf16 v[64:79], a[108:111], v[100:103], v[64:79]\n"
"global_store_dwordx4 v0, v[116:119], s[28:29] offset:1024 sc1\n"
"v_exp_f32 v36, v36\n"
"v_exp_f32 v37, v37\n"
"v_exp_f32 v38, v38\n"
"v_exp_f32 v39, v39\n"
"v_cvt_pk_bf16_f32 v98, v36, v37\n"
"v_cvt_pk_bf16_f32 v99, v38, v39\n"
"v_mfma_f32_32x32x8_bf16 v[48:63], a[122:123], v[108:109], v[48:63]\n"
"v_exp_f32 v40, v40\n"
"v_exp_f32 v41, v41\n"
"v_exp_f32 v42, v42\n"
"v_exp_f32 v43, v43\n"
"v_cvt_pk_bf16_f32 v100, v40, v41\n"
"v_cvt_pk_bf16_f32 v101, v42, v43\n"
"v_mfma_f32_32x32x16_bf16 v[64:79], a[124:127], v[96:99], v[64:79]\n"
"s_add_u32 s18, s60, 10240\n"
"v_exp_f32 v44, v44\n"
"v_exp_f32 v45, v45\n"
"v_exp_f32 v46, v46\n"
"v_exp_f32 v47, v47\n"
"v_cvt_pk_bf16_f32 v102, v44, v45\n"
"v_cvt_pk_bf16_f32 v103, v46, v47\n"
"v_mfma_f32_32x32x16_bf16 v[32:47], a[140:143], v[104:107], 0\n"
"s_lshr_b32 s15, s56, 1\n"
"v_exp_f32 v48, v48\n"
"v_exp_f32 v49, v49\n"
"v_exp_f32 v50, v50\n"
"v_exp_f32 v51, v51\n"
"v_cvt_pk_bf16_f32 v96, v48, v49\n"
"v_cvt_pk_bf16_f32 v97, v50, v51\n"
"v_mfma_f32_32x32x16_bf16 v[64:79], a[128:131], v[100:103], v[64:79]\n"
"s_lshl_b32 s16, s15, 30\n"
"v_exp_f32 v52, v52\n"
"v_exp_f32 v53, v53\n"
"v_exp_f32 v54, v54\n"
"v_exp_f32 v55, v55\n"
"v_cvt_pk_bf16_f32 v98, v52, v53\n"
"v_cvt_pk_bf16_f32 v99, v54, v55\n"
"v_mfma_f32_32x32x8_bf16 v[32:47], a[148:149], v[108:109], v[32:47]\n"
"v_exp_f32 v56, v56\n"
"v_exp_f32 v57, v57\n"
"v_exp_f32 v58, v58\n"
"v_exp_f32 v59, v59\n"
"v_cvt_pk_bf16_f32 v100, v56, v57\n"
"v_cvt_pk_bf16_f32 v101, v58, v59\n"
"v_mfma_f32_32x32x16_bf16 v[64:79], a[132:135], v[96:99], v[64:79]\n"
"global_store_dwordx4 v0, v[116:119], s[28:29] offset:2048 sc1\n"
"v_exp_f32 v60, v60\n"
"v_exp_f32 v61, v61\n"
"v_exp_f32 v62, v62\n"
"v_exp_f32 v63, v63\n"
"v_cvt_pk_bf16_f32 v102, v60, v61\n"
"v_cvt_pk_bf16_f32 v103, v62, v63\n"
"v_mfma_f32_32x32x16_bf16 v[48:63], a[144:147], v[104:107], 0\n"
"s_add_u32 s11, s28, s99\n"
"v_exp_f32 v32, v32\n"
"v_exp_f32 v33, v33\n"
"v_exp_f32 v34, v34\n"
"v_exp_f32 v35, v35\n"
"v_cvt_pk_bf16_f32 v96, v32, v33\n"
"v_cvt_pk_bf16_f32 v97, v34, v35\n"
"v_mfma_f32_32x32x16_bf16 v[64:79], a[136:139], v[100:103], v[64:79]\n"
"s_mov_b32 s17, 40960\n"
"v_exp_f32 v36, v36\n"
"v_exp_f32 v37, v37\n"
"v_exp_f32 v38, v38\n"
"v_exp_f32 v39, v39\n"
"v_cvt_pk_bf16_f32 v98, v36, v37\n"
"v_cvt_pk_bf16_f32 v99, v38, v39\n"
"v_mfma_f32_32x32x8_bf16 v[48:63], a[150:151], v[108:109], v[48:63]\n"
"v_exp_f32 v40, v40\n"
"v_exp_f32 v41, v41\n"
"v_exp_f32 v42, v42\n"
"v_exp_f32 v43, v43\n"
"v_cvt_pk_bf16_f32 v100, v40, v41\n"
"v_cvt_pk_bf16_f32 v101, v42, v43\n"
"v_mfma_f32_32x32x16_bf16 v[64:79], a[152:155], v[96:99], v[64:79]\n"
"s_add_u32 s10, s16, s17\n"
"v_exp_f32 v44, v44\n"
"v_exp_f32 v45, v45\n"
"v_exp_f32 v46, v46\n"
"v_exp_f32 v47, v47\n"
"v_cvt_pk_bf16_f32 v102, v44, v45\n"
"v_cvt_pk_bf16_f32 v103, v46, v47\n"
"v_mfma_f32_32x32x16_bf16 v[32:47], a[168:171], v[104:107], 0\n"
"s_mul_i32 s71, s58, 276480\n"
"v_exp_f32 v48, v48\n"
"v_exp_f32 v49, v49\n"
"v_exp_f32 v50, v50\n"
"v_exp_f32 v51, v51\n"
"v_cvt_pk_bf16_f32 v96, v48, v49\n"
"v_cvt_pk_bf16_f32 v97, v50, v51\n"
"v_mfma_f32_32x32x16_bf16 v[64:79], a[156:159], v[100:103], v[64:79]\n"
"buffer_store_dwordx4 v[116:119], v16, s[20:23], s11 offen offset:3072 sc1\n"
"v_exp_f32 v52, v52\n"
"v_exp_f32 v53, v53\n"
"v_exp_f32 v54, v54\n"
"v_exp_f32 v55, v55\n"
"v_cvt_pk_bf16_f32 v98, v52, v53\n"
"v_cvt_pk_bf16_f32 v99, v54, v55\n"
"v_mfma_f32_32x32x8_bf16 v[32:47], a[176:177], v[108:109], v[32:47]\n"
"v_exp_f32 v56, v56\n"
"v_exp_f32 v57, v57\n"
"v_exp_f32 v58, v58\n"
"v_exp_f32 v59, v59\n"
"v_cvt_pk_bf16_f32 v100, v56, v57\n"
"v_cvt_pk_bf16_f32 v101, v58, v59\n"
"v_mfma_f32_32x32x16_bf16 v[64:79], a[160:163], v[96:99], v[64:79]\n"
"s_add_u32 s71, s71, 18591744\n"
"v_exp_f32 v60, v60\n"
"v_exp_f32 v61, v61\n"
"v_exp_f32 v62, v62\n"
"v_exp_f32 v63, v63\n"
"v_cvt_pk_bf16_f32 v102, v60, v61\n"
"v_cvt_pk_bf16_f32 v103, v62, v63\n"
"v_mfma_f32_32x32x16_bf16 v[48:63], a[172:175], v[104:107], 0\n"
"s_waitcnt lgkmcnt(0)\n"
"v_exp_f32 v32, v32\n"
"v_exp_f32 v33, v33\n"
"v_exp_f32 v34, v34\n"
"v_exp_f32 v35, v35\n"
"v_cvt_pk_bf16_f32 v96, v32, v33\n"
"v_cvt_pk_bf16_f32 v97, v34, v35\n"
"v_mfma_f32_32x32x16_bf16 v[64:79], a[164:167], v[100:103], v[64:79]\n"
"s_add_u32 s71, s71, s20\n"
"v_exp_f32 v36, v36\n"
"v_exp_f32 v37, v37\n"
"v_exp_f32 v38, v38\n"
"v_exp_f32 v39, v39\n"
"v_cvt_pk_bf16_f32 v98, v36, v37\n"
"v_cvt_pk_bf16_f32 v99, v38, v39\n"
"v_mfma_f32_32x32x8_bf16 v[48:63], a[178:179], v[108:109], v[48:63]\n"
"v_exp_f32 v40, v40\n"
"v_exp_f32 v41, v41\n"
"v_exp_f32 v42, v42\n"
"v_exp_f32 v43, v43\n"
"v_cvt_pk_bf16_f32 v100, v40, v41\n"
"v_cvt_pk_bf16_f32 v101, v42, v43\n"
"v_mfma_f32_32x32x16_bf16 v[64:79], a[180:183], v[96:99], v[64:79]\n"
"s_mul_i32 s6, s56, 27\n"
"v_exp_f32 v44, v44\n"
"v_exp_f32 v45, v45\n"
"v_exp_f32 v46, v46\n"
"v_exp_f32 v47, v47\n"
"v_cvt_pk_bf16_f32 v102, v44, v45\n"
"v_cvt_pk_bf16_f32 v103, v46, v47\n"
"v_mfma_f32_32x32x16_bf16 v[32:47], a[196:199], v[104:107], 0\n"
"s_add_u32 s6, s6, 1\n"
"v_exp_f32 v48, v48\n"
"v_exp_f32 v49, v49\n"
"v_exp_f32 v50, v50\n"
"v_exp_f32 v51, v51\n"
"v_cvt_pk_bf16_f32 v96, v48, v49\n"
"v_cvt_pk_bf16_f32 v97, v50, v51\n"
"v_mfma_f32_32x32x16_bf16 v[64:79], a[184:187], v[100:103], v[64:79]\n"
"s_lshr_b32 s6, s6, 1\n"
"v_exp_f32 v52, v52\n"
"v_exp_f32 v53, v53\n"
"v_exp_f32 v54, v54\n"
"v_exp_f32 v55, v55\n"
"v_cvt_pk_bf16_f32 v98, v52, v53\n"
"v_cvt_pk_bf16_f32 v99, v54, v55\n"
"v_mfma_f32_32x32x8_bf16 v[32:47], a[204:205], v[108:109], v[32:47]\n"
"v_exp_f32 v56, v56\n"
"v_exp_f32 v57, v57\n"
"v_exp_f32 v58, v58\n"
"v_exp_f32 v59, v59\n"
"v_cvt_pk_bf16_f32 v100, v56, v57\n"
"v_cvt_pk_bf16_f32 v101, v58, v59\n"
"v_mfma_f32_32x32x16_bf16 v[64:79], a[188:191], v[96:99], v[64:79]\n"
"s_and_b32 s7, s56, 1\n"
"v_exp_f32 v60, v60\n"
"v_exp_f32 v61, v61\n"
"v_exp_f32 v62, v62\n"
"v_exp_f32 v63, v63\n"
"v_cvt_pk_bf16_f32 v102, v60, v61\n"
"v_cvt_pk_bf16_f32 v103, v62, v63\n"
"v_mfma_f32_32x32x16_bf16 v[48:63], a[200:203], v[104:107], 0\n"
"s_mul_i32 s12, s7, 0xbc32bc33\n"
"v_exp_f32 v32, v32\n"
"v_exp_f32 v33, v33\n"
"v_exp_f32 v34, v34\n"
"v_exp_f32 v35, v35\n"
"v_cvt_pk_bf16_f32 v96, v32, v33\n"
"v_cvt_pk_bf16_f32 v97, v34, v35\n"
"v_mfma_f32_32x32x16_bf16 v[64:79], a[192:195], v[100:103], v[64:79]\n"
"s_add_u32 s12, s12, 0xc581b470\n"
"v_exp_f32 v36, v36\n"
"v_exp_f32 v37, v37\n"
"v_exp_f32 v38, v38\n"
"v_exp_f32 v39, v39\n"
"v_cvt_pk_bf16_f32 v98, v36, v37\n"
"v_cvt_pk_bf16_f32 v99, v38, v39\n"
"v_mfma_f32_32x32x8_bf16 v[48:63], a[206:207], v[108:109], v[48:63]\n"
"v_exp_f32 v40, v40\n"
"v_exp_f32 v41, v41\n"
"v_exp_f32 v42, v42\n"
"v_exp_f32 v43, v43\n"
"v_cvt_pk_bf16_f32 v100, v40, v41\n"
"v_cvt_pk_bf16_f32 v101, v42, v43\n"
"v_mfma_f32_32x32x16_bf16 v[64:79], a[208:211], v[96:99], v[64:79]\n"
"s_mov_b32 s13, s12\n"
"v_exp_f32 v44, v44\n"
"v_exp_f32 v45, v45\n"
"v_exp_f32 v46, v46\n"
"v_exp_f32 v47, v47\n"
"v_cvt_pk_bf16_f32 v102, v44, v45\n"
"v_cvt_pk_bf16_f32 v103, v46, v47\n"
"v_mfma_f32_32x32x16_bf16 v[32:47], a[0:3], v[110:113], 0\n"
"s_lshr_b32 s14, s62, 3\n"
"v_exp_f32 v48, v48\n"
"v_exp_f32 v49, v49\n"
"v_exp_f32 v50, v50\n"
"v_exp_f32 v51, v51\n"
"v_cvt_pk_bf16_f32 v96, v48, v49\n"
"v_cvt_pk_bf16_f32 v97, v50, v51\n"
"v_mfma_f32_32x32x16_bf16 v[64:79], a[212:215], v[100:103], v[64:79]\n"
"s_lshr_b32 s15, s62, 5\n"
"v_exp_f32 v52, v52\n"
"v_exp_f32 v53, v53\n"
"v_exp_f32 v54, v54\n"
"v_exp_f32 v55, v55\n"
"v_cvt_pk_bf16_f32 v98, v52, v53\n"
"v_cvt_pk_bf16_f32 v99, v54, v55\n"
"v_mfma_f32_32x32x8_bf16 v[32:47], a[8:9], v[114:115], v[32:47]\n"
"v_exp_f32 v56, v56\n"
"v_exp_f32 v57, v57\n"
"v_exp_f32 v58, v58\n"
"v_exp_f32 v59, v59\n"
"v_cvt_pk_bf16_f32 v100, v56, v57\n"
"v_cvt_pk_bf16_f32 v101, v58, v59\n"
"v_mfma_f32_32x32x16_bf16 v[64:79], a[216:219], v[96:99], v[64:79]\n"
"s_add_u32 s14, s14, s15\n"
"v_exp_f32 v60, v60\n"
"v_exp_f32 v61, v61\n"
"v_exp_f32 v62, v62\n"
"v_exp_f32 v63, v63\n"
"v_cvt_pk_bf16_f32 v102, v60, v61\n"
"v_cvt_pk_bf16_f32 v103, v62, v63\n"
"v_mfma_f32_32x32x16_bf16 v[48:63], a[4:7], v[110:113], 0\n"
"s_and_b32 s14, s14, 0\n"
"v_exp_f32 v32, v32\n"
"v_exp_f32 v33, v33\n"
"v_exp_f32 v34, v34\n"
"v_exp_f32 v35, v35\n"
"v_cvt_pk_bf16_f32 v96, v32, v33\n"
"v_cvt_pk_bf16_f32 v97, v34, v35\n"
"v_mfma_f32_32x32x16_bf16 v[64:79], a[220:223], v[100:103], v[64:79]\n"
"s_lshl_b32 s14, s14, 3\n"
"v_exp_f32 v36, v36\n"
"v_exp_f32 v37, v37\n"
"v_exp_f32 v38, v38\n"
"v_exp_f32 v39, v39\n"
"v_cvt_pk_bf16_f32 v98, v36, v37\n"
"v_cvt_pk_bf16_f32 v99, v38, v39\n"
"v_mfma_f32_32x32x8_bf16 v[48:63], a[10:11], v[114:115], v[48:63]\n"
"v_exp_f32 v40, v40\n"
"v_exp_f32 v41, v41\n"
"v_exp_f32 v42, v42\n"
"v_exp_f32 v43, v43\n"
"v_cvt_pk_bf16_f32 v100, v40, v41\n"
"v_cvt_pk_bf16_f32 v101, v42, v43\n"
"v_mfma_f32_32x32x16_bf16 v[80:95], a[12:15], v[96:99], v[80:95]\n"
"s_lshr_b64 s[12:13], s[12:13], s14\n"
"v_exp_f32 v44, v44\n"
"v_exp_f32 v45, v45\n"
"v_exp_f32 v46, v46\n"
"v_exp_f32 v47, v47\n"
"v_cvt_pk_bf16_f32 v102, v44, v45\n"
"v_cvt_pk_bf16_f32 v103, v46, v47\n"
"v_mfma_f32_32x32x16_bf16 v[32:47], a[28:31], v[110:113], 0\n"
"ds_write_b128 v125, v[64:67] offset:0\n"
"v_exp_f32 v48, v48\n"
"v_exp_f32 v49, v49\n"
"v_exp_f32 v50, v50\n"
"v_exp_f32 v51, v51\n"
"v_cvt_pk_bf16_f32 v96, v48, v49\n"
"v_cvt_pk_bf16_f32 v97, v50, v51\n"
"v_mfma_f32_32x32x16_bf16 v[80:95], a[16:19], v[100:103], v[80:95]\n"
"ds_write_b128 v125, v[68:71] offset:1024\n"
"v_exp_f32 v52, v52\n"
"v_exp_f32 v53, v53\n"
"v_exp_f32 v54, v54\n"
"v_exp_f32 v55, v55\n"
"v_cvt_pk_bf16_f32 v98, v52, v53\n"
"v_cvt_pk_bf16_f32 v99, v54, v55\n"
"v_mfma_f32_32x32x8_bf16 v[32:47], a[36:37], v[114:115], v[32:47]\n"
"v_exp_f32 v56, v56\n"
"v_exp_f32 v57, v57\n"
"v_exp_f32 v58, v58\n"
"v_exp_f32 v59, v59\n"
"v_cvt_pk_bf16_f32 v100, v56, v57\n"
"v_cvt_pk_bf16_f32 v101, v58, v59\n"
"v_mfma_f32_32x32x16_bf16 v[80:95], a[20:23], v[96:99], v[80:95]\n"
"ds_write_b128 v125, v[72:75] offset:2048\n"
"v_exp_f32 v60, v60\n"
"v_exp_f32 v61, v61\n"
"v_exp_f32 v62, v62\n"
"v_exp_f32 v63, v63\n"
"v_cvt_pk_bf16_f32 v102, v60, v61\n"
"v_cvt_pk_bf16_f32 v103, v62, v63\n"
"v_mfma_f32_32x32x16_bf16 v[48:63], a[32:35], v[110:113], 0\n"
"s_add_u32 s63, s56, 2\n"
"v_exp_f32 v32, v32\n"
"v_exp_f32 v33, v33\n"
"v_exp_f32 v34, v34\n"
"v_exp_f32 v35, v35\n"
"v_cvt_pk_bf16_f32 v96, v32, v33\n"
"v_cvt_pk_bf16_f32 v97, v34, v35\n"
"v_mfma_f32_32x32x16_bf16 v[80:95], a[24:27], v[100:103], v[80:95]\n"
"s_and_b32 s63, s63, 3\n"
"v_exp_f32 v36, v36\n"
"v_exp_f32 v37, v37\n"
"v_exp_f32 v38, v38\n"
"v_exp_f32 v39, v39\n"
"v_cvt_pk_bf16_f32 v98, v36, v37\n"
"v_cvt_pk_bf16_f32 v99, v38, v39\n"
"v_mfma_f32_32x32x8_bf16 v[48:63], a[38:39], v[114:115], v[48:63]\n"
"v_exp_f32 v40, v40\n"
"v_exp_f32 v41, v41\n"
"v_exp_f32 v42, v42\n"
"v_exp_f32 v43, v43\n"
"v_cvt_pk_bf16_f32 v100, v40, v41\n"
"v_cvt_pk_bf16_f32 v101, v42, v43\n"
"v_mfma_f32_32x32x16_bf16 v[80:95], a[40:43], v[96:99], v[80:95]\n"
"s_mul_i32 s63, s63, 3072\n"
"v_lshl_add_u32 v124, s63, 1, v28\n"
"v_add_u32 v126, s63, v26\n"
"v_add_u32 v127, s63, v27\n"
"v_exp_f32 v44, v44\n"
"v_exp_f32 v45, v45\n"
"v_exp_f32 v46, v46\n"
"v_exp_f32 v47, v47\n"
"v_cvt_pk_bf16_f32 v102, v44, v45\n"
"v_cvt_pk_bf16_f32 v103, v46, v47\n"
"v_mfma_f32_32x32x16_bf16 v[32:47], a[56:59], v[110:113], 0\n"
"s_waitcnt vmcnt(4)\n"
"ds_read_b64_tr_b16 v[128:129], v13 offset:0\n"
"v_exp_f32 v48, v48\n"
"v_exp_f32 v49, v49\n"
"v_exp_f32 v50, v50\n"
"v_exp_f32 v51, v51\n"
"v_cvt_pk_bf16_f32 v96, v48, v49\n"
"v_cvt_pk_bf16_f32 v97, v50, v51\n"
"v_mfma_f32_32x32x16_bf16 v[80:95], a[44:47], v[100:103], v[80:95]\n"
"s_bfe_u32 s72, s12, 0x40000\n"
"ds_read_b64_tr_b16 v[130:131], v13 offset:512\n"
"v_exp_f32 v52, v52\n"
"v_exp_f32 v53, v53\n"
"v_exp_f32 v54, v54\n"
"v_exp_f32 v55, v55\n"
"v_cvt_pk_bf16_f32 v98, v52, v53\n"
"v_cvt_pk_bf16_f32 v99, v54, v55\n"
"v_mfma_f32_32x32x8_bf16 v[32:47], a[64:65], v[114:115], v[32:47]\n"
"ds_read_b64_tr_b16 v[132:133], v13 offset:64\n"
"v_exp_f32 v56, v56\n"
"v_exp_f32 v57, v57\n"
"v_exp_f32 v58, v58\n"
"v_exp_f32 v59, v59\n"
"v_cvt_pk_bf16_f32 v100, v56, v57\n"
"v_cvt_pk_bf16_f32 v101, v58, v59\n"
"v_mfma_f32_32x32x16_bf16 v[80:95], a[48:51], v[96:99], v[80:95]\n"
"s_add_u32 s72, s72, s6\n"
"ds_read_b64_tr_b16 v[134:135], v13 offset:576\n"
"v_exp_f32 v60, v60\n"
"v_exp_f32 v61, v61\n"
"v_exp_f32 v62, v62\n"
"v_exp_f32 v63, v63\n"
"v_cvt_pk_bf16_f32 v102, v60, v61\n"
"v_cvt_pk_bf16_f32 v103, v62, v63\n"
"v_mfma_f32_32x32x16_bf16 v[48:63], a[60:63], v[110:113], 0\n"
"s_lshl_b32 s72, s72, 10\n"
"ds_read_b64_tr_b16 v[136:137], v13 offset:2048\n"
"v_exp_f32 v32, v32\n"
"v_exp_f32 v33, v33\n"
"v_exp_f32 v34, v34\n"
"v_exp_f32 v35, v35\n"
"v_cvt_pk_bf16_f32 v96, v32, v33\n"
"v_cvt_pk_bf16_f32 v97, v34, v35\n"
"v_mfma_f32_32x32x16_bf16 v[80:95], a[52:55], v[100:103], v[80:95]\n"
"s_bfe_u32 s73, s12, 0x40004\n"
"ds_read_b64_tr_b16 v[138:139], v13 offset:2560\n"
"v_exp_f32 v36, v36\n"
"v_exp_f32 v37, v37\n"
"v_exp_f32 v38, v38\n"
"v_exp_f32 v39, v39\n"
"v_cvt_pk_bf16_f32 v98, v36, v37\n"
"v_cvt_pk_bf16_f32 v99, v38, v39\n"
"v_mfma_f32_32x32x8_bf16 v[48:63], a[66:67], v[114:115], v[48:63]\n"
"ds_read_b64_tr_b16 v[140:141], v13 offset:2112\n"
"v_exp_f32 v40, v40\n"
"v_exp_f32 v41, v41\n"
"v_exp_f32 v42, v42\n"
"v_exp_f32 v43, v43\n"
"v_cvt_pk_bf16_f32 v100, v40, v41\n"
"v_cvt_pk_bf16_f32 v101, v42, v43\n"
"v_mfma_f32_32x32x16_bf16 v[80:95], a[68:71], v[96:99], v[80:95]\n"
"s_add_u32 s73, s73, s6\n"
"ds_read_b64_tr_b16 v[142:143], v13 offset:2624\n"
"v_exp_f32 v44, v44\n"
"v_exp_f32 v45, v45\n"
"v_exp_f32 v46, v46\n"
"v_exp_f32 v47, v47\n"
"v_cvt_pk_bf16_f32 v102, v44, v45\n"
"v_cvt_pk_bf16_f32 v103, v46, v47\n"
"v_mfma_f32_32x32x16_bf16 v[32:47], a[84:87], v[110:113], 0\n"
"s_waitcnt lgkmcnt(8)\n"
"ds_read_b64_tr_b16 v[144:145], v13 offset:4096\n"
"v_exp_f32 v48, v48\n"
"v_exp_f32 v49, v49\n"
"v_exp_f32 v50, v50\n"
"v_exp_f32 v51, v51\n"
"v_cvt_pk_bf16_f32 v96, v48, v49\n"
"v_cvt_pk_bf16_f32 v97, v50, v51\n"
"v_mfma_f32_32x32x16_bf16 v[80:95], a[72:75], v[100:103], v[80:95]\n"
"s_lshl_b32 s73, s73, 10\n"
"ds_read_b64_tr_b16 v[146:147], v13 offset:4608\n"
"v_exp_f32 v52, v52\n"
"v_exp_f32 v53, v53\n"
"v_exp_f32 v54, v54\n"
"v_exp_f32 v55, v55\n"
"v_cvt_pk_bf16_f32 v98, v52, v53\n"
"v_cvt_pk_bf16_f32 v99, v54, v55\n"
"v_mfma_f32_32x32x8_bf16 v[32:47], a[92:93], v[114:115], v[32:47]\n"
"ds_read_b64_tr_b16 v[148:149], v13 offset:4160\n"
"v_exp_f32 v56, v56\n"
"v_exp_f32 v57, v57\n"
"v_exp_f32 v58, v58\n"
"v_exp_f32 v59, v59\n"
"v_cvt_pk_bf16_f32 v100, v56, v57\n"
"v_cvt_pk_bf16_f32 v101, v58, v59\n"
"v_mfma_f32_32x32x16_bf16 v[80:95], a[76:79], v[96:99], v[80:95]\n"
"s_bfe_u32 s74, s12, 0x40008\n"
"ds_read_b64_tr_b16 v[150:151], v13 offset:4672\n"
"v_exp_f32 v60, v60\n"
"v_exp_f32 v61, v61\n"
"v_exp_f32 v62, v62\n"
"v_exp_f32 v63, v63\n"
"v_cvt_pk_bf16_f32 v102, v60, v61\n"
"v_cvt_pk_bf16_f32 v103, v62, v63\n"
"v_mfma_f32_32x32x16_bf16 v[48:63], a[88:91], v[110:113], 0\n"
"s_barrier\n"
"v_exp_f32 v32, v32\n"
"v_exp_f32 v33, v33\n"
"v_exp_f32 v34, v34\n"
"v_exp_f32 v35, v35\n"
"v_cvt_pk_bf16_f32 v96, v32, v33\n"
"v_cvt_pk_bf16_f32 v97, v34, v35\n"
"v_mfma_f32_32x32x16_bf16 v[80:95], a[80:83], v[100:103], v[80:95]\n"
"s_add_u32 s74, s74, s6\n"
"ds_read_b128 v[64:67], v124 offset:0\n"
"ds_read_b64_tr_b16 v[152:153], v13 offset:6144\n"
"v_exp_f32 v36, v36\n"
"v_exp_f32 v37, v37\n"
"v_exp_f32 v38, v38\n"
"v_exp_f32 v39, v39\n"
"v_cvt_pk_bf16_f32 v98, v36, v37\n"
"v_cvt_pk_bf16_f32 v99, v38, v39\n"
"v_mfma_f32_32x32x8_bf16 v[48:63], a[94:95], v[114:115], v[48:63]\n"
"ds_read_b128 v[68:71], v124 offset:1024\n"
"ds_read_b64_tr_b16 v[154:155], v13 offset:6656\n"
"v_exp_f32 v40, v40\n"
"v_exp_f32 v41, v41\n"
"v_exp_f32 v42, v42\n"
"v_exp_f32 v43, v43\n"
"v_cvt_pk_bf16_f32 v100, v40, v41\n"
"v_cvt_pk_bf16_f32 v101, v42, v43\n"
"v_mfma_f32_32x32x16_bf16 v[80:95], a[96:99], v[96:99], v[80:95]\n"
"s_lshl_b32 s74, s74, 10\n"
"ds_read_b128 v[72:75], v124 offset:2048\n"
"ds_read_b64_tr_b16 v[156:157], v13 offset:6208\n"
"v_exp_f32 v44, v44\n"
"v_exp_f32 v45, v45\n"
"v_exp_f32 v46, v46\n"
"v_exp_f32 v47, v47\n"
"v_cvt_pk_bf16_f32 v102, v44, v45\n"
"v_cvt_pk_bf16_f32 v103, v46, v47\n"
"v_mfma_f32_32x32x16_bf16 v[32:47], a[112:115], v[110:113], 0\n"
"s_bfe_u32 s75, s12, 0x4000c\n"
"ds_read_b128 v[104:107], v126 offset:0\n"
"ds_read_b64_tr_b16 v[158:159], v13 offset:6720\n"
"v_exp_f32 v48, v48\n"
"v_exp_f32 v49, v49\n"
"v_exp_f32 v50, v50\n"
"v_exp_f32 v51, v51\n"
"v_cvt_pk_bf16_f32 v96, v48, v49\n"
"v_cvt_pk_bf16_f32 v97, v50, v51\n"
"v_mfma_f32_32x32x16_bf16 v[80:95], a[100:103], v[100:103], v[80:95]\n"
"s_add_u32 s75, s75, s6\n"
"ds_read_b64 v[108:109], v127 offset:32\n"
"ds_read_b64_tr_b16 v[160:161], v13 offset:8192\n"
"v_exp_f32 v52, v52\n"
"v_exp_f32 v53, v53\n"
"v_exp_f32 v54, v54\n"
"v_exp_f32 v55, v55\n"
"v_cvt_pk_bf16_f32 v98, v52, v53\n"
"v_cvt_pk_bf16_f32 v99, v54, v55\n"
"v_mfma_f32_32x32x8_bf16 v[32:47], a[120:121], v[114:115], v[32:47]\n"
"ds_read_b64_tr_b16 v[162:163], v13 offset:8704\n"
"v_exp_f32 v56, v56\n"
"v_exp_f32 v57, v57\n"
"v_exp_f32 v58, v58\n"
"v_exp_f32 v59, v59\n"
"v_cvt_pk_bf16_f32 v100, v56, v57\n"
"v_cvt_pk_bf16_f32 v101, v58, v59\n"
"v_mfma_f32_32x32x16_bf16 v[80:95], a[104:107], v[96:99], v[80:95]\n"
"s_lshl_b32 s75, s75, 10\n"
"ds_read_b64_tr_b16 v[164:165], v13 offset:8256\n"
"v_exp_f32 v60, v60\n"
"v_exp_f32 v61, v61\n"
"v_exp_f32 v62, v62\n"
"v_exp_f32 v63, v63\n"
"v_cvt_pk_bf16_f32 v102, v60, v61\n"
"v_cvt_pk_bf16_f32 v103, v62, v63\n"
"v_mfma_f32_32x32x16_bf16 v[48:63], a[116:119], v[110:113], 0\n"
"s_bfe_u32 s76, s12, 0x40010\n"
"ds_read_b64_tr_b16 v[166:167], v13 offset:8768\n"
"v_exp_f32 v32, v32\n"
"v_exp_f32 v33, v33\n"
"v_exp_f32 v34, v34\n"
"v_exp_f32 v35, v35\n"
"v_cvt_pk_bf16_f32 v96, v32, v33\n"
"v_cvt_pk_bf16_f32 v97, v34, v35\n"
"v_mfma_f32_32x32x16_bf16 v[80:95], a[108:111], v[100:103], v[80:95]\n"
"s_waitcnt lgkmcnt(0)\n"
"v_exp_f32 v36, v36\n"
"v_exp_f32 v37, v37\n"
"v_exp_f32 v38, v38\n"
"v_exp_f32 v39, v39\n"
"v_cvt_pk_bf16_f32 v98, v36, v37\n"
"v_cvt_pk_bf16_f32 v99, v38, v39\n"
"v_mfma_f32_32x32x8_bf16 v[48:63], a[122:123], v[114:115], v[48:63]\n"
"v_exp_f32 v40, v40\n"
"v_exp_f32 v41, v41\n"
"v_exp_f32 v42, v42\n"
"v_exp_f32 v43, v43\n"
"v_cvt_pk_bf16_f32 v100, v40, v41\n"
"v_cvt_pk_bf16_f32 v101, v42, v43\n"
"v_mfma_f32_32x32x16_bf16 v[80:95], a[124:127], v[96:99], v[80:95]\n"
"global_store_dwordx4 v11, v[128:131], s[38:39] offset:0 sc1\n"
"v_exp_f32 v44, v44\n"
"v_exp_f32 v45, v45\n"
"v_exp_f32 v46, v46\n"
"v_exp_f32 v47, v47\n"
"v_cvt_pk_bf16_f32 v102, v44, v45\n"
"v_cvt_pk_bf16_f32 v103, v46, v47\n"
"v_dot2c_f32_bf16 v14, v128, v29\n"
"v_dot2c_f32_bf16 v14, v129, v29\n"
"v_dot2c_f32_bf16 v14, v130, v29\n"
"v_dot2c_f32_bf16 v14, v131, v29\n"
"v_mfma_f32_32x32x16_bf16 v[32:47], a[140:143], v[110:113], 0\n"
"s_add_u32 s76, s76, s6\n"
"v_exp_f32 v48, v48\n"
"v_exp_f32 v49, v49\n"
"v_exp_f32 v50, v50\n"
"v_exp_f32 v51, v51\n"
"v_cvt_pk_bf16_f32 v96, v48, v49\n"
"v_cvt_pk_bf16_f32 v97, v50, v51\n"
"v_mfma_f32_32x32x16_bf16 v[80:95], a[128:131], v[100:103], v[80:95]\n"
"global_store_dwordx4 v11, v[132:135], s[40:41] offset:0 sc1\n"
"v_exp_f32 v52, v52\n"
"v_exp_f32 v53, v53\n"
"v_exp_f32 v54, v54\n"
"v_exp_f32 v55, v55\n"
"v_cvt_pk_bf16_f32 v98, v52, v53\n"
"v_cvt_pk_bf16_f32 v99, v54, v55\n"
"v_dot2c_f32_bf16 v15, v132, v29\n"
"v_dot2c_f32_bf16 v15, v133, v29\n"
"v_dot2c_f32_bf16 v15, v134, v29\n"
"v_dot2c_f32_bf16 v15, v135, v29\n"
"v_mfma_f32_32x32x8_bf16 v[32:47], a[148:149], v[114:115], v[32:47]\n"
"v_exp_f32 v56, v56\n"
"v_exp_f32 v57, v57\n"
"v_exp_f32 v58, v58\n"
"v_exp_f32 v59, v59\n"
"v_cvt_pk_bf16_f32 v100, v56, v57\n"
"v_cvt_pk_bf16_f32 v101, v58, v59\n"
"v_mfma_f32_32x32x16_bf16 v[80:95], a[132:135], v[96:99], v[80:95]\n"
"global_store_dwordx4 v11, v[136:139], s[38:39] offset:512 sc1\n"
"v_exp_f32 v60, v60\n"
"v_exp_f32 v61, v61\n"
"v_exp_f32 v62, v62\n"
"v_exp_f32 v63, v63\n"
"v_cvt_pk_bf16_f32 v102, v60, v61\n"
"v_cvt_pk_bf16_f32 v103, v62, v63\n"
"v_dot2c_f32_bf16 v14, v136, v29\n"
"v_dot2c_f32_bf16 v14, v137, v29\n"
"v_dot2c_f32_bf16 v14, v138, v29\n"
"v_dot2c_f32_bf16 v14, v139, v29\n"
"v_mfma_f32_32x32x16_bf16 v[48:63], a[144:147], v[110:113], 0\n"
"s_lshl_b32 s76, s76, 10\n"
"v_exp_f32 v32, v32\n"
"v_exp_f32 v33, v33\n"
"v_exp_f32 v34, v34\n"
"v_exp_f32 v35, v35\n"
"v_cvt_pk_bf16_f32 v96, v32, v33\n"
"v_cvt_pk_bf16_f32 v97, v34, v35\n"
"v_mfma_f32_32x32x16_bf16 v[80:95], a[136:139], v[100:103], v[80:95]\n"
"global_store_dwordx4 v11, v[140:143], s[40:41] offset:512 sc1\n"
"v_exp_f32 v36, v36\n"
"v_exp_f32 v37, v37\n"
"v_exp_f32 v38, v38\n"
"v_exp_f32 v39, v39\n"
"v_cvt_pk_bf16_f32 v98, v36, v37\n"
"v_cvt_pk_bf16_f32 v99, v38, v39\n"
"v_dot2c_f32_bf16 v15, v140, v29\n"
"v_dot2c_f32_bf16 v15, v141, v29\n"
"v_dot2c_f32_bf16 v15, v142, v29\n"
"v_dot2c_f32_bf16 v15, v143, v29\n"
"v_mfma_f32_32x32x8_bf16 v[48:63], a[150:151], v[114:115], v[48:63]\n"
"v_exp_f32 v40, v40\n"
"v_exp_f32 v41, v41\n"
"v_exp_f32 v42, v42\n"
"v_exp_f32 v43, v43\n"
"v_cvt_pk_bf16_f32 v100, v40, v41\n"
"v_cvt_pk_bf16_f32 v101, v42, v43\n"
"v_mfma_f32_32x32x16_bf16 v[80:95], a[152:155], v[96:99], v[80:95]\n"
"global_store_dwordx4 v11, v[144:147], s[38:39] offset:1024 sc1\n"
"v_exp_f32 v44, v44\n"
"v_exp_f32 v45, v45\n"
"v_exp_f32 v46, v46\n"
"v_exp_f32 v47, v47\n"
"v_cvt_pk_bf16_f32 v102, v44, v45\n"
"v_cvt_pk_bf16_f32 v103, v46, v47\n"
"v_dot2c_f32_bf16 v14, v144, v29\n"
"v_dot2c_f32_bf16 v14, v145, v29\n"
"v_dot2c_f32_bf16 v14, v146, v29\n"
"v_dot2c_f32_bf16 v14, v147, v29\n"
"v_mfma_f32_32x32x16_bf16 v[32:47], a[168:171], v[110:113], 0\n"
"s_bfe_u32 s77, s12, 0x40014\n"
"v_exp_f32 v48, v48\n"
"v_exp_f32 v49, v49\n"
"v_exp_f32 v50, v50\n"
"v_exp_f32 v51, v51\n"
"v_cvt_pk_bf16_f32 v96, v48, v49\n"
"v_cvt_pk_bf16_f32 v97, v50, v51\n"
"v_mfma_f32_32x32x16_bf16 v[80:95], a[156:159], v[100:103], v[80:95]\n"
"global_store_dwordx4 v11, v[148:151], s[40:41] offset:1024 sc1\n"
"v_exp_f32 v52, v52\n"
"v_exp_f32 v53, v53\n"
"v_exp_f32 v54, v54\n"
"v_exp_f32 v55, v55\n"
"v_cvt_pk_bf16_f32 v98, v52, v53\n"
"v_cvt_pk_bf16_f32 v99, v54, v55\n"
"v_dot2c_f32_bf16 v15, v148, v29\n"
"v_dot2c_f32_bf16 v15, v149, v29\n"
"v_dot2c_f32_bf16 v15, v150, v29\n"
"v_dot2c_f32_bf16 v15, v151, v29\n"
"v_mfma_f32_32x32x8_bf16 v[32:47], a[176:177], v[114:115], v[32:47]\n"
"v_exp_f32 v56, v56\n"
"v_exp_f32 v57, v57\n"
"v_exp_f32 v58, v58\n"
"v_exp_f32 v59, v59\n"
"v_cvt_pk_bf16_f32 v100, v56, v57\n"
"v_cvt_pk_bf16_f32 v101, v58, v59\n"
"v_mfma_f32_32x32x16_bf16 v[80:95], a[160:163], v[96:99], v[80:95]\n"
"global_store_dwordx4 v11, v[152:155], s[38:39] offset:1536 sc1\n"
"v_exp_f32 v60, v60\n"
"v_exp_f32 v61, v61\n"
"v_exp_f32 v62, v62\n"
"v_exp_f32 v63, v63\n"
"v_cvt_pk_bf16_f32 v102, v60, v61\n"
"v_cvt_pk_bf16_f32 v103, v62, v63\n"
"v_mfma_f32_32x32x16_bf16 v[48:63], a[172:175], v[110:113], 0\n"
"v_exp_f32 v32, v32\n"
"v_exp_f32 v33, v33\n"
"v_exp_f32 v34, v34\n"
"v_exp_f32 v35, v35\n"
"v_cvt_pk_bf16_f32 v96, v32, v33\n"
"v_cvt_pk_bf16_f32 v97, v34, v35\n"
"v_mfma_f32_32x32x16_bf16 v[80:95], a[164:167], v[100:103], v[80:95]\n"
"s_add_u32 s38, s38, s19\n"
"v_exp_f32 v36, v36\n"
"v_exp_f32 v37, v37\n"
"v_exp_f32 v38, v38\n"
"v_exp_f32 v39, v39\n"
"v_cvt_pk_bf16_f32 v98, v36, v37\n"
"v_cvt_pk_bf16_f32 v99, v38, v39\n"
"v_mfma_f32_32x32x8_bf16 v[48:63], a[178:179], v[114:115], v[48:63]\n"
"v_exp_f32 v40, v40\n"
"v_exp_f32 v41, v41\n"
"v_exp_f32 v42, v42\n"
"v_exp_f32 v43, v43\n"
"v_cvt_pk_bf16_f32 v100, v40, v41\n"
"v_cvt_pk_bf16_f32 v101, v42, v43\n"
"v_mfma_f32_32x32x16_bf16 v[80:95], a[180:183], v[96:99], v[80:95]\n"
"s_sub_u32 s38, s38, s20\n"
"v_exp_f32 v44, v44\n"
"v_exp_f32 v45, v45\n"
"v_exp_f32 v46, v46\n"
"v_exp_f32 v47, v47\n"
"v_cvt_pk_bf16_f32 v102, v44, v45\n"
"v_cvt_pk_bf16_f32 v103, v46, v47\n"
"v_dot2c_f32_bf16 v14, v152, v29\n"
"v_dot2c_f32_bf16 v14, v153, v29\n"
"v_dot2c_f32_bf16 v14, v154, v29\n"
"v_dot2c_f32_bf16 v14, v155, v29\n"
"v_mfma_f32_32x32x16_bf16 v[32:47], a[196:199], v[110:113], 0\n"
"global_store_dwordx4 v11, v[156:159], s[40:41] offset:1536 sc1\n"
"v_exp_f32 v48, v48\n"
"v_exp_f32 v49, v49\n"
"v_exp_f32 v50, v50\n"
"v_exp_f32 v51, v51\n"
"v_cvt_pk_bf16_f32 v96, v48, v49\n"
"v_cvt_pk_bf16_f32 v97, v50, v51\n"
"v_mfma_f32_32x32x16_bf16 v[80:95], a[184:187], v[100:103], v[80:95]\n"
".Lhop3:\n"
"s_cbranch_execz .Lhop7\n"
"s_add_u32 s40, s40, s19\n"
"v_exp_f32 v52, v52\n"
"v_exp_f32 v53, v53\n"
"v_exp_f32 v54, v54\n"
"v_exp_f32 v55, v55\n"
"v_cvt_pk_bf16_f32 v98, v52, v53\n"
"v_cvt_pk_bf16_f32 v99, v54, v55\n"
"v_mfma_f32_32x32x8_bf16 v[32:47], a[204:205], v[114:115], v[32:47]\n"
"v_exp_f32 v56, v56\n"
"v_exp_f32 v57, v57\n"
"v_exp_f32 v58, v58\n"
"v_exp_f32 v59, v59\n"
"v_cvt_pk_bf16_f32 v100, v56, v57\n"
"v_cvt_pk_bf16_f32 v101, v58, v59\n"
"v_mfma_f32_32x32x16_bf16 v[80:95], a[188:191], v[96:99], v[80:95]\n"
"s_sub_u32 s40, s40, s20\n"
"v_exp_f32 v60, v60\n"
"v_exp_f32 v61, v61\n"
"v_exp_f32 v62, v62\n"
"v_exp_f32 v63, v63\n"
"v_cvt_pk_bf16_f32 v102, v60, v61\n"
"v_cvt_pk_bf16_f32 v103, v62, v63\n"
"v_dot2c_f32_bf16 v15, v156, v29\n"
"v_dot2c_f32_bf16 v15, v157, v29\n"
"v_dot2c_f32_bf16 v15, v158, v29\n"
"v_dot2c_f32_bf16 v15, v159, v29\n"
"v_mfma_f32_32x32x16_bf16 v[48:63], a[200:203], v[110:113], 0\n"
"buffer_store_dwordx4 v[160:163], v11, s[20:23], s38 offen offset:2048 sc1\n"
"v_exp_f32 v32, v32\n"
"v_exp_f32 v33, v33\n"
"v_exp_f32 v34, v34\n"
"v_exp_f32 v35, v35\n"
"v_cvt_pk_bf16_f32 v96, v32, v33\n"
"v_cvt_pk_bf16_f32 v97, v34, v35\n"
"v_dot2c_f32_bf16 v14, v160, v29\n"
"v_dot2c_f32_bf16 v14, v161, v29\n"
"v_dot2c_f32_bf16 v14, v162, v29\n"
"v_dot2c_f32_bf16 v14, v163, v29\n"
"v_mfma_f32_32x32x16_bf16 v[80:95], a[192:195], v[100:103], v[80:95]\n"
"s_add_u32 s77, s77, s6\n"
"v_exp_f32 v36, v36\n"
"v_exp_f32 v37, v37\n"
"v_exp_f32 v38, v38\n"
"v_exp_f32 v39, v39\n"
"v_cvt_pk_bf16_f32 v98, v36, v37\n"
"v_cvt_pk_bf16_f32 v99, v38, v39\n"
"v_mfma_f32_32x32x8_bf16 v[48:63], a[206:207], v[114:115], v[48:63]\n"
"v_exp_f32 v40, v40\n"
"v_exp_f32 v41, v41\n"
"v_exp_f32 v42, v42\n"
"v_exp_f32 v43, v43\n"
"v_cvt_pk_bf16_f32 v100, v40, v41\n"
"v_cvt_pk_bf16_f32 v101, v42, v43\n"
"v_mfma_f32_32x32x16_bf16 v[80:95], a[208:211], v[96:99], v[80:95]\n"
"buffer_store_dwordx4 v[164:167], v11, s[20:23], s40 offen offset:2048 sc1\n"
"v_exp_f32 v44, v44\n"
"v_exp_f32 v45, v45\n"
"v_exp_f32 v46, v46\n"
"v_exp_f32 v47, v47\n"
"v_cvt_pk_bf16_f32 v102, v44, v45\n"
"v_cvt_pk_bf16_f32 v103, v46, v47\n"
"v_dot2c_f32_bf16 v15, v164, v29\n"
"v_dot2c_f32_bf16 v15, v165, v29\n"
"v_dot2c_f32_bf16 v15, v166, v29\n"
"v_dot2c_f32_bf16 v15, v167, v29\n"
"v_mfma_f32_32x32x16_bf16 v[32:47], a[0:3], v[104:107], 0\n"
"s_nop 0\n"
"s_nop 0\n"
"v_permlane16_swap_b32 v14, v15\n"
"v_add_f32 v14, v14, v15\n"
"v_cvt_pk_bf16_f32 v15, v14, v14\n"
"v_and_b32 v15, 0xffff0000, v15\n"
"v_exp_f32 v48, v48\n"
"v_exp_f32 v49, v49\n"
"v_exp_f32 v50, v50\n"
"v_exp_f32 v51, v51\n"
"v_cvt_pk_bf16_f32 v96, v48, v49\n"
"v_cvt_pk_bf16_f32 v97, v50, v51\n"
"v_mfma_f32_32x32x16_bf16 v[80:95], a[212:215], v[100:103], v[80:95]\n"
"s_lshl_b32 s77, s77, 10\n"
"v_sub_f32 v15, v14, v15\n"
"v_cvt_pk_bf16_f32 v14, v14, v15\n"
"v_exp_f32 v52, v52\n"
"v_exp_f32 v53, v53\n"
"v_exp_f32 v54, v54\n"
"v_exp_f32 v55, v55\n"
"v_cvt_pk_bf16_f32 v98, v52, v53\n"
"v_cvt_pk_bf16_f32 v99, v54, v55\n"
"v_mfma_f32_32x32x8_bf16 v[32:47], a[8:9], v[108:109], v[32:47]\n"
"v_exp_f32 v56, v56\n"
"v_exp_f32 v57, v57\n"
"v_exp_f32 v58, v58\n"
"v_exp_f32 v59, v59\n"
"v_cvt_pk_bf16_f32 v100, v56, v57\n"
"v_cvt_pk_bf16_f32 v101, v58, v59\n"
"v_mfma_f32_32x32x16_bf16 v[80:95], a[216:219], v[96:99], v[80:95]\n"
"buffer_store_dword v14, v12, s[20:23], s42 offen sc1\n"
"v_exp_f32 v60, v60\n"
"v_exp_f32 v61, v61\n"
"v_exp_f32 v62, v62\n"
"v_exp_f32 v63, v63\n"
"v_cvt_pk_bf16_f32 v102, v60, v61\n"
"v_cvt_pk_bf16_f32 v103, v62, v63\n"
"v_mfma_f32_32x32x16_bf16 v[48:63], a[4:7], v[104:107], 0\n"
"s_bfe_u32 s78, s12, 0x40018\n"
"v_exp_f32 v32, v32\n"
"v_exp_f32 v33, v33\n"
"v_exp_f32 v34, v34\n"
"v_exp_f32 v35, v35\n"
"v_cvt_pk_bf16_f32 v96, v32, v33\n"
"v_cvt_pk_bf16_f32 v97, v34, v35\n"
"v_mfma_f32_32x32x16_bf16 v[80:95], a[220:223], v[100:103], v[80:95]\n"
"s_add_u32 s78, s78, s6\n"
"v_exp_f32 v36, v36\n"
"v_exp_f32 v37, v37\n"
"v_exp_f32 v38, v38\n"
"v_exp_f32 v39, v39\n"
"v_cvt_pk_bf16_f32 v98, v36, v37\n"
"v_cvt_pk_bf16_f32 v99, v38, v39\n"
"v_mfma_f32_32x32x8_bf16 v[48:63], a[10:11], v[108:109], v[48:63]\n"
"v_exp_f32 v40, v40\n"
"v_exp_f32 v41, v41\n"
"v_exp_f32 v42, v42\n"
"v_exp_f32 v43, v43\n"
"v_cvt_pk_bf16_f32 v100, v40, v41\n"
"v_cvt_pk_bf16_f32 v101, v42, v43\n"
"v_mfma_f32_32x32x16_bf16 v[64:79], a[12:15], v[96:99], v[64:79]\n"
"s_lshl_b32 s78, s78, 10\n"
"v_exp_f32 v44, v44\n"
"v_exp_f32 v45, v45\n"
"v_exp_f32 v46, v46\n"
"v_exp_f32 v47, v47\n"
"v_cvt_pk_bf16_f32 v102, v44, v45\n"
"v_cvt_pk_bf16_f32 v103, v46, v47\n"
"v_mfma_f32_32x32x16_bf16 v[32:47], a[28:31], v[104:107], 0\n"
"ds_write_b128 v125, v[80:83] offset:3072\n"
"v_exp_f32 v48, v48\n"
"v_exp_f32 v49, v49\n"
"v_exp_f32 v50, v50\n"
"v_exp_f32 v51, v51\n"
"v_cvt_pk_bf16_f32 v96, v48, v49\n"
"v_cvt_pk_bf16_f32 v97, v50, v51\n"
"v_mfma_f32_32x32x16_bf16 v[64:79], a[16:19], v[100:103], v[64:79]\n"
"ds_write_b128 v125, v[84:87] offset:4096\n"
"v_exp_f32 v52, v52\n"
"v_exp_f32 v53, v53\n"
"v_exp_f32 v54, v54\n"
"v_exp_f32 v55, v55\n"
"v_cvt_pk_bf16_f32 v98, v52, v53\n"
"v_cvt_pk_bf16_f32 v99, v54, v55\n"
"v_mfma_f32_32x32x8_bf16 v[32:47], a[36:37], v[108:109], v[32:47]\n"
"v_exp_f32 v56, v56\n"
"v_exp_f32 v57, v57\n"
"v_exp_f32 v58, v58\n"
"v_exp_f32 v59, v59\n"
"v_cvt_pk_bf16_f32 v100, v56, v57\n"
"v_cvt_pk_bf16_f32 v101, v58, v59\n"
"v_mfma_f32_32x32x16_bf16 v[64:79], a[20:23], v[96:99], v[64:79]\n"
"ds_write_b128 v125, v[88:91] offset:5120\n"
"v_exp_f32 v60, v60\n"
"v_exp_f32 v61, v61\n"
"v_exp_f32 v62, v62\n"
"v_exp_f32 v63, v63\n"
"v_cvt_pk_bf16_f32 v102, v60, v61\n"
"v_cvt_pk_bf16_f32 v103, v62, v63\n"
"v_mfma_f32_32x32x16_bf16 v[48:63], a[32:35], v[104:107], 0\n"
"s_bfe_u32 s79, s12, 0x4001c\n"
"v_exp_f32 v32, v32\n"
"v_exp_f32 v33, v33\n"
"v_exp_f32 v34, v34\n"
"v_exp_f32 v35, v35\n"
"v_cvt_pk_bf16_f32 v96, v32, v33\n"
"v_cvt_pk_bf16_f32 v97, v34, v35\n"
"v_mfma_f32_32x32x16_bf16 v[64:79], a[24:27], v[100:103], v[64:79]\n"
"s_add_u32 s79, s79, s6\n"
"v_exp_f32 v36, v36\n"
"v_exp_f32 v37, v37\n"
"v_exp_f32 v38, v38\n"
"v_exp_f32 v39, v39\n"
"v_cvt_pk_bf16_f32 v98, v36, v37\n"
"v_cvt_pk_bf16_f32 v99, v38, v39\n"
"v_mfma_f32_32x32x8_bf16 v[48:63], a[38:39], v[108:109], v[48:63]\n"
"v_exp_f32 v40, v40\n"
"v_exp_f32 v41, v41\n"
"v_exp_f32 v42, v42\n"
"v_exp_f32 v43, v43\n"
"v_cvt_pk_bf16_f32 v100, v40, v41\n"
"v_cvt_pk_bf16_f32 v101, v42, v43\n"
"v_mfma_f32_32x32x16_bf16 v[64:79], a[40:43], v[96:99], v[64:79]\n"
"s_lshl_b32 s79, s79, 10\n"
"v_exp_f32 v44, v44\n"
"v_exp_f32 v45, v45\n"
"v_exp_f32 v46, v46\n"
"v_exp_f32 v47, v47\n"
"v_cvt_pk_bf16_f32 v102, v44, v45\n"
"v_cvt_pk_bf16_f32 v103, v46, v47\n"
"v_mfma_f32_32x32x16_bf16 v[32:47], a[56:59], v[104:107], 0\n"
"s_lshl_b32 s6, s6, 10\n"
"v_exp_f32 v48, v48\n"
"v_exp_f32 v49, v49\n"
"v_exp_f32 v50, v50\n"
"v_exp_f32 v51, v51\n"
"v_cvt_pk_bf16_f32 v96, v48, v49\n"
"v_cvt_pk_bf16_f32 v97, v50, v51\n"
"v_mfma_f32_32x32x16_bf16 v[64:79], a[44:47], v[100:103], v[64:79]\n"
"s_mul_i32 s80, s7, 3072\n"
"v_exp_f32 v52, v52\n"
"v_exp_f32 v53, v53\n"
"v_exp_f32 v54, v54\n"
"v_exp_f32 v55, v55\n"
"v_cvt_pk_bf16_f32 v98, v52, v53\n"
"v_cvt_pk_bf16_f32 v99, v54, v55\n"
"v_mfma_f32_32x32x8_bf16 v[32:47], a[64:65], v[108:109], v[32:47]\n"
"v_exp_f32 v56, v56\n"
"v_exp_f32 v57, v57\n"
"v_exp_f32 v58, v58\n"
"v_exp_f32 v59, v59\n"
"v_cvt_pk_bf16_f32 v100, v56, v57\n"
"v_cvt_pk_bf16_f32 v101, v58, v59\n"
"v_mfma_f32_32x32x16_bf16 v[64:79], a[48:51], v[96:99], v[64:79]\n"
"s_add_u32 s80, s80, s6\n"
"v_exp_f32 v60, v60\n"
"v_exp_f32 v61, v61\n"
"v_exp_f32 v62, v62\n"
"v_exp_f32 v63, v63\n"
"v_cvt_pk_bf16_f32 v102, v60, v61\n"
"v_cvt_pk_bf16_f32 v103, v62, v63\n"
"v_mfma_f32_32x32x16_bf16 v[48:63], a[60:63], v[104:107], 0\n"
"s_add_u32 s80, s80, 2048\n"
"v_exp_f32 v32, v32\n"
"v_exp_f32 v33, v33\n"
"v_exp_f32 v34, v34\n"
"v_exp_f32 v35, v35\n"
"v_cvt_pk_bf16_f32 v96, v32, v33\n"
"v_cvt_pk_bf16_f32 v97, v34, v35\n"
"v_mfma_f32_32x32x16_bf16 v[64:79], a[52:55], v[100:103], v[64:79]\n"
"s_mul_i32 s81, s7, 3072\n"
"v_exp_f32 v36, v36\n"
"v_exp_f32 v37, v37\n"
"v_exp_f32 v38, v38\n"
"v_exp_f32 v39, v39\n"
"v_cvt_pk_bf16_f32 v98, v36, v37\n"
"v_cvt_pk_bf16_f32 v99, v38, v39\n"
"v_mfma_f32_32x32x8_bf16 v[48:63], a[66:67], v[108:109], v[48:63]\n"
"v_exp_f32 v40, v40\n"
"v_exp_f32 v41, v41\n"
"v_exp_f32 v42, v42\n"
"v_exp_f32 v43, v43\n"
"v_cvt_pk_bf16_f32 v100, v40, v41\n"
"v_cvt_pk_bf16_f32 v101, v42, v43\n"
"v_mfma_f32_32x32x16_bf16 v[64:79], a[68:71], v[96:99], v[64:79]\n"
"s_add_u32 s81, s81, s6\n"
"v_exp_f32 v44, v44\n"
"v_exp_f32 v45, v45\n"
"v_exp_f32 v46, v46\n"
"v_exp_f32 v47, v47\n"
"v_cvt_pk_bf16_f32 v102, v44, v45\n"
"v_cvt_pk_bf16_f32 v103, v46, v47\n"
"v_mfma_f32_32x32x16_bf16 v[32:47], a[84:87], v[104:107], 0\n"
"s_waitcnt lgkmcnt(0)\n"
"v_exp_f32 v48, v48\n"
"v_exp_f32 v49, v49\n"
"v_exp_f32 v50, v50\n"
"v_exp_f32 v51, v51\n"
"v_cvt_pk_bf16_f32 v96, v48, v49\n"
"v_cvt_pk_bf16_f32 v97, v50, v51\n"
"v_mfma_f32_32x32x16_bf16 v[64:79], a[72:75], v[100:103], v[64:79]\n"
"s_add_u32 s81, s81, 9216\n"
"v_exp_f32 v52, v52\n"
"v_exp_f32 v53, v53\n"
"v_exp_f32 v54, v54\n"
"v_exp_f32 v55, v55\n"
"v_cvt_pk_bf16_f32 v98, v52, v53\n"
"v_cvt_pk_bf16_f32 v99, v54, v55\n"
"v_mfma_f32_32x32x8_bf16 v[32:47], a[92:93], v[108:109], v[32:47]\n"
"v_exp_f32 v56, v56\n"
"v_exp_f32 v57, v57\n"
"v_exp_f32 v58, v58\n"
"v_exp_f32 v59, v59\n"
"v_cvt_pk_bf16_f32 v100, v56, v57\n"
"v_cvt_pk_bf16_f32 v101, v58, v59\n"
"v_mfma_f32_32x32x16_bf16 v[64:79], a[76:79], v[96:99], v[64:79]\n"
"s_mul_i32 s82, s7, 1073741824\n"
"v_exp_f32 v60, v60\n"
"v_exp_f32 v61, v61\n"
"v_exp_f32 v62, v62\n"
"v_exp_f32 v63, v63\n"
"v_cvt_pk_bf16_f32 v102, v60, v61\n"
"v_cvt_pk_bf16_f32 v103, v62, v63\n"
"v_mfma_f32_32x32x16_bf16 v[48:63], a[88:91], v[104:107], 0\n"
"s_waitcnt vmcnt(0)\n"
"s_barrier\n"
"v_exp_f32 v32, v32\n"
"v_exp_f32 v33, v33\n"
"v_exp_f32 v34, v34\n"
"v_exp_f32 v35, v35\n"
"v_cvt_pk_bf16_f32 v96, v32, v33\n"
"v_cvt_pk_bf16_f32 v97, v34, v35\n"
"v_mfma_f32_32x32x16_bf16 v[64:79], a[80:83], v[100:103], v[64:79]\n"
"buffer_store_dword v30, v17, s[20:23], s49 offen sc1\n"
"v_exp_f32 v36, v36\n"
"v_exp_f32 v37, v37\n"
"v_exp_f32 v38, v38\n"
"v_exp_f32 v39, v39\n"
"v_cvt_pk_bf16_f32 v98, v36, v37\n"
"v_cvt_pk_bf16_f32 v99, v38, v39\n"
"v_mfma_f32_32x32x8_bf16 v[48:63], a[94:95], v[108:109], v[48:63]\n"
"s_add_u32 s82, s82, s6\n"
"ds_read_b128 v[80:83], v124 offset:3072\n"
"v_exp_f32 v40, v40\n"
"v_exp_f32 v41, v41\n"
"v_exp_f32 v42, v42\n"
"v_exp_f32 v43, v43\n"
"v_cvt_pk_bf16_f32 v100, v40, v41\n"
"v_cvt_pk_bf16_f32 v101, v42, v43\n"
"v_mfma_f32_32x32x16_bf16 v[64:79], a[96:99], v[96:99], v[64:79]\n"
"s_add_u32 s82, s82, 13312\n"
"ds_read_b128 v[84:87], v124 offset:4096\n"
"v_exp_f32 v44, v44\n"
"v_exp_f32 v45, v45\n"
"v_exp_f32 v46, v46\n"
"v_exp_f32 v47, v47\n"
"v_cvt_pk_bf16_f32 v102, v44, v45\n"
"v_cvt_pk_bf16_f32 v103, v46, v47\n"
"v_mfma_f32_32x32x16_bf16 v[32:47], a[112:115], v[104:107], 0\n"
"s_mul_i32 s83, s7, -1024\n"
"ds_read_b128 v[88:91], v124 offset:5120\n"
"v_exp_f32 v48, v48\n"
"v_exp_f32 v49, v49\n"
"v_exp_f32 v50, v50\n"
"v_exp_f32 v51, v51\n"
"v_cvt_pk_bf16_f32 v96, v48, v49\n"
"v_cvt_pk_bf16_f32 v97, v50, v51\n"
"v_mfma_f32_32x32x16_bf16 v[64:79], a[100:103], v[100:103], v[64:79]\n"
"s_add_u32 s83, s83, s6\n"
"ds_read_b128 v[110:113], v126 offset:1536\n"
"v_exp_f32 v52, v52\n"
"v_exp_f32 v53, v53\n"
"v_exp_f32 v54, v54\n"
"v_exp_f32 v55, v55\n"
"v_cvt_pk_bf16_f32 v98, v52, v53\n"
"v_cvt_pk_bf16_f32 v99, v54, v55\n"
"v_mfma_f32_32x32x8_bf16 v[32:47], a[120:121], v[108:109], v[32:47]\n"
"s_add_u32 s83, s83, 3072\n"
"ds_read_b64 v[114:115], v127 offset:1568\n"
"v_exp_f32 v56, v56\n"
"v_exp_f32 v57, v57\n"
"v_exp_f32 v58, v58\n"
"v_exp_f32 v59, v59\n"
"v_cvt_pk_bf16_f32 v100, v56, v57\n"
"v_cvt_pk_bf16_f32 v101, v58, v59\n"
"v_mfma_f32_32x32x16_bf16 v[64:79], a[104:107], v[96:99], v[64:79]\n"
"s_mul_i32 s84, s7, 0\n"
"v_exp_f32 v60, v60\n"
"v_exp_f32 v61, v61\n"
"v_exp_f32 v62, v62\n"
"v_exp_f32 v63, v63\n"
"v_cvt_pk_bf16_f32 v102, v60, v61\n"
"v_cvt_pk_bf16_f32 v103, v62, v63\n"
"v_mfma_f32_32x32x16_bf16 v[48:63], a[116:119], v[104:107], 0\n"
"s_add_u32 s84, s84, s6\n"
"v_exp_f32 v32, v32\n"
"v_exp_f32 v33, v33\n"
"v_exp_f32 v34, v34\n"
"v_exp_f32 v35, v35\n"
"v_cvt_pk_bf16_f32 v96, v32, v33\n"
"v_cvt_pk_bf16_f32 v97, v34, v35\n"
"v_mfma_f32_32x32x16_bf16 v[64:79], a[108:111], v[100:103], v[64:79]\n"
"s_add_u32 s84, s84, 6144\n"
"v_exp_f32 v36, v36\n"
"v_exp_f32 v37, v37\n"
"v_exp_f32 v38, v38\n"
"v_exp_f32 v39, v39\n"
"v_cvt_pk_bf16_f32 v98, v36, v37\n"
"v_cvt_pk_bf16_f32 v99, v38, v39\n"
"v_mfma_f32_32x32x8_bf16 v[48:63], a[122:123], v[108:109], v[48:63]\n"
"s_mul_i32 s85, s7, -1024\n"
"v_exp_f32 v40, v40\n"
"v_exp_f32 v41, v41\n"
"v_exp_f32 v42, v42\n"
"v_exp_f32 v43, v43\n"
"v_cvt_pk_bf16_f32 v100, v40, v41\n"
"v_cvt_pk_bf16_f32 v101, v42, v43\n"
"v_mfma_f32_32x32x16_bf16 v[64:79], a[124:127], v[96:99], v[64:79]\n"
"s_add_u32 s85, s85, s6\n"
"v_exp_f32 v44, v44\n"
"v_exp_f32 v45, v45\n"
"v_exp_f32 v46, v46\n"
"v_exp_f32 v47, v47\n"
"v_cvt_pk_bf16_f32 v102, v44, v45\n"
"v_cvt_pk_bf16_f32 v103, v46, v47\n"
"v_mfma_f32_32x32x16_bf16 v[32:47], a[140:143], v[104:107], 0\n"
"s_add_u32 s85, s85, 10240\n"
"v_exp_f32 v48, v48\n"
"v_exp_f32 v49, v49\n"
"v_exp_f32 v50, v50\n"
"v_exp_f32 v51, v51\n"
"v_cvt_pk_bf16_f32 v96, v48, v49\n"
"v_cvt_pk_bf16_f32 v97, v50, v51\n"
"v_mfma_f32_32x32x16_bf16 v[64:79], a[128:131], v[100:103], v[64:79]\n"
"s_sub_u32 s82, s82, s20\n"
"v_exp_f32 v52, v52\n"
"v_exp_f32 v53, v53\n"
"v_exp_f32 v54, v54\n"
"v_exp_f32 v55, v55\n"
"v_cvt_pk_bf16_f32 v98, v52, v53\n"
"v_cvt_pk_bf16_f32 v99, v54, v55\n"
"v_mfma_f32_32x32x8_bf16 v[32:47], a[148:149], v[108:109], v[32:47]\n"
"s_mul_i32 s70, s57, 221184\n"
"v_exp_f32 v56, v56\n"
"v_exp_f32 v57, v57\n"
"v_exp_f32 v58, v58\n"
"v_exp_f32 v59, v59\n"
"v_cvt_pk_bf16_f32 v100, v56, v57\n"
"v_cvt_pk_bf16_f32 v101, v58, v59\n"
"v_mfma_f32_32x32x16_bf16 v[64:79], a[132:135], v[96:99], v[64:79]\n"
"s_add_u32 s70, s70, 6160384\n"
"v_exp_f32 v60, v60\n"
"v_exp_f32 v61, v61\n"
"v_exp_f32 v62, v62\n"
"v_exp_f32 v63, v63\n"
"v_cvt_pk_bf16_f32 v102, v60, v61\n"
"v_cvt_pk_bf16_f32 v103, v62, v63\n"
"v_mfma_f32_32x32x16_bf16 v[48:63], a[144:147], v[104:107], 0\n"
"s_add_u32 s70, s70, s20\n"
"v_exp_f32 v32, v32\n"
"v_exp_f32 v33, v33\n"
"v_exp_f32 v34, v34\n"
"v_exp_f32 v35, v35\n"
"v_cvt_pk_bf16_f32 v96, v32, v33\n"
"v_cvt_pk_bf16_f32 v97, v34, v35\n"
"v_mfma_f32_32x32x16_bf16 v[64:79], a[136:139], v[100:103], v[64:79]\n"
"s_and_b32 s13, s56, 1\n"
"v_exp_f32 v36, v36\n"
"v_exp_f32 v37, v37\n"
"v_exp_f32 v38, v38\n"
"v_exp_f32 v39, v39\n"
"v_cvt_pk_bf16_f32 v98, v36, v37\n"
"v_cvt_pk_bf16_f32 v99, v38, v39\n"
"v_mfma_f32_32x32x8_bf16 v[48:63], a[150:151], v[108:109], v[48:63]\n"
"s_lshr_b32 s15, s56, 1\n"
"v_exp_f32 v40, v40\n"
"v_exp_f32 v41, v41\n"
"v_exp_f32 v42, v42\n"
"v_exp_f32 v43, v43\n"
"v_cvt_pk_bf16_f32 v100, v40, v41\n"
"v_cvt_pk_bf16_f32 v101, v42, v43\n"
"v_mfma_f32_32x32x16_bf16 v[64:79], a[152:155], v[96:99], v[64:79]\n"
"s_lshl2_add_u32 s12, s15, s13\n"
"v_exp_f32 v44, v44\n"
"v_exp_f32 v45, v45\n"
"v_exp_f32 v46, v46\n"
"v_exp_f32 v47, v47\n"
"v_cvt_pk_bf16_f32 v102, v44, v45\n"
"v_cvt_pk_bf16_f32 v103, v46, v47\n"
"v_mfma_f32_32x32x16_bf16 v[32:47], a[168:171], v[104:107], 0\n"
"s_lshl3_add_u32 s12, s59, s12\n"
"v_exp_f32 v48, v48\n"
"v_exp_f32 v49, v49\n"
"v_exp_f32 v50, v50\n"
"v_exp_f32 v51, v51\n"
"v_cvt_pk_bf16_f32 v96, v48, v49\n"
"v_cvt_pk_bf16_f32 v97, v50, v51\n"
"v_mfma_f32_32x32x16_bf16 v[64:79], a[156:159], v[100:103], v[64:79]\n"
"s_mul_i32 s66, s12, 20480\n"
"v_exp_f32 v52, v52\n"
"v_exp_f32 v53, v53\n"
"v_exp_f32 v54, v54\n"
"v_exp_f32 v55, v55\n"
"v_cvt_pk_bf16_f32 v98, v52, v53\n"
"v_cvt_pk_bf16_f32 v99, v54, v55\n"
"v_mfma_f32_32x32x8_bf16 v[32:47], a[176:177], v[108:109], v[32:47]\n"
"s_add_u32 s66, s66, 3276800\n"
"v_exp_f32 v56, v56\n"
"v_exp_f32 v57, v57\n"
"v_exp_f32 v58, v58\n"
"v_exp_f32 v59, v59\n"
"v_cvt_pk_bf16_f32 v100, v56, v57\n"
"v_cvt_pk_bf16_f32 v101, v58, v59\n"
"v_mfma_f32_32x32x16_bf16 v[64:79], a[160:163], v[96:99], v[64:79]\n"
"s_add_u32 s66, s66, s20\n"
"v_exp_f32 v60, v60\n"
"v_exp_f32 v61, v61\n"
"v_exp_f32 v62, v62\n"
"v_exp_f32 v63, v63\n"
"v_cvt_pk_bf16_f32 v102, v60, v61\n"
"v_cvt_pk_bf16_f32 v103, v62, v63\n"
"v_mfma_f32_32x32x16_bf16 v[48:63], a[172:175], v[104:107], 0\n"
"s_waitcnt lgkmcnt(0)\n"
"v_exp_f32 v32, v32\n"
"v_exp_f32 v33, v33\n"
"v_exp_f32 v34, v34\n"
"v_exp_f32 v35, v35\n"
"v_cvt_pk_bf16_f32 v96, v32, v33\n"
"v_cvt_pk_bf16_f32 v97, v34, v35\n"
"v_mfma_f32_32x32x16_bf16 v[64:79], a[164:167], v[100:103], v[64:79]\n"
"s_lshl_b32 s67, s12, 11\n"
"v_exp_f32 v36, v36\n"
"v_exp_f32 v37, v37\n"
"v_exp_f32 v38, v38\n"
"v_exp_f32 v39, v39\n"
"v_cvt_pk_bf16_f32 v98, v36, v37\n"
"v_cvt_pk_bf16_f32 v99, v38, v39\n"
"v_mfma_f32_32x32x8_bf16 v[48:63], a[178:179], v[108:109], v[48:63]\n"
"s_add_u32 s67, s67, 5898240\n"
"v_exp_f32 v40, v40\n"
"v_exp_f32 v41, v41\n"
"v_exp_f32 v42, v42\n"
"v_exp_f32 v43, v43\n"
"v_cvt_pk_bf16_f32 v100, v40, v41\n"
"v_cvt_pk_bf16_f32 v101, v42, v43\n"
"v_mfma_f32_32x32x16_bf16 v[64:79], a[180:183], v[96:99], v[64:79]\n"
"s_add_u32 s67, s67, s20\n"
"v_exp_f32 v44, v44\n"
"v_exp_f32 v45, v45\n"
"v_exp_f32 v46, v46\n"
"v_exp_f32 v47, v47\n"
"v_cvt_pk_bf16_f32 v102, v44, v45\n"
"v_cvt_pk_bf16_f32 v103, v46, v47\n"
"v_mfma_f32_32x32x16_bf16 v[32:47], a[196:199], v[104:107], 0\n"
"buffer_load_dword v5, v25, s[20:23], s50 offen sc1\n"
"v_exp_f32 v48, v48\n"
"v_exp_f32 v49, v49\n"
"v_exp_f32 v50, v50\n"
"v_exp_f32 v51, v51\n"
"v_cvt_pk_bf16_f32 v96, v48, v49\n"
"v_cvt_pk_bf16_f32 v97, v50, v51\n"
"v_mfma_f32_32x32x16_bf16 v[64:79], a[184:187], v[100:103], v[64:79]\n"
"s_lshl_b32 s14, s98, 9\n"
"v_exp_f32 v52, v52\n"
"v_exp_f32 v53, v53\n"
"v_exp_f32 v54, v54\n"
"v_exp_f32 v55, v55\n"
"v_cvt_pk_bf16_f32 v98, v52, v53\n"
"v_cvt_pk_bf16_f32 v99, v54, v55\n"
"v_mfma_f32_32x32x8_bf16 v[32:47], a[204:205], v[108:109], v[32:47]\n"
"s_add_u32 s14, s14, 6160384\n"
"v_exp_f32 v56, v56\n"
"v_exp_f32 v57, v57\n"
"v_exp_f32 v58, v58\n"
"v_exp_f32 v59, v59\n"
"v_cvt_pk_bf16_f32 v100, v56, v57\n"
"v_cvt_pk_bf16_f32 v101, v58, v59\n"
"v_mfma_f32_32x32x16_bf16 v[64:79], a[188:191], v[96:99], v[64:79]\n"
"s_mul_i32 s68, s12, 55296\n"
"v_exp_f32 v60, v60\n"
"v_exp_f32 v61, v61\n"
"v_exp_f32 v62, v62\n"
"v_exp_f32 v63, v63\n"
"v_cvt_pk_bf16_f32 v102, v60, v61\n"
"v_cvt_pk_bf16_f32 v103, v62, v63\n"
"v_mfma_f32_32x32x16_bf16 v[48:63], a[200:203], v[104:107], 0\n"
"s_add_u32 s68, s68, s14\n"
"v_exp_f32 v32, v32\n"
"v_exp_f32 v33, v33\n"
"v_exp_f32 v34, v34\n"
"v_exp_f32 v35, v35\n"
"v_cvt_pk_bf16_f32 v96, v32, v33\n"
"v_cvt_pk_bf16_f32 v97, v34, v35\n"
"v_mfma_f32_32x32x16_bf16 v[64:79], a[192:195], v[100:103], v[64:79]\n"
"s_add_u32 s68, s68, s20\n"
"v_exp_f32 v36, v36\n"
"v_exp_f32 v37, v37\n"
"v_exp_f32 v38, v38\n"
"v_exp_f32 v39, v39\n"
"v_cvt_pk_bf16_f32 v98, v36, v37\n"
"v_cvt_pk_bf16_f32 v99, v38, v39\n"
"v_mfma_f32_32x32x8_bf16 v[48:63], a[206:207], v[108:109], v[48:63]\n"
"s_lshl2_add_u32 s12, s57, s13\n"
"v_exp_f32 v40, v40\n"
"v_exp_f32 v41, v41\n"
"v_exp_f32 v42, v42\n"
"v_exp_f32 v43, v43\n"
"v_cvt_pk_bf16_f32 v100, v40, v41\n"
"v_cvt_pk_bf16_f32 v101, v42, v43\n"
"v_mfma_f32_32x32x16_bf16 v[64:79], a[208:211], v[96:99], v[64:79]\n"
"s_lshl3_add_u32 s12, s12, s58\n"
"v_exp_f32 v44, v44\n"
"v_exp_f32 v45, v45\n"
"v_exp_f32 v46, v46\n"
"v_exp_f32 v47, v47\n"
"v_cvt_pk_bf16_f32 v102, v44, v45\n"
"v_cvt_pk_bf16_f32 v103, v46, v47\n"
"v_mfma_f32_32x32x16_bf16 v[32:47], a[0:3], v[110:113], 0\n"
"s_mul_i32 s59, s12, 2560\n"
"v_exp_f32 v48, v48\n"
"v_exp_f32 v49, v49\n"
"v_exp_f32 v50, v50\n"
"v_exp_f32 v51, v51\n"
"v_cvt_pk_bf16_f32 v96, v48, v49\n"
"v_cvt_pk_bf16_f32 v97, v50, v51\n"
"v_mfma_f32_32x32x16_bf16 v[64:79], a[212:215], v[100:103], v[64:79]\n"
"s_mul_i32 s14, s15, 1536\n"
"v_exp_f32 v52, v52\n"
"v_exp_f32 v53, v53\n"
"v_exp_f32 v54, v54\n"
"v_exp_f32 v55, v55\n"
"v_cvt_pk_bf16_f32 v98, v52, v53\n"
"v_cvt_pk_bf16_f32 v99, v54, v55\n"
"v_mfma_f32_32x32x8_bf16 v[32:47], a[8:9], v[114:115], v[32:47]\n"
"s_add_u32 s14, s14, 3276800\n"
"v_exp_f32 v56, v56\n"
"v_exp_f32 v57, v57\n"
"v_exp_f32 v58, v58\n"
"v_exp_f32 v59, v59\n"
"v_cvt_pk_bf16_f32 v100, v56, v57\n"
"v_cvt_pk_bf16_f32 v101, v58, v59\n"
"v_mfma_f32_32x32x16_bf16 v[64:79], a[216:219], v[96:99], v[64:79]\n"
"s_add_u32 s59, s59, s14\n"
"v_exp_f32 v60, v60\n"
"v_exp_f32 v61, v61\n"
"v_exp_f32 v62, v62\n"
"v_exp_f32 v63, v63\n"
"v_cvt_pk_bf16_f32 v102, v60, v61\n"
"v_cvt_pk_bf16_f32 v103, v62, v63\n"
"v_mfma_f32_32x32x16_bf16 v[48:63], a[4:7], v[110:113], 0\n"
"s_add_u32 s59, s59, s20\n"
"v_exp_f32 v32, v32\n"
"v_exp_f32 v33, v33\n"
"v_exp_f32 v34, v34\n"
"v_exp_f32 v35, v35\n"
"v_cvt_pk_bf16_f32 v96, v32, v33\n"
"v_cvt_pk_bf16_f32 v97, v34, v35\n"
"v_mfma_f32_32x32x16_bf16 v[64:79], a[220:223], v[100:103], v[64:79]\n"
"s_lshl_b32 s12, s12, 8\n"
"v_exp_f32 v36, v36\n"
"v_exp_f32 v37, v37\n"
"v_exp_f32 v38, v38\n"
"v_exp_f32 v39, v39\n"
"v_cvt_pk_bf16_f32 v98, v36, v37\n"
"v_cvt_pk_bf16_f32 v99, v38, v39\n"
"v_mfma_f32_32x32x8_bf16 v[48:63], a[10:11], v[114:115], v[48:63]\n"
"s_lshl3_add_u32 s96, s15, s12\n"
"v_exp_f32 v40, v40\n"
"v_exp_f32 v41, v41\n"
"v_exp_f32 v42, v42\n"
"v_exp_f32 v43, v43\n"
"v_cvt_pk_bf16_f32 v100, v40, v41\n"
"v_cvt_pk_bf16_f32 v101, v42, v43\n"
"v_mfma_f32_32x32x16_bf16 v[80:95], a[12:15], v[96:99], v[80:95]\n"
"s_add_u32 s96, s96, 5898240\n"
"v_exp_f32 v44, v44\n"
"v_exp_f32 v45, v45\n"
"v_exp_f32 v46, v46\n"
"v_exp_f32 v47, v47\n"
"v_cvt_pk_bf16_f32 v102, v44, v45\n"
"v_cvt_pk_bf16_f32 v103, v46, v47\n"
"v_mfma_f32_32x32x16_bf16 v[32:47], a[28:31], v[110:113], 0\n"
"ds_write_b128 v124, v[64:67] offset:0\n"
"v_exp_f32 v48, v48\n"
"v_exp_f32 v49, v49\n"
"v_exp_f32 v50, v50\n"
"v_exp_f32 v51, v51\n"
"v_cvt_pk_bf16_f32 v96, v48, v49\n"
"v_cvt_pk_bf16_f32 v97, v50, v51\n"
"v_mfma_f32_32x32x16_bf16 v[80:95], a[16:19], v[100:103], v[80:95]\n"
"ds_write_b128 v124, v[68:71] offset:1024\n"
"v_exp_f32 v52, v52\n"
"v_exp_f32 v53, v53\n"
"v_exp_f32 v54, v54\n"
"v_exp_f32 v55, v55\n"
"v_cvt_pk_bf16_f32 v98, v52, v53\n"
"v_cvt_pk_bf16_f32 v99, v54, v55\n"
"v_mfma_f32_32x32x8_bf16 v[32:47], a[36:37], v[114:115], v[32:47]\n"
"s_add_u32 s96, s96, s20\n"
"v_exp_f32 v56, v56\n"
"v_exp_f32 v57, v57\n"
"v_exp_f32 v58, v58\n"
"v_exp_f32 v59, v59\n"
"v_cvt_pk_bf16_f32 v100, v56, v57\n"
"v_cvt_pk_bf16_f32 v101, v58, v59\n"
"v_mfma_f32_32x32x16_bf16 v[80:95], a[20:23], v[96:99], v[80:95]\n"
"ds_write_b128 v124, v[72:75] offset:2048\n"
"v_exp_f32 v60, v60\n"
"v_exp_f32 v61, v61\n"
"v_exp_f32 v62, v62\n"
"v_exp_f32 v63, v63\n"
"v_cvt_pk_bf16_f32 v102, v60, v61\n"
"v_cvt_pk_bf16_f32 v103, v62, v63\n"
"v_mfma_f32_32x32x16_bf16 v[48:63], a[32:35], v[110:113], 0\n"
"s_waitcnt vmcnt(0)\n"
".Lrdy_chk:\n"
"v_cmp_ne_u32 vcc, s61, v5\n"
"s_nop 1\n"
"s_cbranch_vccnz .Lrdy_re\n"
".Lrdy_ok:\n"
"s_barrier\n"
"v_exp_f32 v32, v32\n"
"v_exp_f32 v33, v33\n"
"v_exp_f32 v34, v34\n"
"v_exp_f32 v35, v35\n"
"v_cvt_pk_bf16_f32 v96, v32, v33\n"
"v_cvt_pk_bf16_f32 v97, v34, v35\n"
"v_mfma_f32_32x32x16_bf16 v[80:95], a[24:27], v[100:103], v[80:95]\n"
"buffer_store_dword v30, v17, s[20:23], s51 offen sc1\n"
"v_exp_f32 v36, v36\n"
"v_exp_f32 v37, v37\n"
"v_exp_f32 v38, v38\n"
"v_exp_f32 v39, v39\n"
"v_cvt_pk_bf16_f32 v98, v36, v37\n"
"v_cvt_pk_bf16_f32 v99, v38, v39\n"
"v_mfma_f32_32x32x8_bf16 v[48:63], a[38:39], v[114:115], v[48:63]\n"
"s_add_u32 s64, s60, 0\n"
"v_exp_f32 v40, v40\n"
"v_exp_f32 v41, v41\n"
"v_exp_f32 v42, v42\n"
"v_exp_f32 v43, v43\n"
"v_cvt_pk_bf16_f32 v100, v40, v41\n"
"v_cvt_pk_bf16_f32 v101, v42, v43\n"
"v_mfma_f32_32x32x16_bf16 v[80:95], a[40:43], v[96:99], v[80:95]\n"
"s_add_u32 s63, s56, 3\n"
"v_exp_f32 v44, v44\n"
"v_exp_f32 v45, v45\n"
"v_exp_f32 v46, v46\n"
"v_exp_f32 v47, v47\n"
"v_cvt_pk_bf16_f32 v102, v44, v45\n"
"v_cvt_pk_bf16_f32 v103, v46, v47\n"
"v_mfma_f32_32x32x16_bf16 v[32:47], a[56:59], v[110:113], 0\n"
".Lhop4:\n"
"s_cbranch_execz .Lhop8\n"
"s_and_b32 s63, s63, 3\n"
"v_exp_f32 v48, v48\n"
"v_exp_f32 v49, v49\n"
"v_exp_f32 v50, v50\n"
"v_exp_f32 v51, v51\n"
"v_cvt_pk_bf16_f32 v96, v48, v49\n"
"v_cvt_pk_bf16_f32 v97, v50, v51\n"
"v_mfma_f32_32x32x16_bf16 v[80:95], a[44:47], v[100:103], v[80:95]\n"
"s_mul_i32 s63, s63, 3072\n"
"v_lshl_add_u32 v125, s63, 1, v28\n"
"v_add_u32 v126, s63, v26\n"
"v_add_u32 v127, s63, v27\n"
"v_exp_f32 v52, v52\n"
"v_exp_f32 v53, v53\n"
"v_exp_f32 v54, v54\n"
"v_exp_f32 v55, v55\n"
"v_cvt_pk_bf16_f32 v98, v52, v53\n"
"v_cvt_pk_bf16_f32 v99, v54, v55\n"
"v_mfma_f32_32x32x8_bf16 v[32:47], a[64:65], v[114:115], v[32:47]\n"
"s_add_u32 m0, s86, 0\n"
"v_exp_f32 v56, v56\n"
"v_exp_f32 v57, v57\n"
"v_exp_f32 v58, v58\n"
"v_exp_f32 v59, v59\n"
"v_cvt_pk_bf16_f32 v100, v56, v57\n"
"v_cvt_pk_bf16_f32 v101, v58, v59\n"
"v_mfma_f32_32x32x16_bf16 v[80:95], a[48:51], v[96:99], v[80:95]\n"
"global_load_lds_dwordx4 v0, s[64:65] offset:0 sc1\n"
"v_exp_f32 v60, v60\n"
"v_exp_f32 v61, v61\n"
"v_exp_f32 v62, v62\n"
"v_exp_f32 v63, v63\n"
"v_cvt_pk_bf16_f32 v102, v60, v61\n"
"v_cvt_pk_bf16_f32 v103, v62, v63\n"
"v_mfma_f32_32x32x16_bf16 v[48:63], a[60:63], v[110:113], 0\n"
"global_load_lds_dwordx4 v0, s[64:65] offset:1024 sc1\n"
"v_exp_f32 v32, v32\n"
"v_exp_f32 v33, v33\n"
"v_exp_f32 v34, v34\n"
"v_exp_f32 v35, v35\n"
"v_cvt_pk_bf16_f32 v96, v32, v33\n"
"v_cvt_pk_bf16_f32 v97, v34, v35\n"
"v_mfma_f32_32x32x16_bf16 v[80:95], a[52:55], v[100:103], v[80:95]\n"
"global_load_lds_dwordx4 v0, s[64:65] offset:2048 sc1\n"
"v_exp_f32 v36, v36\n"
"v_exp_f32 v37, v37\n"
"v_exp_f32 v38, v38\n"
"v_exp_f32 v39, v39\n"
"v_cvt_pk_bf16_f32 v98, v36, v37\n"
"v_cvt_pk_bf16_f32 v99, v38, v39\n"
"v_mfma_f32_32x32x8_bf16 v[48:63], a[66:67], v[114:115], v[48:63]\n"
"s_add_u32 s57, s68, s99\n"
"v_exp_f32 v40, v40\n"
"v_exp_f32 v41, v41\n"
"v_exp_f32 v42, v42\n"
"v_exp_f32 v43, v43\n"
"v_cvt_pk_bf16_f32 v100, v40, v41\n"
"v_cvt_pk_bf16_f32 v101, v42, v43\n"
"v_mfma_f32_32x32x16_bf16 v[80:95], a[68:71], v[96:99], v[80:95]\n"
"global_load_lds_dwordx4 v0, s[64:65] offset:3072 sc1\n"
"v_exp_f32 v44, v44\n"
"v_exp_f32 v45, v45\n"
"v_exp_f32 v46, v46\n"
"v_exp_f32 v47, v47\n"
"v_cvt_pk_bf16_f32 v102, v44, v45\n"
"v_cvt_pk_bf16_f32 v103, v46, v47\n"
"v_mfma_f32_32x32x16_bf16 v[32:47], a[84:87], v[110:113], 0\n"
"s_waitcnt lgkmcnt(0)\n"
"v_exp_f32 v48, v48\n"
"v_exp_f32 v49, v49\n"
"v_exp_f32 v50, v50\n"
"v_exp_f32 v51, v51\n"
"v_cvt_pk_bf16_f32 v96, v48, v49\n"
"v_cvt_pk_bf16_f32 v97, v50, v51\n"
"v_mfma_f32_32x32x16_bf16 v[80:95], a[72:75], v[100:103], v[80:95]\n"
"s_add_u32 m0, m0, 4096\n"
"v_exp_f32 v52, v52\n"
"v_exp_f32 v53, v53\n"
"v_exp_f32 v54, v54\n"
"v_exp_f32 v55, v55\n"
"v_cvt_pk_bf16_f32 v98, v52, v53\n"
"v_cvt_pk_bf16_f32 v99, v54, v55\n"
"v_mfma_f32_32x32x8_bf16 v[32:47], a[92:93], v[114:115], v[32:47]\n"
"v_exp_f32 v56, v56\n"
"v_exp_f32 v57, v57\n"
"v_exp_f32 v58, v58\n"
"v_exp_f32 v59, v59\n"
"v_cvt_pk_bf16_f32 v100, v56, v57\n"
"v_cvt_pk_bf16_f32 v101, v58, v59\n"
"v_mfma_f32_32x32x16_bf16 v[80:95], a[76:79], v[96:99], v[80:95]\n"
"global_load_lds_dwordx4 v1, s[64:65] offset:0 sc1\n"
"v_exp_f32 v60, v60\n"
"v_exp_f32 v61, v61\n"
"v_exp_f32 v62, v62\n"
"v_exp_f32 v63, v63\n"
"v_cvt_pk_bf16_f32 v102, v60, v61\n"
"v_cvt_pk_bf16_f32 v103, v62, v63\n"
"v_mfma_f32_32x32x16_bf16 v[48:63], a[88:91], v[110:113], 0\n"
"s_barrier\n"
"v_exp_f32 v32, v32\n"
"v_exp_f32 v33, v33\n"
"v_exp_f32 v34, v34\n"
"v_exp_f32 v35, v35\n"
"v_cvt_pk_bf16_f32 v96, v32, v33\n"
"v_cvt_pk_bf16_f32 v97, v34, v35\n"
"v_mfma_f32_32x32x16_bf16 v[80:95], a[80:83], v[100:103], v[80:95]\n"
"s_add_u32 s64, s60, 41984\n"
"ds_read_b128 v[64:67], v125 offset:0\n"
"v_exp_f32 v36, v36\n"
"v_exp_f32 v37, v37\n"
"v_exp_f32 v38, v38\n"
"v_exp_f32 v39, v39\n"
"v_cvt_pk_bf16_f32 v98, v36, v37\n"
"v_cvt_pk_bf16_f32 v99, v38, v39\n"
"v_mfma_f32_32x32x8_bf16 v[48:63], a[94:95], v[114:115], v[48:63]\n"
"s_add_u32 m0, s86, 20480\n"
"ds_read_b128 v[68:71], v125 offset:1024\n"
"v_exp_f32 v40, v40\n"
"v_exp_f32 v41, v41\n"
"v_exp_f32 v42, v42\n"
"v_exp_f32 v43, v43\n"
"v_cvt_pk_bf16_f32 v100, v40, v41\n"
"v_cvt_pk_bf16_f32 v101, v42, v43\n"
"v_mfma_f32_32x32x16_bf16 v[80:95], a[96:99], v[96:99], v[80:95]\n"
"global_load_lds_dwordx4 v0, s[64:65] offset:0 sc1\n"
"v_exp_f32 v44, v44\n"
"v_exp_f32 v45, v45\n"
"v_exp_f32 v46, v46\n"
"v_exp_f32 v47, v47\n"
"v_cvt_pk_bf16_f32 v102, v44, v45\n"
"v_cvt_pk_bf16_f32 v103, v46, v47\n"
"v_mfma_f32_32x32x16_bf16 v[32:47], a[112:115], v[110:113], 0\n"
"global_load_lds_dwordx4 v0, s[64:65] offset:1024 sc1\n"
"v_exp_f32 v48, v48\n"
"v_exp_f32 v49, v49\n"
"v_exp_f32 v50, v50\n"
"v_exp_f32 v51, v51\n"
"v_cvt_pk_bf16_f32 v96, v48, v49\n"
"v_cvt_pk_bf16_f32 v97, v50, v51\n"
"v_mfma_f32_32x32x16_bf16 v[80:95], a[100:103], v[100:103], v[80:95]\n"
"global_load_lds_dwordx4 v0, s[64:65] offset:2048 sc1\n"
"v_exp_f32 v52, v52\n"
"v_exp_f32 v53, v53\n"
"v_exp_f32 v54, v54\n"
"v_exp_f32 v55, v55\n"
"v_cvt_pk_bf16_f32 v98, v52, v53\n"
"v_cvt_pk_bf16_f32 v99, v54, v55\n"
"v_mfma_f32_32x32x8_bf16 v[32:47], a[120:121], v[114:115], v[32:47]\n"
"ds_read_b128 v[72:75], v125 offset:2048\n"
"v_exp_f32 v56, v56\n"
"v_exp_f32 v57, v57\n"
"v_exp_f32 v58, v58\n"
"v_exp_f32 v59, v59\n"
"v_cvt_pk_bf16_f32 v100, v56, v57\n"
"v_cvt_pk_bf16_f32 v101, v58, v59\n"
"v_mfma_f32_32x32x16_bf16 v[80:95], a[104:107], v[96:99], v[80:95]\n"
"global_load_lds_dwordx4 v0, s[64:65] offset:3072 sc1\n"
"v_exp_f32 v60, v60\n"
"v_exp_f32 v61, v61\n"
"v_exp_f32 v62, v62\n"
"v_exp_f32 v63, v63\n"
"v_cvt_pk_bf16_f32 v102, v60, v61\n"
"v_cvt_pk_bf16_f32 v103, v62, v63\n"
"v_mfma_f32_32x32x16_bf16 v[48:63], a[116:119], v[110:113], 0\n"
"s_add_u32 m0, m0, 4096\n"
"ds_read_b128 v[104:107], v126 offset:0\n"
"v_exp_f32 v32, v32\n"
"v_exp_f32 v33, v33\n"
"v_exp_f32 v34, v34\n"
"v_exp_f32 v35, v35\n"
"v_cvt_pk_bf16_f32 v96, v32, v33\n"
"v_cvt_pk_bf16_f32 v97, v34, v35\n"
"v_mfma_f32_32x32x16_bf16 v[80:95], a[108:111], v[100:103], v[80:95]\n"
"global_load_lds_dwordx4 v1, s[64:65] offset:0 sc1\n"
"v_exp_f32 v36, v36\n"
"v_exp_f32 v37, v37\n"
"v_exp_f32 v38, v38\n"
"v_exp_f32 v39, v39\n"
"v_cvt_pk_bf16_f32 v98, v36, v37\n"
"v_cvt_pk_bf16_f32 v99, v38, v39\n"
"v_mfma_f32_32x32x8_bf16 v[48:63], a[122:123], v[114:115], v[48:63]\n"
"s_add_u32 s64, s60, 83968\n"
"ds_read_b64 v[108:109], v127 offset:32\n"
"v_exp_f32 v40, v40\n"
"v_exp_f32 v41, v41\n"
"v_exp_f32 v42, v42\n"
"v_exp_f32 v43, v43\n"
"v_cvt_pk_bf16_f32 v100, v40, v41\n"
"v_cvt_pk_bf16_f32 v101, v42, v43\n"
"v_mfma_f32_32x32x16_bf16 v[80:95], a[124:127], v[96:99], v[80:95]\n"
"s_add_u32 m0, s86, 40960\n"
"v_exp_f32 v44, v44\n"
"v_exp_f32 v45, v45\n"
"v_exp_f32 v46, v46\n"
"v_exp_f32 v47, v47\n"
"v_cvt_pk_bf16_f32 v102, v44, v45\n"
"v_cvt_pk_bf16_f32 v103, v46, v47\n"
"v_mfma_f32_32x32x16_bf16 v[32:47], a[140:143], v[110:113], 0\n"
"global_load_lds_dwordx4 v0, s[64:65] offset:0 sc1\n"
"v_exp_f32 v48, v48\n"
"v_exp_f32 v49, v49\n"
"v_exp_f32 v50, v50\n"
"v_exp_f32 v51, v51\n"
"v_cvt_pk_bf16_f32 v96, v48, v49\n"
"v_cvt_pk_bf16_f32 v97, v50, v51\n"
"v_mfma_f32_32x32x16_bf16 v[80:95], a[128:131], v[100:103], v[80:95]\n"
"global_load_lds_dwordx4 v0, s[64:65] offset:1024 sc1\n"
"v_exp_f32 v52, v52\n"
"v_exp_f32 v53, v53\n"
"v_exp_f32 v54, v54\n"
"v_exp_f32 v55, v55\n"
"v_cvt_pk_bf16_f32 v98, v52, v53\n"
"v_cvt_pk_bf16_f32 v99, v54, v55\n"
"v_mfma_f32_32x32x8_bf16 v[32:47], a[148:149], v[114:115], v[32:47]\n"
"v_exp_f32 v56, v56\n"
"v_exp_f32 v57, v57\n"
"v_exp_f32 v58, v58\n"
"v_exp_f32 v59, v59\n"
"v_cvt_pk_bf16_f32 v100, v56, v57\n"
"v_cvt_pk_bf16_f32 v101, v58, v59\n"
"v_mfma_f32_32x32x16_bf16 v[80:95], a[132:135], v[96:99], v[80:95]\n"
"global_load_lds_dwordx4 v0, s[64:65] offset:2048 sc1\n"
"v_exp_f32 v60, v60\n"
"v_exp_f32 v61, v61\n"
"v_exp_f32 v62, v62\n"
"v_exp_f32 v63, v63\n"
"v_cvt_pk_bf16_f32 v102, v60, v61\n"
"v_cvt_pk_bf16_f32 v103, v62, v63\n"
"v_mfma_f32_32x32x16_bf16 v[48:63], a[144:147], v[110:113], 0\n"
"global_load_lds_dwordx4 v0, s[64:65] offset:3072 sc1\n"
"v_exp_f32 v32, v32\n"
"v_exp_f32 v33, v33\n"
"v_exp_f32 v34, v34\n"
"v_exp_f32 v35, v35\n"
"v_cvt_pk_bf16_f32 v96, v32, v33\n"
"v_cvt_pk_bf16_f32 v97, v34, v35\n"
"v_mfma_f32_32x32x16_bf16 v[80:95], a[136:139], v[100:103], v[80:95]\n"
"s_add_u32 m0, m0, 4096\n"
"v_exp_f32 v36, v36\n"
"v_exp_f32 v37, v37\n"
"v_exp_f32 v38, v38\n"
"v_exp_f32 v39, v39\n"
"v_cvt_pk_bf16_f32 v98, v36, v37\n"
"v_cvt_pk_bf16_f32 v99, v38, v39\n"
"v_mfma_f32_32x32x8_bf16 v[48:63], a[150:151], v[114:115], v[48:63]\n"
"v_exp_f32 v40, v40\n"
"v_exp_f32 v41, v41\n"
"v_exp_f32 v42, v42\n"
"v_exp_f32 v43, v43\n"
"v_cvt_pk_bf16_f32 v100, v40, v41\n"
"v_cvt_pk_bf16_f32 v101, v42, v43\n"
"v_mfma_f32_32x32x16_bf16 v[80:95], a[152:155], v[96:99], v[80:95]\n"
"global_load_lds_dwordx4 v1, s[64:65] offset:0 sc1\n"
"v_exp_f32 v44, v44\n"
"v_exp_f32 v45, v45\n"
"v_exp_f32 v46, v46\n"
"v_exp_f32 v47, v47\n"
"v_cvt_pk_bf16_f32 v102, v44, v45\n"
"v_cvt_pk_bf16_f32 v103, v46, v47\n"
"v_mfma_f32_32x32x16_bf16 v[32:47], a[168:171], v[110:113], 0\n"
"s_add_u32 s64, s18, 0\n"
"v_exp_f32 v48, v48\n"
"v_exp_f32 v49, v49\n"
"v_exp_f32 v50, v50\n"
"v_exp_f32 v51, v51\n"
"v_cvt_pk_bf16_f32 v96, v48, v49\n"
"v_cvt_pk_bf16_f32 v97, v50, v51\n"
"v_mfma_f32_32x32x16_bf16 v[80:95], a[156:159], v[100:103], v[80:95]\n"
"s_add_u32 m0, s86, 61440\n"
"v_exp_f32 v52, v52\n"
"v_exp_f32 v53, v53\n"
"v_exp_f32 v54, v54\n"
"v_exp_f32 v55, v55\n"
"v_cvt_pk_bf16_f32 v98, v52, v53\n"
"v_cvt_pk_bf16_f32 v99, v54, v55\n"
"v_mfma_f32_32x32x8_bf16 v[32:47], a[176:177], v[114:115], v[32:47]\n"
"v_exp_f32 v56, v56\n"
"v_exp_f32 v57, v57\n"
"v_exp_f32 v58, v58\n"
"v_exp_f32 v59, v59\n"
"v_cvt_pk_bf16_f32 v100, v56, v57\n"
"v_cvt_pk_bf16_f32 v101, v58, v59\n"
"v_mfma_f32_32x32x16_bf16 v[80:95], a[160:163], v[96:99], v[80:95]\n"
"global_load_lds_dwordx4 v0, s[64:65] offset:0 sc1\n"
"v_exp_f32 v60, v60\n"
"v_exp_f32 v61, v61\n"
"v_exp_f32 v62, v62\n"
"v_exp_f32 v63, v63\n"
"v_cvt_pk_bf16_f32 v102, v60, v61\n"
"v_cvt_pk_bf16_f32 v103, v62, v63\n"
"v_mfma_f32_32x32x16_bf16 v[48:63], a[172:175], v[110:113], 0\n"
"global_load_lds_dwordx4 v0, s[64:65] offset:1024 sc1\n"
"v_exp_f32 v32, v32\n"
"v_exp_f32 v33, v33\n"
"v_exp_f32 v34, v34\n"
"v_exp_f32 v35, v35\n"
"v_cvt_pk_bf16_f32 v96, v32, v33\n"
"v_cvt_pk_bf16_f32 v97, v34, v35\n"
"v_mfma_f32_32x32x16_bf16 v[80:95], a[164:167], v[100:103], v[80:95]\n"
"global_load_lds_dwordx4 v0, s[64:65] offset:2048 sc1\n"
"v_exp_f32 v36, v36\n"
"v_exp_f32 v37, v37\n"
"v_exp_f32 v38, v38\n"
"v_exp_f32 v39, v39\n"
"v_cvt_pk_bf16_f32 v98, v36, v37\n"
"v_cvt_pk_bf16_f32 v99, v38, v39\n"
"v_mfma_f32_32x32x8_bf16 v[48:63], a[178:179], v[114:115], v[48:63]\n"
"v_exp_f32 v40, v40\n"
"v_exp_f32 v41, v41\n"
"v_exp_f32 v42, v42\n"
"v_exp_f32 v43, v43\n"
"v_cvt_pk_bf16_f32 v100, v40, v41\n"
"v_cvt_pk_bf16_f32 v101, v42, v43\n"
"v_mfma_f32_32x32x16_bf16 v[80:95], a[180:183], v[96:99], v[80:95]\n"
"global_load_lds_dwordx4 v0, s[64:65] offset:3072 sc1\n"
"v_exp_f32 v44, v44\n"
"v_exp_f32 v45, v45\n"
"v_exp_f32 v46, v46\n"
"v_exp_f32 v47, v47\n"
"v_cvt_pk_bf16_f32 v102, v44, v45\n"
"v_cvt_pk_bf16_f32 v103, v46, v47\n"
"v_mfma_f32_32x32x16_bf16 v[32:47], a[196:199], v[110:113], 0\n"
"s_waitcnt lgkmcnt(0)\n"
"v_exp_f32 v48, v48\n"
"v_exp_f32 v49, v49\n"
"v_exp_f32 v50, v50\n"
"v_exp_f32 v51, v51\n"
"v_cvt_pk_bf16_f32 v96, v48, v49\n"
"v_cvt_pk_bf16_f32 v97, v50, v51\n"
"v_mfma_f32_32x32x16_bf16 v[80:95], a[184:187], v[100:103], v[80:95]\n"
"s_add_u32 m0, m0, 4096\n"
"v_exp_f32 v52, v52\n"
"v_exp_f32 v53, v53\n"
"v_exp_f32 v54, v54\n"
"v_exp_f32 v55, v55\n"
"v_cvt_pk_bf16_f32 v98, v52, v53\n"
"v_cvt_pk_bf16_f32 v99, v54, v55\n"
"v_mfma_f32_32x32x8_bf16 v[32:47], a[204:205], v[114:115], v[32:47]\n"
"v_exp_f32 v56, v56\n"
"v_exp_f32 v57, v57\n"
"v_exp_f32 v58, v58\n"
"v_exp_f32 v59, v59\n"
"v_cvt_pk_bf16_f32 v100, v56, v57\n"
"v_cvt_pk_bf16_f32 v101, v58, v59\n"
"v_mfma_f32_32x32x16_bf16 v[80:95], a[188:191], v[96:99], v[80:95]\n"
"global_load_lds_dwordx4 v1, s[64:65] offset:0 sc1\n"
"v_exp_f32 v60, v60\n"
"v_exp_f32 v61, v61\n"
"v_exp_f32 v62, v62\n"
"v_exp_f32 v63, v63\n"
"v_cvt_pk_bf16_f32 v102, v60, v61\n"
"v_cvt_pk_bf16_f32 v103, v62, v63\n"
"v_mfma_f32_32x32x16_bf16 v[48:63], a[200:203], v[110:113], 0\n"
"s_add_u32 s64, s18, 41984\n"
"v_exp_f32 v32, v32\n"
"v_exp_f32 v33, v33\n"
"v_exp_f32 v34, v34\n"
"v_exp_f32 v35, v35\n"
"v_cvt_pk_bf16_f32 v96, v32, v33\n"
"v_cvt_pk_bf16_f32 v97, v34, v35\n"
"v_mfma_f32_32x32x16_bf16 v[80:95], a[192:195], v[100:103], v[80:95]\n"
"s_add_u32 m0, s86, 81920\n"
"v_exp_f32 v36, v36\n"
"v_exp_f32 v37, v37\n"
"v_exp_f32 v38, v38\n"
"v_exp_f32 v39, v39\n"
"v_cvt_pk_bf16_f32 v98, v36, v37\n"
"v_cvt_pk_bf16_f32 v99, v38, v39\n"
"v_mfma_f32_32x32x8_bf16 v[48:63], a[206:207], v[114:115], v[48:63]\n"
"v_exp_f32 v40, v40\n"
"v_exp_f32 v41, v41\n"
"v_exp_f32 v42, v42\n"
"v_exp_f32 v43, v43\n"
"v_cvt_pk_bf16_f32 v100, v40, v41\n"
"v_cvt_pk_bf16_f32 v101, v42, v43\n"
"v_mfma_f32_32x32x16_bf16 v[80:95], a[208:211], v[96:99], v[80:95]\n"
"global_load_lds_dwordx4 v0, s[64:65] offset:0 sc1\n"
"v_exp_f32 v44, v44\n"
"v_exp_f32 v45, v45\n"
"v_exp_f32 v46, v46\n"
"v_exp_f32 v47, v47\n"
"v_cvt_pk_bf16_f32 v102, v44, v45\n"
"v_cvt_pk_bf16_f32 v103, v46, v47\n"
"v_mfma_f32_32x32x16_bf16 v[32:47], a[0:3], v[104:107], 0\n"
"global_load_lds_dwordx4 v0, s[64:65] offset:1024 sc1\n"
"v_exp_f32 v48, v48\n"
"v_exp_f32 v49, v49\n"
"v_exp_f32 v50, v50\n"
"v_exp_f32 v51, v51\n"
"v_cvt_pk_bf16_f32 v96, v48, v49\n"
"v_cvt_pk_bf16_f32 v97, v50, v51\n"
"v_mfma_f32_32x32x16_bf16 v[80:95], a[212:215], v[100:103], v[80:95]\n"
"global_load_lds_dwordx4 v0, s[64:65] offset:2048 sc1\n"
"v_exp_f32 v52, v52\n"
"v_exp_f32 v53, v53\n"
"v_exp_f32 v54, v54\n"
"v_exp_f32 v55, v55\n"
"v_cvt_pk_bf16_f32 v98, v52, v53\n"
"v_cvt_pk_bf16_f32 v99, v54, v55\n"
"v_mfma_f32_32x32x8_bf16 v[32:47], a[8:9], v[108:109], v[32:47]\n"
"v_exp_f32 v56, v56\n"
"v_exp_f32 v57, v57\n"
"v_exp_f32 v58, v58\n"
"v_exp_f32 v59, v59\n"
"v_cvt_pk_bf16_f32 v100, v56, v57\n"
"v_cvt_pk_bf16_f32 v101, v58, v59\n"
"v_mfma_f32_32x32x16_bf16 v[80:95], a[216:219], v[96:99], v[80:95]\n"
"global_load_lds_dwordx4 v0, s[64:65] offset:3072 sc1\n"
"v_exp_f32 v60, v60\n"
"v_exp_f32 v61, v61\n"
"v_exp_f32 v62, v62\n"
"v_exp_f32 v63, v63\n"
"v_cvt_pk_bf16_f32 v102, v60, v61\n"
"v_cvt_pk_bf16_f32 v103, v62, v63\n"
"v_mfma_f32_32x32x16_bf16 v[48:63], a[4:7], v[104:107], 0\n"
"s_add_u32 m0, m0, 4096\n"
"v_exp_f32 v32, v32\n"
"v_exp_f32 v33, v33\n"
"v_exp_f32 v34, v34\n"
"v_exp_f32 v35, v35\n"
"v_cvt_pk_bf16_f32 v96, v32, v33\n"
"v_cvt_pk_bf16_f32 v97, v34, v35\n"
"v_mfma_f32_32x32x16_bf16 v[80:95], a[220:223], v[100:103], v[80:95]\n"
"global_load_lds_dwordx4 v1, s[64:65] offset:0 sc1\n"
"v_exp_f32 v36, v36\n"
"v_exp_f32 v37, v37\n"
"v_exp_f32 v38, v38\n"
"v_exp_f32 v39, v39\n"
"v_cvt_pk_bf16_f32 v98, v36, v37\n"
"v_cvt_pk_bf16_f32 v99, v38, v39\n"
"v_mfma_f32_32x32x8_bf16 v[48:63], a[10:11], v[108:109], v[48:63]\n"
"s_add_u32 s64, s18, 83968\n"
"v_exp_f32 v40, v40\n"
"v_exp_f32 v41, v41\n"
"v_exp_f32 v42, v42\n"
"v_exp_f32 v43, v43\n"
"v_cvt_pk_bf16_f32 v100, v40, v41\n"
"v_cvt_pk_bf16_f32 v101, v42, v43\n"
"v_mfma_f32_32x32x16_bf16 v[64:79], a[12:15], v[96:99], v[64:79]\n"
"s_add_u32 m0, s86, 102400\n"
"v_exp_f32 v44, v44\n"
"v_exp_f32 v45, v45\n"
"v_exp_f32 v46, v46\n"
"v_exp_f32 v47, v47\n"
"v_cvt_pk_bf16_f32 v102, v44, v45\n"
"v_cvt_pk_bf16_f32 v103, v46, v47\n"
"v_mfma_f32_32x32x16_bf16 v[32:47], a[28:31], v[104:107], 0\n"
"ds_write_b128 v124, v[80:83] offset:3072\n"
"v_exp_f32 v48, v48\n"
"v_exp_f32 v49, v49\n"
"v_exp_f32 v50, v50\n"
"v_exp_f32 v51, v51\n"
"v_cvt_pk_bf16_f32 v96, v48, v49\n"
"v_cvt_pk_bf16_f32 v97, v50, v51\n"
"v_mfma_f32_32x32x16_bf16 v[64:79], a[16:19], v[100:103], v[64:79]\n"
"ds_write_b128 v124, v[84:87] offset:4096\n"
"v_exp_f32 v52, v52\n"
"v_exp_f32 v53, v53\n"
"v_exp_f32 v54, v54\n"
"v_exp_f32 v55, v55\n"
"v_cvt_pk_bf16_f32 v98, v52, v53\n"
"v_cvt_pk_bf16_f32 v99, v54, v55\n"
"v_mfma_f32_32x32x8_bf16 v[32:47], a[36:37], v[108:109], v[32:47]\n"
"v_exp_f32 v56, v56\n"
"v_exp_f32 v57, v57\n"
"v_exp_f32 v58, v58\n"
"v_exp_f32 v59, v59\n"
"v_cvt_pk_bf16_f32 v100, v56, v57\n"
"v_cvt_pk_bf16_f32 v101, v58, v59\n"
"v_mfma_f32_32x32x16_bf16 v[64:79], a[20:23], v[96:99], v[64:79]\n"
"ds_write_b128 v124, v[88:91] offset:5120\n"
"v_exp_f32 v60, v60\n"
"v_exp_f32 v61, v61\n"
"v_exp_f32 v62, v62\n"
"v_exp_f32 v63, v63\n"
"v_cvt_pk_bf16_f32 v102, v60, v61\n"
"v_cvt_pk_bf16_f32 v103, v62, v63\n"
"v_mfma_f32_32x32x16_bf16 v[48:63], a[32:35], v[104:107], 0\n"
"global_load_lds_dwordx4 v0, s[64:65] offset:0 sc1\n"
"v_exp_f32 v32, v32\n"
"v_exp_f32 v33, v33\n"
"v_exp_f32 v34, v34\n"
"v_exp_f32 v35, v35\n"
"v_cvt_pk_bf16_f32 v96, v32, v33\n"
"v_cvt_pk_bf16_f32 v97, v34, v35\n"
"v_mfma_f32_32x32x16_bf16 v[64:79], a[24:27], v[100:103], v[64:79]\n"
"global_load_lds_dwordx4 v0, s[64:65] offset:1024 sc1\n"
"v_exp_f32 v36, v36\n"
"v_exp_f32 v37, v37\n"
"v_exp_f32 v38, v38\n"
"v_exp_f32 v39, v39\n"
"v_cvt_pk_bf16_f32 v98, v36, v37\n"
"v_cvt_pk_bf16_f32 v99, v38, v39\n"
"v_mfma_f32_32x32x8_bf16 v[48:63], a[38:39], v[108:109], v[48:63]\n"
"v_exp_f32 v40, v40\n"
"v_exp_f32 v41, v41\n"
"v_exp_f32 v42, v42\n"
"v_exp_f32 v43, v43\n"
"v_cvt_pk_bf16_f32 v100, v40, v41\n"
"v_cvt_pk_bf16_f32 v101, v42, v43\n"
"v_mfma_f32_32x32x16_bf16 v[64:79], a[40:43], v[96:99], v[64:79]\n"
"global_load_lds_dwordx4 v0, s[64:65] offset:2048 sc1\n"
"v_exp_f32 v44, v44\n"
"v_exp_f32 v45, v45\n"
"v_exp_f32 v46, v46\n"
"v_exp_f32 v47, v47\n"
"v_cvt_pk_bf16_f32 v102, v44, v45\n"
"v_cvt_pk_bf16_f32 v103, v46, v47\n"
"v_mfma_f32_32x32x16_bf16 v[32:47], a[56:59], v[104:107], 0\n"
"global_load_lds_dwordx4 v0, s[64:65] offset:3072 sc1\n"
"v_exp_f32 v48, v48\n"
"v_exp_f32 v49, v49\n"
"v_exp_f32 v50, v50\n"
"v_exp_f32 v51, v51\n"
"v_cvt_pk_bf16_f32 v96, v48, v49\n"
"v_cvt_pk_bf16_f32 v97, v50, v51\n"
"v_mfma_f32_32x32x16_bf16 v[64:79], a[44:47], v[100:103], v[64:79]\n"
"s_add_u32 m0, m0, 4096\n"
"v_exp_f32 v52, v52\n"
"v_exp_f32 v53, v53\n"
"v_exp_f32 v54, v54\n"
"v_exp_f32 v55, v55\n"
"v_cvt_pk_bf16_f32 v98, v52, v53\n"
"v_cvt_pk_bf16_f32 v99, v54, v55\n"
"v_mfma_f32_32x32x8_bf16 v[32:47], a[64:65], v[108:109], v[32:47]\n"
"v_exp_f32 v56, v56\n"
"v_exp_f32 v57, v57\n"
"v_exp_f32 v58, v58\n"
"v_exp_f32 v59, v59\n"
"v_cvt_pk_bf16_f32 v100, v56, v57\n"
"v_cvt_pk_bf16_f32 v101, v58, v59\n"
"v_mfma_f32_32x32x16_bf16 v[64:79], a[48:51], v[96:99], v[64:79]\n"
"global_load_lds_dwordx4 v1, s[64:65] offset:0 sc1\n"
"v_exp_f32 v60, v60\n"
"v_exp_f32 v61, v61\n"
"v_exp_f32 v62, v62\n"
"v_exp_f32 v63, v63\n"
"v_cvt_pk_bf16_f32 v102, v60, v61\n"
"v_cvt_pk_bf16_f32 v103, v62, v63\n"
"v_mfma_f32_32x32x16_bf16 v[48:63], a[60:63], v[104:107], 0\n"
"s_add_u32 s64, s48, 0\n"
"v_exp_f32 v32, v32\n"
"v_exp_f32 v33, v33\n"
"v_exp_f32 v34, v34\n"
"v_exp_f32 v35, v35\n"
"v_cvt_pk_bf16_f32 v96, v32, v33\n"
"v_cvt_pk_bf16_f32 v97, v34, v35\n"
"v_mfma_f32_32x32x16_bf16 v[64:79], a[52:55], v[100:103], v[64:79]\n"
"global_load_dwordx4 v[188:191], v0, s[64:65] offset:0 sc1\n"
"v_exp_f32 v36, v36\n"
"v_exp_f32 v37, v37\n"
"v_exp_f32 v38, v38\n"
"v_exp_f32 v39, v39\n"
"v_cvt_pk_bf16_f32 v98, v36, v37\n"
"v_cvt_pk_bf16_f32 v99, v38, v39\n"
"v_mfma_f32_32x32x8_bf16 v[48:63], a[66:67], v[108:109], v[48:63]\n"
"v_exp_f32 v40, v40\n"
"v_exp_f32 v41, v41\n"
"v_exp_f32 v42, v42\n"
"v_exp_f32 v43, v43\n"
"v_cvt_pk_bf16_f32 v100, v40, v41\n"
"v_cvt_pk_bf16_f32 v101, v42, v43\n"
"v_mfma_f32_32x32x16_bf16 v[64:79], a[68:71], v[96:99], v[64:79]\n"
"global_load_dwordx4 v[208:211], v0, s[64:65] offset:1024 sc1\n"
"v_exp_f32 v44, v44\n"
"v_exp_f32 v45, v45\n"
"v_exp_f32 v46, v46\n"
"v_exp_f32 v47, v47\n"
"v_cvt_pk_bf16_f32 v102, v44, v45\n"
"v_cvt_pk_bf16_f32 v103, v46, v47\n"
"v_mfma_f32_32x32x16_bf16 v[32:47], a[84:87], v[104:107], 0\n"
"global_load_dwordx4 v[228:231], v0, s[64:65] offset:2048 sc1\n"
"v_exp_f32 v48, v48\n"
"v_exp_f32 v49, v49\n"
"v_exp_f32 v50, v50\n"
"v_exp_f32 v51, v51\n"
"v_cvt_pk_bf16_f32 v96, v48, v49\n"
"v_cvt_pk_bf16_f32 v97, v50, v51\n"
"v_mfma_f32_32x32x16_bf16 v[64:79], a[72:75], v[100:103], v[64:79]\n"
"global_load_dwordx4 v[248:251], v0, s[64:65] offset:3072 sc1\n"
"v_exp_f32 v52, v52\n"
"v_exp_f32 v53, v53\n"
"v_exp_f32 v54, v54\n"
"v_exp_f32 v55, v55\n"
"v_cvt_pk_bf16_f32 v98, v52, v53\n"
"v_cvt_pk_bf16_f32 v99, v54, v55\n"
"v_mfma_f32_32x32x8_bf16 v[32:47], a[92:93], v[108:109], v[32:47]\n"
"v_exp_f32 v56, v56\n"
"v_exp_f32 v57, v57\n"
"v_exp_f32 v58, v58\n"
"v_exp_f32 v59, v59\n"
"v_cvt_pk_bf16_f32 v100, v56, v57\n"
"v_cvt_pk_bf16_f32 v101, v58, v59\n"
"v_mfma_f32_32x32x16_bf16 v[64:79], a[76:79], v[96:99], v[64:79]\n"
"global_load_dwordx4 a[236:239], v1, s[64:65] offset:0 sc1\n"
"v_exp_f32 v60, v60\n"
"v_exp_f32 v61, v61\n"
"v_exp_f32 v62, v62\n"
"v_exp_f32 v63, v63\n"
"v_cvt_pk_bf16_f32 v102, v60, v61\n"
"v_cvt_pk_bf16_f32 v103, v62, v63\n"
"v_mfma_f32_32x32x16_bf16 v[48:63], a[88:91], v[104:107], 0\n"
"s_waitcnt lgkmcnt(0)\n"
"s_barrier\n"
"v_exp_f32 v32, v32\n"
"v_exp_f32 v33, v33\n"
"v_exp_f32 v34, v34\n"
"v_exp_f32 v35, v35\n"
"v_cvt_pk_bf16_f32 v96, v32, v33\n"
"v_cvt_pk_bf16_f32 v97, v34, v35\n"
"v_mfma_f32_32x32x16_bf16 v[64:79], a[80:83], v[100:103], v[64:79]\n"
"s_add_u32 s64, s48, 20480\n"
"ds_read_b128 v[80:83], v125 offset:3072\n"
"v_exp_f32 v36, v36\n"
"v_exp_f32 v37, v37\n"
"v_exp_f32 v38, v38\n"
"v_exp_f32 v39, v39\n"
"v_cvt_pk_bf16_f32 v98, v36, v37\n"
"v_cvt_pk_bf16_f32 v99, v38, v39\n"
"v_mfma_f32_32x32x8_bf16 v[48:63], a[94:95], v[108:109], v[48:63]\n"
"ds_read_b128 v[84:87], v125 offset:4096\n"
"v_exp_f32 v40, v40\n"
"v_exp_f32 v41, v41\n"
"v_exp_f32 v42, v42\n"
"v_exp_f32 v43, v43\n"
"v_cvt_pk_bf16_f32 v100, v40, v41\n"
"v_cvt_pk_bf16_f32 v101, v42, v43\n"
"v_mfma_f32_32x32x16_bf16 v[64:79], a[96:99], v[96:99], v[64:79]\n"
"global_load_dwordx4 v[192:195], v0, s[64:65] offset:0 sc1\n"
"v_exp_f32 v44, v44\n"
"v_exp_f32 v45, v45\n"
"v_exp_f32 v46, v46\n"
"v_exp_f32 v47, v47\n"
"v_cvt_pk_bf16_f32 v102, v44, v45\n"
"v_cvt_pk_bf16_f32 v103, v46, v47\n"
"v_mfma_f32_32x32x16_bf16 v[32:47], a[112:115], v[104:107], 0\n"
"global_load_dwordx4 v[212:215], v0, s[64:65] offset:1024 sc1\n"
"v_exp_f32 v48, v48\n"
"v_exp_f32 v49, v49\n"
"v_exp_f32 v50, v50\n"
"v_exp_f32 v51, v51\n"
"v_cvt_pk_bf16_f32 v96, v48, v49\n"
"v_cvt_pk_bf16_f32 v97, v50, v51\n"
"v_mfma_f32_32x32x16_bf16 v[64:79], a[100:103], v[100:103], v[64:79]\n"
"global_load_dwordx4 v[232:235], v0, s[64:65] offset:2048 sc1\n"
"v_exp_f32 v52, v52\n"
"v_exp_f32 v53, v53\n"
"v_exp_f32 v54, v54\n"
"v_exp_f32 v55, v55\n"
"v_cvt_pk_bf16_f32 v98, v52, v53\n"
"v_cvt_pk_bf16_f32 v99, v54, v55\n"
"v_mfma_f32_32x32x8_bf16 v[32:47], a[120:121], v[108:109], v[32:47]\n"
"ds_read_b128 v[88:91], v125 offset:5120\n"
"v_exp_f32 v56, v56\n"
"v_exp_f32 v57, v57\n"
"v_exp_f32 v58, v58\n"
"v_exp_f32 v59, v59\n"
"v_cvt_pk_bf16_f32 v100, v56, v57\n"
"v_cvt_pk_bf16_f32 v101, v58, v59\n"
"v_mfma_f32_32x32x16_bf16 v[64:79], a[104:107], v[96:99], v[64:79]\n"
"global_load_dwordx4 v[252:255], v0, s[64:65] offset:3072 sc1\n"
"v_exp_f32 v60, v60\n"
"v_exp_f32 v61, v61\n"
"v_exp_f32 v62, v62\n"
"v_exp_f32 v63, v63\n"
"v_cvt_pk_bf16_f32 v102, v60, v61\n"
"v_cvt_pk_bf16_f32 v103, v62, v63\n"
"v_mfma_f32_32x32x16_bf16 v[48:63], a[116:119], v[104:107], 0\n"
"global_load_dwordx4 a[240:243], v1, s[64:65] offset:0 sc1\n"
"v_exp_f32 v32, v32\n"
"v_exp_f32 v33, v33\n"
"v_exp_f32 v34, v34\n"
"v_exp_f32 v35, v35\n"
"v_cvt_pk_bf16_f32 v96, v32, v33\n"
"v_cvt_pk_bf16_f32 v97, v34, v35\n"
"v_mfma_f32_32x32x16_bf16 v[64:79], a[108:111], v[100:103], v[64:79]\n"
"s_add_u32 s64, s48, 40960\n"
"ds_read_b128 v[110:113], v126 offset:1536\n"
"v_exp_f32 v36, v36\n"
"v_exp_f32 v37, v37\n"
"v_exp_f32 v38, v38\n"
"v_exp_f32 v39, v39\n"
"v_cvt_pk_bf16_f32 v98, v36, v37\n"
"v_cvt_pk_bf16_f32 v99, v38, v39\n"
"v_mfma_f32_32x32x8_bf16 v[48:63], a[122:123], v[108:109], v[48:63]\n"
"ds_read_b64 v[114:115], v127 offset:1568\n"
"v_exp_f32 v40, v40\n"
"v_exp_f32 v41, v41\n"
"v_exp_f32 v42, v42\n"
"v_exp_f32 v43, v43\n"
"v_cvt_pk_bf16_f32 v100, v40, v41\n"
"v_cvt_pk_bf16_f32 v101, v42, v43\n"
"v_mfma_f32_32x32x16_bf16 v[64:79], a[124:127], v[96:99], v[64:79]\n"
".Lhop5:\n"
"s_cbranch_execz .Lhop9\n"
"global_load_dwordx4 v[196:199], v0, s[64:65] offset:0 sc1\n"
"v_exp_f32 v44, v44\n"
"v_exp_f32 v45, v45\n"
"v_exp_f32 v46, v46\n"
"v_exp_f32 v47, v47\n"
"v_cvt_pk_bf16_f32 v102, v44, v45\n"
"v_cvt_pk_bf16_f32 v103, v46, v47\n"
"v_mfma_f32_32x32x16_bf16 v[32:47], a[140:143], v[104:107], 0\n"
"global_load_dwordx4 v[216:219], v0, s[64:65] offset:1024 sc1\n"
"v_exp_f32 v48, v48\n"
"v_exp_f32 v49, v49\n"
"v_exp_f32 v50, v50\n"
"v_exp_f32 v51, v51\n"
"v_cvt_pk_bf16_f32 v96, v48, v49\n"
"v_cvt_pk_bf16_f32 v97, v50, v51\n"
"v_mfma_f32_32x32x16_bf16 v[64:79], a[128:131], v[100:103], v[64:79]\n"
"global_load_dwordx4 v[236:239], v0, s[64:65] offset:2048 sc1\n"
"v_exp_f32 v52, v52\n"
"v_exp_f32 v53, v53\n"
"v_exp_f32 v54, v54\n"
"v_exp_f32 v55, v55\n"
"v_cvt_pk_bf16_f32 v98, v52, v53\n"
"v_cvt_pk_bf16_f32 v99, v54, v55\n"
"v_mfma_f32_32x32x8_bf16 v[32:47], a[148:149], v[108:109], v[32:47]\n"
"v_exp_f32 v56, v56\n"
"v_exp_f32 v57, v57\n"
"v_exp_f32 v58, v58\n"
"v_exp_f32 v59, v59\n"
"v_cvt_pk_bf16_f32 v100, v56, v57\n"
"v_cvt_pk_bf16_f32 v101, v58, v59\n"
"v_mfma_f32_32x32x16_bf16 v[64:79], a[132:135], v[96:99], v[64:79]\n"
"global_load_dwordx4 a[224:227], v0, s[64:65] offset:3072 sc1\n"
"v_exp_f32 v60, v60\n"
"v_exp_f32 v61, v61\n"
"v_exp_f32 v62, v62\n"
"v_exp_f32 v63, v63\n"
"v_cvt_pk_bf16_f32 v102, v60, v61\n"
"v_cvt_pk_bf16_f32 v103, v62, v63\n"
"v_mfma_f32_32x32x16_bf16 v[48:63], a[144:147], v[104:107], 0\n"
"global_load_dwordx4 a[244:247], v1, s[64:65] offset:0 sc1\n"
"v_exp_f32 v32, v32\n"
"v_exp_f32 v33, v33\n"
"v_exp_f32 v34, v34\n"
"v_exp_f32 v35, v35\n"
"v_cvt_pk_bf16_f32 v96, v32, v33\n"
"v_cvt_pk_bf16_f32 v97, v34, v35\n"
"v_mfma_f32_32x32x16_bf16 v[64:79], a[136:139], v[100:103], v[64:79]\n"
"s_add_u32 s64, s48, 61440\n"
"v_exp_f32 v36, v36\n"
"v_exp_f32 v37, v37\n"
"v_exp_f32 v38, v38\n"
"v_exp_f32 v39, v39\n"
"v_cvt_pk_bf16_f32 v98, v36, v37\n"
"v_cvt_pk_bf16_f32 v99, v38, v39\n"
"v_mfma_f32_32x32x8_bf16 v[48:63], a[150:151], v[108:109], v[48:63]\n"
"v_exp_f32 v40, v40\n"
"v_exp_f32 v41, v41\n"
"v_exp_f32 v42, v42\n"
"v_exp_f32 v43, v43\n"
"v_cvt_pk_bf16_f32 v100, v40, v41\n"
"v_cvt_pk_bf16_f32 v101, v42, v43\n"
"v_mfma_f32_32x32x16_bf16 v[64:79], a[152:155], v[96:99], v[64:79]\n"
"global_load_dwordx4 v[200:203], v0, s[64:65] offset:0 sc1\n"
"v_exp_f32 v44, v44\n"
"v_exp_f32 v45, v45\n"
"v_exp_f32 v46, v46\n"
"v_exp_f32 v47, v47\n"
"v_cvt_pk_bf16_f32 v102, v44, v45\n"
"v_cvt_pk_bf16_f32 v103, v46, v47\n"
"v_mfma_f32_32x32x16_bf16 v[32:47], a[168:171], v[104:107], 0\n"
"global_load_dwordx4 v[220:223], v0, s[64:65] offset:1024 sc1\n"
"v_exp_f32 v48, v48\n"
"v_exp_f32 v49, v49\n"
"v_exp_f32 v50, v50\n"
"v_exp_f32 v51, v51\n"
"v_cvt_pk_bf16_f32 v96, v48, v49\n"
"v_cvt_pk_bf16_f32 v97, v50, v51\n"
"v_mfma_f32_32x32x16_bf16 v[64:79], a[156:159], v[100:103], v[64:79]\n"
"global_load_dwordx4 v[240:243], v0, s[64:65] offset:2048 sc1\n"
"v_exp_f32 v52, v52\n"
"v_exp_f32 v53, v53\n"
"v_exp_f32 v54, v54\n"
"v_exp_f32 v55, v55\n"
"v_cvt_pk_bf16_f32 v98, v52, v53\n"
"v_cvt_pk_bf16_f32 v99, v54, v55\n"
"v_mfma_f32_32x32x8_bf16 v[32:47], a[176:177], v[108:109], v[32:47]\n"
"v_exp_f32 v56, v56\n"
"v_exp_f32 v57, v57\n"
"v_exp_f32 v58, v58\n"
"v_exp_f32 v59, v59\n"
"v_cvt_pk_bf16_f32 v100, v56, v57\n"
"v_cvt_pk_bf16_f32 v101, v58, v59\n"
"v_mfma_f32_32x32x16_bf16 v[64:79], a[160:163], v[96:99], v[64:79]\n"
"global_load_dwordx4 a[228:231], v0, s[64:65] offset:3072 sc1\n"
"v_exp_f32 v60, v60\n"
"v_exp_f32 v61, v61\n"
"v_exp_f32 v62, v62\n"
"v_exp_f32 v63, v63\n"
"v_cvt_pk_bf16_f32 v102, v60, v61\n"
"v_cvt_pk_bf16_f32 v103, v62, v63\n"
"v_mfma_f32_32x32x16_bf16 v[48:63], a[172:175], v[104:107], 0\n"
"global_load_dwordx4 a[248:251], v1, s[64:65] offset:0 sc1\n"
"v_exp_f32 v32, v32\n"
"v_exp_f32 v33, v33\n"
"v_exp_f32 v34, v34\n"
"v_exp_f32 v35, v35\n"
"v_cvt_pk_bf16_f32 v96, v32, v33\n"
"v_cvt_pk_bf16_f32 v97, v34, v35\n"
"v_mfma_f32_32x32x16_bf16 v[64:79], a[164:167], v[100:103], v[64:79]\n"
"s_waitcnt lgkmcnt(0)\n"
"v_exp_f32 v36, v36\n"
"v_exp_f32 v37, v37\n"
"v_exp_f32 v38, v38\n"
"v_exp_f32 v39, v39\n"
"v_cvt_pk_bf16_f32 v98, v36, v37\n"
"v_cvt_pk_bf16_f32 v99, v38, v39\n"
"v_mfma_f32_32x32x8_bf16 v[48:63], a[178:179], v[108:109], v[48:63]\n"
"s_add_u32 s64, s48, 81920\n"
"v_exp_f32 v40, v40\n"
"v_exp_f32 v41, v41\n"
"v_exp_f32 v42, v42\n"
"v_exp_f32 v43, v43\n"
"v_cvt_pk_bf16_f32 v100, v40, v41\n"
"v_cvt_pk_bf16_f32 v101, v42, v43\n"
"v_mfma_f32_32x32x16_bf16 v[64:79], a[180:183], v[96:99], v[64:79]\n"
"global_load_dwordx4 v[204:207], v0, s[64:65] offset:0 sc1\n"
"v_exp_f32 v44, v44\n"
"v_exp_f32 v45, v45\n"
"v_exp_f32 v46, v46\n"
"v_exp_f32 v47, v47\n"
"v_cvt_pk_bf16_f32 v102, v44, v45\n"
"v_cvt_pk_bf16_f32 v103, v46, v47\n"
"v_mfma_f32_32x32x16_bf16 v[32:47], a[196:199], v[104:107], 0\n"
"global_load_dwordx4 v[224:227], v0, s[64:65] offset:1024 sc1\n"
"v_exp_f32 v48, v48\n"
"v_exp_f32 v49, v49\n"
"v_exp_f32 v50, v50\n"
"v_exp_f32 v51, v51\n"
"v_cvt_pk_bf16_f32 v96, v48, v49\n"
"v_cvt_pk_bf16_f32 v97, v50, v51\n"
"v_mfma_f32_32x32x16_bf16 v[64:79], a[184:187], v[100:103], v[64:79]\n"
"global_load_dwordx4 v[244:247], v0, s[64:65] offset:2048 sc1\n"
"v_exp_f32 v52, v52\n"
"v_exp_f32 v53, v53\n"
"v_exp_f32 v54, v54\n"
"v_exp_f32 v55, v55\n"
"v_cvt_pk_bf16_f32 v98, v52, v53\n"
"v_cvt_pk_bf16_f32 v99, v54, v55\n"
"v_mfma_f32_32x32x8_bf16 v[32:47], a[204:205], v[108:109], v[32:47]\n"
"v_exp_f32 v56, v56\n"
"v_exp_f32 v57, v57\n"
"v_exp_f32 v58, v58\n"
"v_exp_f32 v59, v59\n"
"v_cvt_pk_bf16_f32 v100, v56, v57\n"
"v_cvt_pk_bf16_f32 v101, v58, v59\n"
"v_mfma_f32_32x32x16_bf16 v[64:79], a[188:191], v[96:99], v[64:79]\n"
"global_load_dwordx4 a[232:235], v0, s[64:65] offset:3072 sc1\n"
"v_exp_f32 v60, v60\n"
"v_exp_f32 v61, v61\n"
"v_exp_f32 v62, v62\n"
"v_exp_f32 v63, v63\n"
"v_cvt_pk_bf16_f32 v102, v60, v61\n"
"v_cvt_pk_bf16_f32 v103, v62, v63\n"
"v_mfma_f32_32x32x16_bf16 v[48:63], a[200:203], v[104:107], 0\n"
"global_load_dwordx4 a[252:255], v1, s[64:65] offset:0 sc1\n"
"v_exp_f32 v32, v32\n"
"v_exp_f32 v33, v33\n"
"v_exp_f32 v34, v34\n"
"v_exp_f32 v35, v35\n"
"v_cvt_pk_bf16_f32 v96, v32, v33\n"
"v_cvt_pk_bf16_f32 v97, v34, v35\n"
"v_mfma_f32_32x32x16_bf16 v[64:79], a[192:195], v[100:103], v[64:79]\n"
"buffer_load_dwordx2 v[6:7], v24, s[24:27], 0 offen offset:0\n"
"v_exp_f32 v36, v36\n"
"v_exp_f32 v37, v37\n"
"v_exp_f32 v38, v38\n"
"v_exp_f32 v39, v39\n"
"v_cvt_pk_bf16_f32 v98, v36, v37\n"
"v_cvt_pk_bf16_f32 v99, v38, v39\n"
"v_mfma_f32_32x32x8_bf16 v[48:63], a[206:207], v[108:109], v[48:63]\n"
"v_exp_f32 v40, v40\n"
"v_exp_f32 v41, v41\n"
"v_exp_f32 v42, v42\n"
"v_exp_f32 v43, v43\n"
"v_cvt_pk_bf16_f32 v100, v40, v41\n"
"v_cvt_pk_bf16_f32 v101, v42, v43\n"
"v_mfma_f32_32x32x16_bf16 v[64:79], a[208:211], v[96:99], v[64:79]\n"
"buffer_load_dwordx2 v[8:9], v24, s[24:27], 0 offen offset:32\n"
"v_exp_f32 v44, v44\n"
"v_exp_f32 v45, v45\n"
"v_exp_f32 v46, v46\n"
"v_exp_f32 v47, v47\n"
"v_cvt_pk_bf16_f32 v102, v44, v45\n"
"v_cvt_pk_bf16_f32 v103, v46, v47\n"
"v_mfma_f32_32x32x16_bf16 v[32:47], a[0:3], v[110:113], 0\n"
"buffer_load_dwordx2 v[10:11], v24, s[24:27], s16 offen offset:64\n"
"v_exp_f32 v48, v48\n"
"v_exp_f32 v49, v49\n"
"v_exp_f32 v50, v50\n"
"v_exp_f32 v51, v51\n"
"v_cvt_pk_bf16_f32 v96, v48, v49\n"
"v_cvt_pk_bf16_f32 v97, v50, v51\n"
"v_mfma_f32_32x32x16_bf16 v[64:79], a[212:215], v[100:103], v[64:79]\n"
"buffer_load_dwordx2 v[12:13], v24, s[24:27], s17 offen offset:0\n"
"v_exp_f32 v52, v52\n"
"v_exp_f32 v53, v53\n"
"v_exp_f32 v54, v54\n"
"v_exp_f32 v55, v55\n"
"v_cvt_pk_bf16_f32 v98, v52, v53\n"
"v_cvt_pk_bf16_f32 v99, v54, v55\n"
"v_mfma_f32_32x32x8_bf16 v[32:47], a[8:9], v[114:115], v[32:47]\n"
"v_exp_f32 v56, v56\n"
"v_exp_f32 v57, v57\n"
"v_exp_f32 v58, v58\n"
"v_exp_f32 v59, v59\n"
"v_cvt_pk_bf16_f32 v100, v56, v57\n"
"v_cvt_pk_bf16_f32 v101, v58, v59\n"
"v_mfma_f32_32x32x16_bf16 v[64:79], a[216:219], v[96:99], v[64:79]\n"
"buffer_load_dwordx2 v[14:15], v24, s[24:27], s17 offen offset:32\n"
"v_exp_f32 v60, v60\n"
"v_exp_f32 v61, v61\n"
"v_exp_f32 v62, v62\n"
"v_exp_f32 v63, v63\n"
"v_cvt_pk_bf16_f32 v102, v60, v61\n"
"v_cvt_pk_bf16_f32 v103, v62, v63\n"
"v_mfma_f32_32x32x16_bf16 v[48:63], a[4:7], v[110:113], 0\n"
"buffer_load_dwordx2 v[16:17], v24, s[24:27], s10 offen offset:64\n"
"v_exp_f32 v32, v32\n"
"v_exp_f32 v33, v33\n"
"v_exp_f32 v34, v34\n"
"v_exp_f32 v35, v35\n"
"v_cvt_pk_bf16_f32 v96, v32, v33\n"
"v_cvt_pk_bf16_f32 v97, v34, v35\n"
"v_mfma_f32_32x32x16_bf16 v[64:79], a[220:223], v[100:103], v[64:79]\n"
"v_exp_f32 v36, v36\n"
"v_exp_f32 v37, v37\n"
"v_exp_f32 v38, v38\n"
"v_exp_f32 v39, v39\n"
"v_cvt_pk_bf16_f32 v98, v36, v37\n"
"v_cvt_pk_bf16_f32 v99, v38, v39\n"
"v_mfma_f32_32x32x8_bf16 v[48:63], a[10:11], v[114:115], v[48:63]\n"
"v_exp_f32 v40, v40\n"
"v_exp_f32 v41, v41\n"
"v_exp_f32 v42, v42\n"
"v_exp_f32 v43, v43\n"
"v_cvt_pk_bf16_f32 v100, v40, v41\n"
"v_cvt_pk_bf16_f32 v101, v42, v43\n"
"v_mfma_f32_32x32x16_bf16 v[80:95], a[12:15], v[96:99], v[80:95]\n"
"v_exp_f32 v44, v44\n"
"v_exp_f32 v45, v45\n"
"v_exp_f32 v46, v46\n"
"v_exp_f32 v47, v47\n"
"v_cvt_pk_bf16_f32 v102, v44, v45\n"
"v_cvt_pk_bf16_f32 v103, v46, v47\n"
"v_mfma_f32_32x32x16_bf16 v[32:47], a[28:31], v[110:113], 0\n"
"s_add_u32 s28, s54, 0\n"
"v_mov_b32 v29, v72\n"
"v_mov_b32 v30, v72\n"
"s_nop 1\n"
"v_permlane32_swap_b32 v29, v30\n"
"v_rcp_f32 v30, v30\n"
"v_exp_f32 v48, v48\n"
"v_exp_f32 v49, v49\n"
"v_exp_f32 v50, v50\n"
"v_exp_f32 v51, v51\n"
"v_cvt_pk_bf16_f32 v96, v48, v49\n"
"v_cvt_pk_bf16_f32 v97, v50, v51\n"
"v_mfma_f32_32x32x16_bf16 v[80:95], a[16:19], v[100:103], v[80:95]\n"
"v_mul_f32 v64, v30, v64\n"
"v_mul_f32 v65, v30, v65\n"
"v_mul_f32 v66, v30, v66\n"
"v_mul_f32 v67, v30, v67\n"
"v_exp_f32 v52, v52\n"
"v_exp_f32 v53, v53\n"
"v_exp_f32 v54, v54\n"
"v_exp_f32 v55, v55\n"
"v_cvt_pk_bf16_f32 v98, v52, v53\n"
"v_cvt_pk_bf16_f32 v99, v54, v55\n"
"v_mfma_f32_32x32x8_bf16 v[32:47], a[36:37], v[114:115], v[32:47]\n"
"v_cvt_pk_bf16_f32 v64, v64, v65\n"
"v_cvt_pk_bf16_f32 v65, v66, v67\n"
"v_exp_f32 v56, v56\n"
"v_exp_f32 v57, v57\n"
"v_exp_f32 v58, v58\n"
"v_exp_f32 v59, v59\n"
"v_cvt_pk_bf16_f32 v100, v56, v57\n"
"v_cvt_pk_bf16_f32 v101, v58, v59\n"
"v_mfma_f32_32x32x16_bf16 v[80:95], a[20:23], v[96:99], v[80:95]\n"
"global_store_dwordx2 v18, v[64:65], s[28:29] offset:0 sc1\n"
"v_mul_f32 v68, v30, v68\n"
"v_mul_f32 v69, v30, v69\n"
"v_mul_f32 v70, v30, v70\n"
"v_mul_f32 v71, v30, v71\n"
"v_exp_f32 v60, v60\n"
"v_exp_f32 v61, v61\n"
"v_exp_f32 v62, v62\n"
"v_exp_f32 v63, v63\n"
"v_cvt_pk_bf16_f32 v102, v60, v61\n"
"v_cvt_pk_bf16_f32 v103, v62, v63\n"
"v_mfma_f32_32x32x16_bf16 v[48:63], a[32:35], v[110:113], 0\n"
"v_cvt_pk_bf16_f32 v68, v68, v69\n"
"v_cvt_pk_bf16_f32 v69, v70, v71\n"
"v_exp_f32 v32, v32\n"
"v_exp_f32 v33, v33\n"
"v_exp_f32 v34, v34\n"
"v_exp_f32 v35, v35\n"
"v_cvt_pk_bf16_f32 v96, v32, v33\n"
"v_cvt_pk_bf16_f32 v97, v34, v35\n"
"v_mfma_f32_32x32x16_bf16 v[80:95], a[24:27], v[100:103], v[80:95]\n"
"global_store_dwordx2 v18, v[68:69], s[28:29] offset:256 sc1\n"
"v_exp_f32 v36, v36\n"
"v_exp_f32 v37, v37\n"
"v_exp_f32 v38, v38\n"
"v_exp_f32 v39, v39\n"
"v_cvt_pk_bf16_f32 v98, v36, v37\n"
"v_cvt_pk_bf16_f32 v99, v38, v39\n"
"v_mfma_f32_32x32x8_bf16 v[48:63], a[38:39], v[114:115], v[48:63]\n"
"v_exp_f32 v40, v40\n"
"v_exp_f32 v41, v41\n"
"v_exp_f32 v42, v42\n"
"v_exp_f32 v43, v43\n"
"v_cvt_pk_bf16_f32 v100, v40, v41\n"
"v_cvt_pk_bf16_f32 v101, v42, v43\n"
"v_mfma_f32_32x32x16_bf16 v[80:95], a[40:43], v[96:99], v[80:95]\n"
"s_sub_u32 s28, s28, s20\n"
"v_mul_f32 v72, v30, v72\n"
"v_mul_f32 v73, v30, v73\n"
"v_mul_f32 v74, v30, v74\n"
"v_mul_f32 v75, v30, v75\n"
"v_exp_f32 v44, v44\n"
"v_exp_f32 v45, v45\n"
"v_exp_f32 v46, v46\n"
"v_exp_f32 v47, v47\n"
"v_cvt_pk_bf16_f32 v102, v44, v45\n"
"v_cvt_pk_bf16_f32 v103, v46, v47\n"
"v_mfma_f32_32x32x16_bf16 v[32:47], a[56:59], v[110:113], 0\n"
"s_add_u32 s64, s72, s71\n"
"v_cvt_pk_bf16_f32 v72, v72, v73\n"
"v_cvt_pk_bf16_f32 v73, v74, v75\n"
"v_exp_f32 v48, v48\n"
"v_exp_f32 v49, v49\n"
"v_exp_f32 v50, v50\n"
"v_exp_f32 v51, v51\n"
"v_cvt_pk_bf16_f32 v96, v48, v49\n"
"v_cvt_pk_bf16_f32 v97, v50, v51\n"
"v_mfma_f32_32x32x16_bf16 v[80:95], a[44:47], v[100:103], v[80:95]\n"
"buffer_store_dwordx2 v[72:73], v19, s[20:23], s28 offen offset:512 sc1\n"
"v_exp_f32 v52, v52\n"
"v_exp_f32 v53, v53\n"
"v_exp_f32 v54, v54\n"
"v_exp_f32 v55, v55\n"
"v_cvt_pk_bf16_f32 v98, v52, v53\n"
"v_cvt_pk_bf16_f32 v99, v54, v55\n"
"v_mfma_f32_32x32x8_bf16 v[32:47], a[64:65], v[114:115], v[32:47]\n"
"v_exp_f32 v56, v56\n"
"v_exp_f32 v57, v57\n"
"v_exp_f32 v58, v58\n"
"v_exp_f32 v59, v59\n"
"v_cvt_pk_bf16_f32 v100, v56, v57\n"
"v_cvt_pk_bf16_f32 v101, v58, v59\n"
"v_mfma_f32_32x32x16_bf16 v[80:95], a[48:51], v[96:99], v[80:95]\n"
"global_load_dwordx4 a[0:3], v0, s[64:65] offset:0 sc1\n"
"v_exp_f32 v60, v60\n"
"v_exp_f32 v61, v61\n"
"v_exp_f32 v62, v62\n"
"v_exp_f32 v63, v63\n"
"v_cvt_pk_bf16_f32 v102, v60, v61\n"
"v_cvt_pk_bf16_f32 v103, v62, v63\n"
"v_mfma_f32_32x32x16_bf16 v[48:63], a[60:63], v[110:113], 0\n"
"global_load_dwordx4 a[4:7], v20, s[64:65] offset:0 sc1\n"
"v_exp_f32 v32, v32\n"
"v_exp_f32 v33, v33\n"
"v_exp_f32 v34, v34\n"
"v_exp_f32 v35, v35\n"
"v_cvt_pk_bf16_f32 v96, v32, v33\n"
"v_cvt_pk_bf16_f32 v97, v34, v35\n"
"v_mfma_f32_32x32x16_bf16 v[80:95], a[52:55], v[100:103], v[80:95]\n"
"global_load_dwordx4 a[8:11], v21, s[64:65] offset:0 sc1\n"
"v_exp_f32 v36, v36\n"
"v_exp_f32 v37, v37\n"
"v_exp_f32 v38, v38\n"
"v_exp_f32 v39, v39\n"
"v_cvt_pk_bf16_f32 v98, v36, v37\n"
"v_cvt_pk_bf16_f32 v99, v38, v39\n"
"v_mfma_f32_32x32x8_bf16 v[48:63], a[66:67], v[114:115], v[48:63]\n"
"v_exp_f32 v40, v40\n"
"v_exp_f32 v41, v41\n"
"v_exp_f32 v42, v42\n"
"v_exp_f32 v43, v43\n"
"v_cvt_pk_bf16_f32 v100, v40, v41\n"
"v_cvt_pk_bf16_f32 v101, v42, v43\n"
"v_mfma_f32_32x32x16_bf16 v[80:95], a[68:71], v[96:99], v[80:95]\n"
"global_load_dwordx4 a[12:15], v22, s[64:65] offset:0 sc1\n"
"v_exp_f32 v44, v44\n"
"v_exp_f32 v45, v45\n"
"v_exp_f32 v46, v46\n"
"v_exp_f32 v47, v47\n"
"v_cvt_pk_bf16_f32 v102, v44, v45\n"
"v_cvt_pk_bf16_f32 v103, v46, v47\n"
"v_mfma_f32_32x32x16_bf16 v[32:47], a[84:87], v[110:113], 0\n"
"global_load_dwordx4 a[16:19], v23, s[64:65] offset:0 sc1\n"
"v_exp_f32 v48, v48\n"
"v_exp_f32 v49, v49\n"
"v_exp_f32 v50, v50\n"
"v_exp_f32 v51, v51\n"
"v_cvt_pk_bf16_f32 v96, v48, v49\n"
"v_cvt_pk_bf16_f32 v97, v50, v51\n"
"v_mfma_f32_32x32x16_bf16 v[80:95], a[72:75], v[100:103], v[80:95]\n"
"s_add_u32 s64, s73, s71\n"
"v_exp_f32 v52, v52\n"
"v_exp_f32 v53, v53\n"
"v_exp_f32 v54, v54\n"
"v_exp_f32 v55, v55\n"
"v_cvt_pk_bf16_f32 v98, v52, v53\n"
"v_cvt_pk_bf16_f32 v99, v54, v55\n"
"v_mfma_f32_32x32x8_bf16 v[32:47], a[92:93], v[114:115], v[32:47]\n"
"v_exp_f32 v56, v56\n"
"v_exp_f32 v57, v57\n"
"v_exp_f32 v58, v58\n"
"v_exp_f32 v59, v59\n"
"v_cvt_pk_bf16_f32 v100, v56, v57\n"
"v_cvt_pk_bf16_f32 v101, v58, v59\n"
"v_mfma_f32_32x32x16_bf16 v[80:95], a[76:79], v[96:99], v[80:95]\n"
"global_load_dwordx4 a[20:23], v0, s[64:65] offset:0 sc1\n"
"v_exp_f32 v60, v60\n"
"v_exp_f32 v61, v61\n"
"v_exp_f32 v62, v62\n"
"v_exp_f32 v63, v63\n"
"v_cvt_pk_bf16_f32 v102, v60, v61\n"
"v_cvt_pk_bf16_f32 v103, v62, v63\n"
"v_mfma_f32_32x32x16_bf16 v[48:63], a[88:91], v[110:113], 0\n"
"s_barrier\n"
"v_exp_f32 v32, v32\n"
"v_exp_f32 v33, v33\n"
"v_exp_f32 v34, v34\n"
"v_exp_f32 v35, v35\n"
"v_cvt_pk_bf16_f32 v96, v32, v33\n"
"v_cvt_pk_bf16_f32 v97, v34, v35\n"
"v_mfma_f32_32x32x16_bf16 v[80:95], a[80:83], v[100:103], v[80:95]\n"
"global_load_dwordx4 a[24:27], v20, s[64:65] offset:0 sc1\n"
"v_exp_f32 v36, v36\n"
"v_exp_f32 v37, v37\n"
"v_exp_f32 v38, v38\n"
"v_exp_f32 v39, v39\n"
"v_cvt_pk_bf16_f32 v98, v36, v37\n"
"v_cvt_pk_bf16_f32 v99, v38, v39\n"
"v_mfma_f32_32x32x8_bf16 v[48:63], a[94:95], v[114:115], v[48:63]\n"
"v_exp_f32 v40, v40\n"
"v_exp_f32 v41, v41\n"
"v_exp_f32 v42, v42\n"
"v_exp_f32 v43, v43\n"
"v_cvt_pk_bf16_f32 v100, v40, v41\n"
"v_cvt_pk_bf16_f32 v101, v42, v43\n"
"v_mfma_f32_32x32x16_bf16 v[80:95], a[96:99], v[96:99], v[80:95]\n"
"global_load_dwordx4 a[28:31], v21, s[64:65] offset:0 sc1\n"
"v_exp_f32 v44, v44\n"
"v_exp_f32 v45, v45\n"
"v_exp_f32 v46, v46\n"
"v_exp_f32 v47, v47\n"
"v_cvt_pk_bf16_f32 v102, v44, v45\n"
"v_cvt_pk_bf16_f32 v103, v46, v47\n"
"v_mfma_f32_32x32x16_bf16 v[32:47], a[112:115], v[110:113], 0\n"
"global_load_dwordx4 a[32:35], v22, s[64:65] offset:0 sc1\n"
"v_exp_f32 v48, v48\n"
"v_exp_f32 v49, v49\n"
"v_exp_f32 v50, v50\n"
"v_exp_f32 v51, v51\n"
"v_cvt_pk_bf16_f32 v96, v48, v49\n"
"v_cvt_pk_bf16_f32 v97, v50, v51\n"
"v_mfma_f32_32x32x16_bf16 v[80:95], a[100:103], v[100:103], v[80:95]\n"
"global_load_dwordx4 a[36:39], v23, s[64:65] offset:0 sc1\n"
"v_exp_f32 v52, v52\n"
"v_exp_f32 v53, v53\n"
"v_exp_f32 v54, v54\n"
"v_exp_f32 v55, v55\n"
"v_cvt_pk_bf16_f32 v98, v52, v53\n"
"v_cvt_pk_bf16_f32 v99, v54, v55\n"
"v_mfma_f32_32x32x8_bf16 v[32:47], a[120:121], v[114:115], v[32:47]\n"
"s_add_u32 s64, s74, s71\n"
"v_exp_f32 v56, v56\n"
"v_exp_f32 v57, v57\n"
"v_exp_f32 v58, v58\n"
"v_exp_f32 v59, v59\n"
"v_cvt_pk_bf16_f32 v100, v56, v57\n"
"v_cvt_pk_bf16_f32 v101, v58, v59\n"
"v_mfma_f32_32x32x16_bf16 v[80:95], a[104:107], v[96:99], v[80:95]\n"
"global_load_dwordx4 a[40:43], v0, s[64:65] offset:0 sc1\n"
"v_exp_f32 v60, v60\n"
"v_exp_f32 v61, v61\n"
"v_exp_f32 v62, v62\n"
"v_exp_f32 v63, v63\n"
"v_cvt_pk_bf16_f32 v102, v60, v61\n"
"v_cvt_pk_bf16_f32 v103, v62, v63\n"
"v_mfma_f32_32x32x16_bf16 v[48:63], a[116:119], v[110:113], 0\n"
"global_load_dwordx4 a[44:47], v20, s[64:65] offset:0 sc1\n"
"v_exp_f32 v32, v32\n"
"v_exp_f32 v33, v33\n"
"v_exp_f32 v34, v34\n"
"v_exp_f32 v35, v35\n"
"v_cvt_pk_bf16_f32 v96, v32, v33\n"
"v_cvt_pk_bf16_f32 v97, v34, v35\n"
"v_mfma_f32_32x32x16_bf16 v[80:95], a[108:111], v[100:103], v[80:95]\n"
"global_load_dwordx4 a[48:51], v21, s[64:65] offset:0 sc1\n"
"v_exp_f32 v36, v36\n"
"v_exp_f32 v37, v37\n"
"v_exp_f32 v38, v38\n"
"v_exp_f32 v39, v39\n"
"v_cvt_pk_bf16_f32 v98, v36, v37\n"
"v_cvt_pk_bf16_f32 v99, v38, v39\n"
"v_mfma_f32_32x32x8_bf16 v[48:63], a[122:123], v[114:115], v[48:63]\n"
"v_exp_f32 v40, v40\n"
"v_exp_f32 v41, v41\n"
"v_exp_f32 v42, v42\n"
"v_exp_f32 v43, v43\n"
"v_cvt_pk_bf16_f32 v100, v40, v41\n"
"v_cvt_pk_bf16_f32 v101, v42, v43\n"
"v_mfma_f32_32x32x16_bf16 v[80:95], a[124:127], v[96:99], v[80:95]\n"
"global_load_dwordx4 a[52:55], v22, s[64:65] offset:0 sc1\n"
"v_exp_f32 v44, v44\n"
"v_exp_f32 v45, v45\n"
"v_exp_f32 v46, v46\n"
"v_exp_f32 v47, v47\n"
"v_cvt_pk_bf16_f32 v102, v44, v45\n"
"v_cvt_pk_bf16_f32 v103, v46, v47\n"
"v_mfma_f32_32x32x16_bf16 v[32:47], a[140:143], v[110:113], 0\n"
"global_load_dwordx4 a[56:59], v23, s[64:65] offset:0 sc1\n"
"v_exp_f32 v48, v48\n"
"v_exp_f32 v49, v49\n"
"v_exp_f32 v50, v50\n"
"v_exp_f32 v51, v51\n"
"v_cvt_pk_bf16_f32 v96, v48, v49\n"
"v_cvt_pk_bf16_f32 v97, v50, v51\n"
"v_mfma_f32_32x32x16_bf16 v[80:95], a[128:131], v[100:103], v[80:95]\n"
"s_add_u32 s64, s75, s71\n"
"v_exp_f32 v52, v52\n"
"v_exp_f32 v53, v53\n"
"v_exp_f32 v54, v54\n"
"v_exp_f32 v55, v55\n"
"v_cvt_pk_bf16_f32 v98, v52, v53\n"
"v_cvt_pk_bf16_f32 v99, v54, v55\n"
"v_mfma_f32_32x32x8_bf16 v[32:47], a[148:149], v[114:115], v[32:47]\n"
"v_exp_f32 v56, v56\n"
"v_exp_f32 v57, v57\n"
"v_exp_f32 v58, v58\n"
"v_exp_f32 v59, v59\n"
"v_cvt_pk_bf16_f32 v100, v56, v57\n"
"v_cvt_pk_bf16_f32 v101, v58, v59\n"
"v_mfma_f32_32x32x16_bf16 v[80:95], a[132:135], v[96:99], v[80:95]\n"
"global_load_dwordx4 a[60:63], v0, s[64:65] offset:0 sc1\n"
"v_exp_f32 v60, v60\n"
"v_exp_f32 v61, v61\n"
"v_exp_f32 v62, v62\n"
"v_exp_f32 v63, v63\n"
"v_cvt_pk_bf16_f32 v102, v60, v61\n"
"v_cvt_pk_bf16_f32 v103, v62, v63\n"
"v_mfma_f32_32x32x16_bf16 v[48:63], a[144:147], v[110:113], 0\n"
"s_add_u32 s28, s55, 0\n"
"v_exp_f32 v32, v32\n"
"v_exp_f32 v33, v33\n"
"v_exp_f32 v34, v34\n"
"v_exp_f32 v35, v35\n"
"v_cvt_pk_bf16_f32 v96, v32, v33\n"
"v_cvt_pk_bf16_f32 v97, v34, v35\n"
"v_mfma_f32_32x32x16_bf16 v[80:95], a[136:139], v[100:103], v[80:95]\n"
"global_load_dwordx4 v[128:131], v0, s[28:29] offset:0 sc1\n"
"v_exp_f32 v36, v36\n"
"v_exp_f32 v37, v37\n"
"v_exp_f32 v38, v38\n"
"v_exp_f32 v39, v39\n"
"v_cvt_pk_bf16_f32 v98, v36, v37\n"
"v_cvt_pk_bf16_f32 v99, v38, v39\n"
"v_mfma_f32_32x32x8_bf16 v[48:63], a[150:151], v[114:115], v[48:63]\n"
"v_exp_f32 v40, v40\n"
"v_exp_f32 v41, v41\n"
"v_exp_f32 v42, v42\n"
"v_exp_f32 v43, v43\n"
"v_cvt_pk_bf16_f32 v100, v40, v41\n"
"v_cvt_pk_bf16_f32 v101, v42, v43\n"
"v_mfma_f32_32x32x16_bf16 v[80:95], a[152:155], v[96:99], v[80:95]\n"
"global_load_dwordx4 v[132:135], v0, s[28:29] offset:1024 sc1\n"
"v_exp_f32 v44, v44\n"
"v_exp_f32 v45, v45\n"
"v_exp_f32 v46, v46\n"
"v_exp_f32 v47, v47\n"
"v_cvt_pk_bf16_f32 v102, v44, v45\n"
"v_cvt_pk_bf16_f32 v103, v46, v47\n"
"v_mfma_f32_32x32x16_bf16 v[32:47], a[168:171], v[110:113], 0\n"
"global_load_dwordx4 v[136:139], v0, s[28:29] offset:2048 sc1\n"
"v_exp_f32 v48, v48\n"
"v_exp_f32 v49, v49\n"
"v_exp_f32 v50, v50\n"
"v_exp_f32 v51, v51\n"
"v_cvt_pk_bf16_f32 v96, v48, v49\n"
"v_cvt_pk_bf16_f32 v97, v50, v51\n"
"v_mfma_f32_32x32x16_bf16 v[80:95], a[156:159], v[100:103], v[80:95]\n"
"global_load_dwordx4 v[140:143], v0, s[28:29] offset:3072 sc1\n"
"v_exp_f32 v52, v52\n"
"v_exp_f32 v53, v53\n"
"v_exp_f32 v54, v54\n"
"v_exp_f32 v55, v55\n"
"v_cvt_pk_bf16_f32 v98, v52, v53\n"
"v_cvt_pk_bf16_f32 v99, v54, v55\n"
"v_mfma_f32_32x32x8_bf16 v[32:47], a[176:177], v[114:115], v[32:47]\n"
"v_exp_f32 v56, v56\n"
"v_exp_f32 v57, v57\n"
"v_exp_f32 v58, v58\n"
"v_exp_f32 v59, v59\n"
"v_cvt_pk_bf16_f32 v100, v56, v57\n"
"v_cvt_pk_bf16_f32 v101, v58, v59\n"
"v_mfma_f32_32x32x16_bf16 v[80:95], a[160:163], v[96:99], v[80:95]\n"
"global_load_dwordx4 v[144:147], v1, s[28:29] offset:0 sc1\n"
"v_exp_f32 v60, v60\n"
"v_exp_f32 v61, v61\n"
"v_exp_f32 v62, v62\n"
"v_exp_f32 v63, v63\n"
"v_cvt_pk_bf16_f32 v102, v60, v61\n"
"v_cvt_pk_bf16_f32 v103, v62, v63\n"
"v_mfma_f32_32x32x16_bf16 v[48:63], a[172:175], v[110:113], 0\n"
"s_add_u32 s28, s55, 20480\n"
"v_exp_f32 v32, v32\n"
"v_exp_f32 v33, v33\n"
"v_exp_f32 v34, v34\n"
"v_exp_f32 v35, v35\n"
"v_cvt_pk_bf16_f32 v96, v32, v33\n"
"v_cvt_pk_bf16_f32 v97, v34, v35\n"
"v_mfma_f32_32x32x16_bf16 v[80:95], a[164:167], v[100:103], v[80:95]\n"
"global_load_dwordx4 v[148:151], v0, s[28:29] offset:0 sc1\n"
"v_exp_f32 v36, v36\n"
"v_exp_f32 v37, v37\n"
"v_exp_f32 v38, v38\n"
"v_exp_f32 v39, v39\n"
"v_cvt_pk_bf16_f32 v98, v36, v37\n"
"v_cvt_pk_bf16_f32 v99, v38, v39\n"
"v_mfma_f32_32x32x8_bf16 v[48:63], a[178:179], v[114:115], v[48:63]\n"
"v_exp_f32 v40, v40\n"
"v_exp_f32 v41, v41\n"
"v_exp_f32 v42, v42\n"
"v_exp_f32 v43, v43\n"
"v_cvt_pk_bf16_f32 v100, v40, v41\n"
"v_cvt_pk_bf16_f32 v101, v42, v43\n"
"v_mfma_f32_32x32x16_bf16 v[80:95], a[180:183], v[96:99], v[80:95]\n"
"global_load_dwordx4 v[152:155], v0, s[28:29] offset:1024 sc1\n"
"v_exp_f32 v44, v44\n"
"v_exp_f32 v45, v45\n"
"v_exp_f32 v46, v46\n"
"v_exp_f32 v47, v47\n"
"v_cvt_pk_bf16_f32 v102, v44, v45\n"
"v_cvt_pk_bf16_f32 v103, v46, v47\n"
"v_mfma_f32_32x32x16_bf16 v[32:47], a[196:199], v[110:113], 0\n"
"global_load_dwordx4 v[156:159], v0, s[28:29] offset:2048 sc1\n"
"v_exp_f32 v48, v48\n"
"v_exp_f32 v49, v49\n"
"v_exp_f32 v50, v50\n"
"v_exp_f32 v51, v51\n"
"v_cvt_pk_bf16_f32 v96, v48, v49\n"
"v_cvt_pk_bf16_f32 v97, v50, v51\n"
"v_mfma_f32_32x32x16_bf16 v[80:95], a[184:187], v[100:103], v[80:95]\n"
"global_load_dwordx4 v[160:163], v0, s[28:29] offset:3072 sc1\n"
"v_exp_f32 v52, v52\n"
"v_exp_f32 v53, v53\n"
"v_exp_f32 v54, v54\n"
"v_exp_f32 v55, v55\n"
"v_cvt_pk_bf16_f32 v98, v52, v53\n"
"v_cvt_pk_bf16_f32 v99, v54, v55\n"
"v_mfma_f32_32x32x8_bf16 v[32:47], a[204:205], v[114:115], v[32:47]\n"
"v_exp_f32 v56, v56\n"
"v_exp_f32 v57, v57\n"
"v_exp_f32 v58, v58\n"
"v_exp_f32 v59, v59\n"
"v_cvt_pk_bf16_f32 v100, v56, v57\n"
"v_cvt_pk_bf16_f32 v101, v58, v59\n"
"v_mfma_f32_32x32x16_bf16 v[80:95], a[188:191], v[96:99], v[80:95]\n"
"global_load_dwordx4 v[164:167], v1, s[28:29] offset:0 sc1\n"
"v_exp_f32 v60, v60\n"
"v_exp_f32 v61, v61\n"
"v_exp_f32 v62, v62\n"
"v_exp_f32 v63, v63\n"
"v_cvt_pk_bf16_f32 v102, v60, v61\n"
"v_cvt_pk_bf16_f32 v103, v62, v63\n"
"v_mfma_f32_32x32x16_bf16 v[48:63], a[200:203], v[110:113], 0\n"
"global_load_dwordx4 a[64:67], v20, s[64:65] offset:0 sc1\n"
"v_exp_f32 v32, v32\n"
"v_exp_f32 v33, v33\n"
"v_exp_f32 v34, v34\n"
"v_exp_f32 v35, v35\n"
"v_cvt_pk_bf16_f32 v96, v32, v33\n"
"v_cvt_pk_bf16_f32 v97, v34, v35\n"
"v_mfma_f32_32x32x16_bf16 v[80:95], a[192:195], v[100:103], v[80:95]\n"
"global_load_dwordx4 a[68:71], v21, s[64:65] offset:0 sc1\n"
"v_exp_f32 v36, v36\n"
"v_exp_f32 v37, v37\n"
"v_exp_f32 v38, v38\n"
"v_exp_f32 v39, v39\n"
"v_cvt_pk_bf16_f32 v98, v36, v37\n"
"v_cvt_pk_bf16_f32 v99, v38, v39\n"
"v_mfma_f32_32x32x8_bf16 v[48:63], a[206:207], v[114:115], v[48:63]\n"
".Lhop6:\n"
"s_cbranch_execz .Lhop10\n"
"v_exp_f32 v40, v40\n"
"v_exp_f32 v41, v41\n"
"v_exp_f32 v42, v42\n"
"v_exp_f32 v43, v43\n"
"v_cvt_pk_bf16_f32 v100, v40, v41\n"
"v_cvt_pk_bf16_f32 v101, v42, v43\n"
"v_mfma_f32_32x32x16_bf16 v[80:95], a[208:211], v[96:99], v[80:95]\n"
"global_load_dwordx4 a[72:75], v22, s[64:65] offset:0 sc1\n"
"v_exp_f32 v44, v44\n"
"v_exp_f32 v45, v45\n"
"v_exp_f32 v46, v46\n"
"v_exp_f32 v47, v47\n"
"v_cvt_pk_bf16_f32 v102, v44, v45\n"
"v_cvt_pk_bf16_f32 v103, v46, v47\n"
"global_load_dwordx4 a[76:79], v23, s[64:65] offset:0 sc1\n"
"v_exp_f32 v48, v48\n"
"v_exp_f32 v49, v49\n"
"v_exp_f32 v50, v50\n"
"v_exp_f32 v51, v51\n"
"v_cvt_pk_bf16_f32 v96, v48, v49\n"
"v_cvt_pk_bf16_f32 v97, v50, v51\n"
"v_mfma_f32_32x32x16_bf16 v[80:95], a[212:215], v[100:103], v[80:95]\n"
"s_add_u32 s64, s76, s71\n"
"v_exp_f32 v52, v52\n"
"v_exp_f32 v53, v53\n"
"v_exp_f32 v54, v54\n"
"v_exp_f32 v55, v55\n"
"v_cvt_pk_bf16_f32 v98, v52, v53\n"
"v_cvt_pk_bf16_f32 v99, v54, v55\n"
"v_exp_f32 v56, v56\n"
"v_exp_f32 v57, v57\n"
"v_exp_f32 v58, v58\n"
"v_exp_f32 v59, v59\n"
"v_cvt_pk_bf16_f32 v100, v56, v57\n"
"v_cvt_pk_bf16_f32 v101, v58, v59\n"
"v_mfma_f32_32x32x16_bf16 v[80:95], a[216:219], v[96:99], v[80:95]\n"
"global_load_dwordx4 a[80:83], v0, s[64:65] offset:0 sc1\n"
"v_exp_f32 v60, v60\n"
"v_exp_f32 v61, v61\n"
"v_exp_f32 v62, v62\n"
"v_exp_f32 v63, v63\n"
"v_cvt_pk_bf16_f32 v102, v60, v61\n"
"v_cvt_pk_bf16_f32 v103, v62, v63\n"
"s_nop 1\n"
"v_mfma_f32_32x32x16_bf16 v[80:95], a[220:223], v[100:103], v[80:95]\n"
"global_load_dwordx4 a[84:87], v20, s[64:65] offset:0 sc1\n"
"global_load_dwordx4 a[88:91], v21, s[64:65] offset:0 sc1\n"
"s_add_u32 s28, s54, 40960\n"
"s_nop 8\n"
"v_mov_b32 v29, v88\n"
"v_mov_b32 v30, v88\n"
"s_nop 1\n"
"v_permlane32_swap_b32 v29, v30\n"
"v_rcp_f32 v30, v30\n"
"s_nop 0\n"
"v_pk_mul_f32 v[80:81], v[80:81], v[30:31] op_sel_hi:[1,0]\n"
"v_pk_mul_f32 v[82:83], v[82:83], v[30:31] op_sel_hi:[1,0]\n"
"v_cvt_pk_bf16_f32 v80, v80, v81\n"
"v_cvt_pk_bf16_f32 v81, v82, v83\n"
"global_store_dwordx2 v18, v[80:81], s[28:29] offset:0 sc1\n"
"v_pk_mul_f32 v[84:85], v[84:85], v[30:31] op_sel_hi:[1,0]\n"
"v_pk_mul_f32 v[86:87], v[86:87], v[30:31] op_sel_hi:[1,0]\n"
"v_cvt_pk_bf16_f32 v84, v84, v85\n"
"v_cvt_pk_bf16_f32 v85, v86, v87\n"
"global_store_dwordx2 v18, v[84:85], s[28:29] offset:256 sc1\n"
"s_sub_u32 s28, s28, s20\n"
"v_pk_mul_f32 v[88:89], v[88:89], v[30:31] op_sel_hi:[1,0]\n"
"v_pk_mul_f32 v[90:91], v[90:91], v[30:31] op_sel_hi:[1,0]\n"
"v_cvt_pk_bf16_f32 v88, v88, v89\n"
"v_cvt_pk_bf16_f32 v89, v90, v91\n"
"buffer_store_dwordx2 v[88:89], v19, s[20:23], s28 offen offset:512 sc1\n"
"s_waitcnt vmcnt(39)\n"
"s_waitcnt vmcnt(15)\n"
"v_mfma_f32_16x16x32_bf16 v[72:75], v[188:191], v[128:131], 0\n"
"global_load_dwordx4 a[92:95], v22, s[64:65] offset:0 sc1\n"
"v_mfma_f32_16x16x32_bf16 v[72:75], v[208:211], v[132:135], v[72:75]\n"
"v_max_u32 v29, v128, v129\n"
"s_lshl1_add_u32 s6, s86, 122880\n"
"v_mfma_f32_16x16x32_bf16 v[72:75], v[228:231], v[136:139], v[72:75]\n"
"v_max3_u32 v29, v130, v131, v29\n"
"v_lshrrev_b32 v120, 2, v2\n"
"v_mfma_f32_16x16x32_bf16 v[72:75], v[248:251], v[140:143], v[72:75]\n"
"global_load_dwordx4 a[96:99], v23, s[64:65] offset:0 sc1\n"
"v_mfma_f32_16x16x32_bf16 v[72:75], a[236:239], v[144:147], v[72:75]\n"
"s_add_u32 s64, s77, s71\n"
"v_mfma_f32_16x16x32_bf16 v[76:79], v[192:195], v[128:131], 0\n"
"v_max3_u32 v29, v132, v133, v29\n"
"v_and_b32 v120, 8, v120\n"
"v_mfma_f32_16x16x32_bf16 v[76:79], v[212:215], v[132:135], v[76:79]\n"
"global_load_dwordx4 a[100:103], v0, s[64:65] offset:0 sc1\n"
"v_mfma_f32_16x16x32_bf16 v[76:79], v[232:235], v[136:139], v[76:79]\n"
"v_max3_u32 v29, v134, v135, v29\n"
"v_bfe_u32 v121, v2, 4, 1\n"
"v_mfma_f32_16x16x32_bf16 v[76:79], v[252:255], v[140:143], v[76:79]\n"
"v_max3_u32 v29, v136, v137, v29\n"
"v_xor_b32 v120, v120, v2\n"
"v_mfma_f32_16x16x32_bf16 v[76:79], a[240:243], v[144:147], v[76:79]\n"
"global_load_dwordx4 a[104:107], v20, s[64:65] offset:0 sc1\n"
"v_mfma_f32_16x16x32_bf16 v[80:83], v[196:199], v[128:131], 0\n"
"v_max3_u32 v29, v138, v139, v29\n"
"v_xor_b32 v120, v120, v121\n"
"v_mfma_f32_16x16x32_bf16 v[80:83], v[216:219], v[132:135], v[80:83]\n"
"v_max3_u32 v29, v140, v141, v29\n"
"v_lshl_add_u32 v25, v120, 4, s6\n"
"v_mfma_f32_16x16x32_bf16 v[80:83], v[236:239], v[136:139], v[80:83]\n"
"global_load_dwordx4 a[108:111], v21, s[64:65] offset:0 sc1\n"
"v_mfma_f32_16x16x32_bf16 v[80:83], a[224:227], v[140:143], v[80:83]\n"
"v_max3_u32 v29, v142, v143, v29\n"
"v_and_b32 v120, 2, v2\n"
"v_mfma_f32_16x16x32_bf16 v[80:83], a[244:247], v[144:147], v[80:83]\n"
"v_max3_u32 v29, v144, v145, v29\n"
"v_and_b32 v121, 1, v2\n"
"v_mfma_f32_16x16x32_bf16 v[84:87], v[200:203], v[128:131], 0\n"
"ds_write_b128 v25, v[72:75] offset:0\n"
"v_mfma_f32_16x16x32_bf16 v[84:87], v[220:223], v[132:135], v[84:87]\n"
"global_load_dwordx4 a[112:115], v22, s[64:65] offset:0 sc1\n"
"v_mfma_f32_16x16x32_bf16 v[84:87], v[240:243], v[136:139], v[84:87]\n"
"ds_write_b128 v25, v[76:79] offset:1024\n"
"v_mfma_f32_16x16x32_bf16 v[84:87], a[228:231], v[140:143], v[84:87]\n"
"v_max3_u32 v29, v146, v147, v29\n"
"v_lshl_add_u32 v120, v120, 2, v121\n"
"v_mfma_f32_16x16x32_bf16 v[84:87], a[248:251], v[144:147], v[84:87]\n"
"ds_write_b128 v25, v[80:83] offset:2048\n"
"v_mfma_f32_16x16x32_bf16 v[88:91], v[204:207], v[128:131], 0\n"
"global_load_dwordx4 a[116:119], v23, s[64:65] offset:0 sc1\n"
"v_mfma_f32_16x16x32_bf16 v[88:91], v[224:227], v[132:135], v[88:91]\n"
"s_add_u32 s64, s78, s71\n"
"v_mfma_f32_16x16x32_bf16 v[88:91], v[244:247], v[136:139], v[88:91]\n"
"v_lshrrev_b32 v121, 2, v2\n"
"v_xor_b32 v120, v120, v121\n"
"v_mfma_f32_16x16x32_bf16 v[88:91], a[232:235], v[140:143], v[88:91]\n"
"ds_write_b128 v25, v[84:87] offset:3072\n"
"v_mfma_f32_16x16x32_bf16 v[88:91], a[252:255], v[144:147], v[88:91]\n"
"global_load_dwordx4 a[120:123], v0, s[64:65] offset:0 sc1\n"
"s_waitcnt vmcnt(18)\n"
"v_mfma_f32_16x16x32_bf16 v[92:95], v[188:191], v[148:151], 0\n"
"v_max3_u32 v29, v148, v149, v29\n"
"v_and_b32 v121, 3, v2\n"
"v_mfma_f32_16x16x32_bf16 v[92:95], v[208:211], v[152:155], v[92:95]\n"
"v_max3_u32 v29, v150, v151, v29\n"
"v_lshl_add_u32 v120, v121, 4, v120\n"
"v_mfma_f32_16x16x32_bf16 v[92:95], v[228:231], v[156:159], v[92:95]\n"
"global_load_dwordx4 a[124:127], v20, s[64:65] offset:0 sc1\n"
"v_mfma_f32_16x16x32_bf16 v[92:95], v[248:251], v[160:163], v[92:95]\n"
"ds_write_b128 v25, v[88:91] offset:4096\n"
"v_mfma_f32_16x16x32_bf16 v[92:95], a[236:239], v[164:167], v[92:95]\n"
"v_max3_u32 v29, v152, v153, v29\n"
"s_and_b32 s7, s56, 1\n"
"v_mfma_f32_16x16x32_bf16 v[96:99], v[192:195], v[148:151], 0\n"
"global_load_dwordx4 a[128:131], v21, s[64:65] offset:0 sc1\n"
"v_mfma_f32_16x16x32_bf16 v[96:99], v[212:215], v[152:155], v[96:99]\n"
"v_max3_u32 v29, v154, v155, v29\n"
"s_mul_i32 s7, s7, 5120\n"
"v_mfma_f32_16x16x32_bf16 v[96:99], v[232:235], v[156:159], v[96:99]\n"
"v_max3_u32 v29, v156, v157, v29\n"
"s_lshr_b32 s8, s56, 1\n"
"v_mfma_f32_16x16x32_bf16 v[96:99], v[252:255], v[160:163], v[96:99]\n"
"ds_write_b128 v25, v[92:95] offset:5120\n"
"v_mfma_f32_16x16x32_bf16 v[96:99], a[240:243], v[164:167], v[96:99]\n"
"global_load_dwordx4 a[132:135], v22, s[64:65] offset:0 sc1\n"
"v_mfma_f32_16x16x32_bf16 v[100:103], v[196:199], v[148:151], 0\n"
"v_max3_u32 v29, v158, v159, v29\n"
"s_mul_i32 s8, s8, 3072\n"
"v_mfma_f32_16x16x32_bf16 v[100:103], v[216:219], v[152:155], v[100:103]\n"
"v_max3_u32 v29, v160, v161, v29\n"
"s_add_u32 s7, s7, s8\n"
"v_mfma_f32_16x16x32_bf16 v[100:103], v[236:239], v[156:159], v[100:103]\n"
"global_load_dwordx4 a[136:139], v23, s[64:65] offset:0 sc1\n"
"v_mfma_f32_16x16x32_bf16 v[100:103], a[224:227], v[160:163], v[100:103]\n"
"ds_write_b128 v25, v[96:99] offset:6144\n"
"v_mfma_f32_16x16x32_bf16 v[100:103], a[244:247], v[164:167], v[100:103]\n"
"v_max3_u32 v29, v162, v163, v29\n"
"s_add_u32 s7, s7, 122880\n"
"v_mfma_f32_16x16x32_bf16 v[104:107], v[200:203], v[148:151], 0\n"
"v_max3_u32 v29, v164, v165, v29\n"
"v_lshl_add_u32 v26, v120, 4, s7\n"
"v_mfma_f32_16x16x32_bf16 v[104:107], v[220:223], v[152:155], v[104:107]\n"
"v_max3_u32 v29, v166, v167, v29\n"
"v_lshrrev_b32 v28, 2, v2\n"
"v_mfma_f32_16x16x32_bf16 v[104:107], v[240:243], v[156:159], v[104:107]\n"
"v_cmp_eq_u32 vcc, 0xffbfffbf, v29\n"
"v_and_b32 v120, 2, v2\n"
"v_mfma_f32_16x16x32_bf16 v[104:107], a[228:231], v[160:163], v[104:107]\n"
"ds_write_b128 v25, v[100:103] offset:7168\n"
"v_mfma_f32_16x16x32_bf16 v[104:107], a[248:251], v[164:167], v[104:107]\n"
"v_and_b32 v121, 1, v2\n"
"v_lshlrev_b32 v27, 7, v120\n"
"v_mfma_f32_16x16x32_bf16 v[108:111], v[204:207], v[148:151], 0\n"
"v_lshl_add_u32 v27, v121, 3, v27\n"
"v_lshl_add_u32 v27, v28, 4, v27\n"
"v_mfma_f32_16x16x32_bf16 v[108:111], v[224:227], v[152:155], v[108:111]\n"
"v_lshlrev_b32 v28, 4, v28\n"
"s_sub_u32 s9, s59, s20\n"
"v_mfma_f32_16x16x32_bf16 v[108:111], v[244:247], v[156:159], v[108:111]\n"
"s_add_u32 s9, s9, s16\n"
"s_add_u32 s10, s9, 40960\n"
"v_mfma_f32_16x16x32_bf16 v[108:111], a[232:235], v[160:163], v[108:111]\n"
"ds_write_b128 v25, v[104:107] offset:8192\n"
"v_mfma_f32_16x16x32_bf16 v[108:111], a[252:255], v[164:167], v[108:111]\n"
"s_add_u32 s64, s55, 40960\n"
"v_mov_b32 v114, 0x3f803f80\n"
"global_load_dwordx4 v[32:35], v0, s[64:65] offset:0 sc1\n"
"global_load_dwordx4 v[36:39], v0, s[64:65] offset:1024 sc1\n"
"global_load_dwordx4 v[40:43], v0, s[64:65] offset:2048 sc1\n"
"global_load_dwordx4 v[44:47], v0, s[64:65] offset:3072 sc1\n"
"s_nop 1\n"
"ds_write_b128 v25, v[108:111] offset:9216\n"
"global_load_dwordx4 v[48:51], v1, s[64:65] offset:0 sc1\n"
"s_add_u32 s64, s55, 61440\n"
"global_load_dwordx4 v[52:55], v0, s[64:65] offset:0 sc1\n"
"global_load_dwordx4 v[56:59], v0, s[64:65] offset:1024 sc1\n"
"global_load_dwordx4 v[60:63], v0, s[64:65] offset:2048 sc1\n"
"global_load_dwordx4 v[64:67], v0, s[64:65] offset:3072 sc1\n"
"global_load_dwordx4 v[68:71], v1, s[64:65] offset:0 sc1\n"
"s_cbranch_vccnz .Lre_eo\n"
".Lok_eo:\n"
"s_waitcnt lgkmcnt(0)\n"
"s_barrier\n"
"ds_read_b128 v[128:131], v26 offset:0\n"
"ds_read_b128 v[140:143], v26 offset:10240\n"
"ds_read_b128 v[152:155], v26 offset:20480\n"
"ds_read_b128 v[72:75], v26 offset:30720\n"
"ds_read_b128 v[132:135], v26 offset:1024\n"
"ds_read_b128 v[144:147], v26 offset:11264\n"
"ds_read_b128 v[156:159], v26 offset:21504\n"
"ds_read_b128 v[76:79], v26 offset:31744\n"
"ds_read_b128 v[136:139], v26 offset:2048\n"
"ds_read_b128 v[148:151], v26 offset:12288\n"
"ds_read_b128 v[160:163], v26 offset:22528\n"
"ds_read_b128 v[80:83], v26 offset:32768\n"
"v_lshlrev_b32 v84, 16, v6\n"
"v_and_b32 v85, 0xffff0000, v6\n"
"v_lshlrev_b32 v86, 16, v7\n"
"v_and_b32 v87, 0xffff0000, v7\n"
"v_lshlrev_b32 v88, 16, v8\n"
"v_and_b32 v89, 0xffff0000, v8\n"
"v_lshlrev_b32 v90, 16, v9\n"
"v_and_b32 v91, 0xffff0000, v9\n"
"v_lshlrev_b32 v92, 16, v10\n"
"v_and_b32 v93, 0xffff0000, v10\n"
"v_lshlrev_b32 v94, 16, v11\n"
"v_and_b32 v95, 0xffff0000, v11\n"
"s_add_u32 s64, s59, 0\n"
"s_add_u32 s28, s96, 0\n"
"v_mov_b32 v112, 0\n"
"v_mov_b32 v113, 0\n"
"s_waitcnt lgkmcnt(8)\n"
"v_pk_add_f32 v[128:129], v[128:129], v[140:141]\n"
"v_pk_add_f32 v[152:153], v[152:153], v[72:73]\n"
"v_pk_add_f32 v[130:131], v[130:131], v[142:143]\n"
"v_pk_add_f32 v[154:155], v[154:155], v[74:75]\n"
"v_pk_add_f32 v[128:129], v[128:129], v[84:85]\n"
"v_pk_add_f32 v[130:131], v[130:131], v[86:87]\n"
"v_pk_add_f32 v[128:129], v[128:129], v[152:153]\n"
"v_pk_add_f32 v[130:131], v[130:131], v[154:155]\n"
"v_cvt_pk_bf16_f32 v6, v128, v129\n"
"v_cvt_pk_bf16_f32 v7, v130, v131\n"
"global_store_dwordx2 v27, v[6:7], s[64:65] offset:0 sc1\n"
"s_waitcnt lgkmcnt(4)\n"
"v_pk_add_f32 v[132:133], v[132:133], v[144:145]\n"
"v_pk_add_f32 v[156:157], v[156:157], v[76:77]\n"
"v_pk_add_f32 v[134:135], v[134:135], v[146:147]\n"
"v_pk_add_f32 v[158:159], v[158:159], v[78:79]\n"
"v_pk_add_f32 v[132:133], v[132:133], v[88:89]\n"
"v_pk_add_f32 v[134:135], v[134:135], v[90:91]\n"
"v_pk_add_f32 v[132:133], v[132:133], v[156:157]\n"
"v_pk_add_f32 v[134:135], v[134:135], v[158:159]\n"
"v_cvt_pk_bf16_f32 v8, v132, v133\n"
"v_cvt_pk_bf16_f32 v9, v134, v135\n"
"global_store_dwordx2 v27, v[8:9], s[64:65] offset:512 sc1\n"
"s_cmp_ge_u32 s56, 2\n"
"s_cbranch_scc1 .Lred_fa\n"
"s_waitcnt lgkmcnt(0)\n"
"v_pk_add_f32 v[136:137], v[136:137], v[148:149]\n"
"v_pk_add_f32 v[160:161], v[160:161], v[80:81]\n"
"v_pk_add_f32 v[138:139], v[138:139], v[150:151]\n"
"v_pk_add_f32 v[162:163], v[162:163], v[82:83]\n"
"v_pk_add_f32 v[136:137], v[136:137], v[92:93]\n"
"v_pk_add_f32 v[138:139], v[138:139], v[94:95]\n"
"v_pk_add_f32 v[136:137], v[136:137], v[160:161]\n"
"v_pk_add_f32 v[138:139], v[138:139], v[162:163]\n"
"v_cvt_pk_bf16_f32 v10, v136, v137\n"
"v_cvt_pk_bf16_f32 v11, v138, v139\n"
".Lred_fa:\n"
"buffer_store_dwordx2 v[10:11], v27, s[20:23], s9 offen offset:1024 sc1\n"
"s_waitcnt lgkmcnt(0)\n"
"v_dot2c_f32_bf16 v112, v6, v114\n"
"v_dot2c_f32_bf16 v113, v6, v6\n"
"v_dot2c_f32_bf16 v112, v7, v114\n"
"v_dot2c_f32_bf16 v113, v7, v7\n"
"v_dot2c_f32_bf16 v112, v8, v114\n"
"v_dot2c_f32_bf16 v113, v8, v8\n"
"v_dot2c_f32_bf16 v112, v9, v114\n"
"v_dot2c_f32_bf16 v113, v9, v9\n"
"v_dot2c_f32_bf16 v112, v10, v114\n"
"v_dot2c_f32_bf16 v113, v10, v10\n"
"v_dot2c_f32_bf16 v112, v11, v114\n"
"v_dot2c_f32_bf16 v113, v11, v11\n"
"s_nop 1\n"
"v_add_f32_dpp v112, v112, v112 quad_perm:[1,0,3,2] row_mask:0xf bank_mask:0xf\n"
"v_add_f32_dpp v113, v113, v113 quad_perm:[1,0,3,2] row_mask:0xf bank_mask:0xf\n"
"s_nop 0\n"
"v_add_f32_dpp v112, v112, v112 quad_perm:[2,3,0,1] row_mask:0xf bank_mask:0xf\n"
"v_add_f32_dpp v113, v113, v113 quad_perm:[2,3,0,1] row_mask:0xf bank_mask:0xf\n"
"global_store_dwordx2 v28, v[112:113], s[28:29] offset:0 sc1\n"
"s_waitcnt vmcnt(9)\n"
".Lchk_lo:\n"
"v_max_u32 v29, v32, v33\n"
"v_max3_u32 v29, v34, v35, v29\n"
"v_max3_u32 v29, v36, v37, v29\n"
"v_max3_u32 v29, v38, v39, v29\n"
"v_max3_u32 v29, v40, v41, v29\n"
"v_max3_u32 v29, v42, v43, v29\n"
"v_max3_u32 v29, v44, v45, v29\n"
"v_max3_u32 v29, v46, v47, v29\n"
"v_max3_u32 v29, v48, v49, v29\n"
"v_max3_u32 v29, v50, v51, v29\n"
"v_cmp_eq_u32 vcc, 0xffbfffbf, v29\n"
"s_nop 1\n"
"s_cbranch_vccnz .Lre_lo\n"
".Lok_lo:\n"
"v_mfma_f32_16x16x32_bf16 v[72:75], v[188:191], v[32:35], 0\n"
"s_add_u32 s64, s79, s71\n"
"v_mfma_f32_16x16x32_bf16 v[72:75], v[208:211], v[36:39], v[72:75]\n"
"global_load_dwordx4 a[140:143], v0, s[64:65] offset:0 sc1\n"
"v_mfma_f32_16x16x32_bf16 v[72:75], v[228:231], v[40:43], v[72:75]\n"
"v_mfma_f32_16x16x32_bf16 v[72:75], v[248:251], v[44:47], v[72:75]\n"
"v_mfma_f32_16x16x32_bf16 v[72:75], a[236:239], v[48:51], v[72:75]\n"
"global_load_dwordx4 a[144:147], v20, s[64:65] offset:0 sc1\n"
"v_mfma_f32_16x16x32_bf16 v[76:79], v[192:195], v[32:35], 0\n"
"v_mfma_f32_16x16x32_bf16 v[76:79], v[212:215], v[36:39], v[76:79]\n"
"v_mfma_f32_16x16x32_bf16 v[76:79], v[232:235], v[40:43], v[76:79]\n"
"global_load_dwordx4 a[148:151], v21, s[64:65] offset:0 sc1\n"
"v_mfma_f32_16x16x32_bf16 v[76:79], v[252:255], v[44:47], v[76:79]\n"
"v_mfma_f32_16x16x32_bf16 v[76:79], a[240:243], v[48:51], v[76:79]\n"
"v_mfma_f32_16x16x32_bf16 v[80:83], v[196:199], v[32:35], 0\n"
"global_load_dwordx4 a[152:155], v22, s[64:65] offset:0 sc1\n"
"v_mfma_f32_16x16x32_bf16 v[80:83], v[216:219], v[36:39], v[80:83]\n"
"s_waitcnt vmcnt(8)\n"
"v_max_u32 v29, v52, v53\n"
"v_mfma_f32_16x16x32_bf16 v[80:83], v[236:239], v[40:43], v[80:83]\n"
"v_max3_u32 v29, v54, v55, v29\n"
"v_max3_u32 v29, v56, v57, v29\n"
"v_mfma_f32_16x16x32_bf16 v[80:83], a[224:227], v[44:47], v[80:83]\n"
"global_load_dwordx4 a[156:159], v23, s[64:65] offset:0 sc1\n"
"v_mfma_f32_16x16x32_bf16 v[80:83], a[244:247], v[48:51], v[80:83]\n"
"s_barrier\n"
"v_mfma_f32_16x16x32_bf16 v[84:87], v[200:203], v[32:35], 0\n"
"ds_write_b128 v25, v[72:75] offset:0\n"
"v_mfma_f32_16x16x32_bf16 v[84:87], v[220:223], v[36:39], v[84:87]\n"
"v_max3_u32 v29, v58, v59, v29\n"
"v_max3_u32 v29, v60, v61, v29\n"
"v_mfma_f32_16x16x32_bf16 v[84:87], v[240:243], v[40:43], v[84:87]\n"
"ds_write_b128 v25, v[76:79] offset:1024\n"
"v_mfma_f32_16x16x32_bf16 v[84:87], a[228:231], v[44:47], v[84:87]\n"
"v_max3_u32 v29, v62, v63, v29\n"
"v_max3_u32 v29, v64, v65, v29\n"
"v_mfma_f32_16x16x32_bf16 v[84:87], a[248:251], v[48:51], v[84:87]\n"
"ds_write_b128 v25, v[80:83] offset:2048\n"
"v_mfma_f32_16x16x32_bf16 v[88:91], v[204:207], v[32:35], 0\n"
"v_max3_u32 v29, v66, v67, v29\n"
"v_max3_u32 v29, v68, v69, v29\n"
"v_mfma_f32_16x16x32_bf16 v[88:91], v[224:227], v[36:39], v[88:91]\n"
"v_max3_u32 v29, v70, v71, v29\n"
"v_cmp_eq_u32 vcc, 0xffbfffbf, v29\n"
"v_mfma_f32_16x16x32_bf16 v[88:91], v[244:247], v[40:43], v[88:91]\n"
"v_mfma_f32_16x16x32_bf16 v[88:91], a[232:235], v[44:47], v[88:91]\n"
"ds_write_b128 v25, v[84:87] offset:3072\n"
"v_mfma_f32_16x16x32_bf16 v[88:91], a[252:255], v[48:51], v[88:91]\n"
"s_cbranch_vccnz .Lre_lob\n"
".Lok_lob:\n"
"v_mfma_f32_16x16x32_bf16 v[92:95], v[188:191], v[52:55], 0\n"
"s_add_u32 s64, s66, 0\n"
"v_mfma_f32_16x16x32_bf16 v[92:95], v[208:211], v[56:59], v[92:95]\n"
"global_load_dwordx4 v[120:123], v0, s[64:65] offset:0 sc1\n"
"v_mfma_f32_16x16x32_bf16 v[92:95], v[228:231], v[60:63], v[92:95]\n"
"v_mfma_f32_16x16x32_bf16 v[92:95], v[248:251], v[64:67], v[92:95]\n"
"s_nop 0\n"
"ds_write_b128 v25, v[88:91] offset:4096\n"
"v_mfma_f32_16x16x32_bf16 v[92:95], a[236:239], v[68:71], v[92:95]\n"
"global_load_dwordx4 v[124:127], v0, s[64:65] offset:1024 sc1\n"
"v_mfma_f32_16x16x32_bf16 v[96:99], v[192:195], v[52:55], 0\n"
"v_mfma_f32_16x16x32_bf16 v[96:99], v[212:215], v[56:59], v[96:99]\n"
"v_mfma_f32_16x16x32_bf16 v[96:99], v[232:235], v[60:63], v[96:99]\n"
"global_load_dwordx4 v[128:131], v0, s[64:65] offset:2048 sc1\n"
"v_mfma_f32_16x16x32_bf16 v[96:99], v[252:255], v[64:67], v[96:99]\n"
"s_nop 1\n"
"ds_write_b128 v25, v[92:95] offset:5120\n"
"v_mfma_f32_16x16x32_bf16 v[96:99], a[240:243], v[68:71], v[96:99]\n"
"v_mfma_f32_16x16x32_bf16 v[100:103], v[196:199], v[52:55], 0\n"
"global_load_dwordx4 v[132:135], v0, s[64:65] offset:3072 sc1\n"
"v_mfma_f32_16x16x32_bf16 v[100:103], v[216:219], v[56:59], v[100:103]\n"
"v_mfma_f32_16x16x32_bf16 v[100:103], v[236:239], v[60:63], v[100:103]\n"
"v_mfma_f32_16x16x32_bf16 v[100:103], a[224:227], v[64:67], v[100:103]\n"
"s_nop 2\n"
"ds_write_b128 v25, v[96:99] offset:6144\n"
"v_mfma_f32_16x16x32_bf16 v[100:103], a[244:247], v[68:71], v[100:103]\n"
"global_load_dwordx4 v[136:139], v1, s[64:65] offset:0 sc1\n"
"v_mfma_f32_16x16x32_bf16 v[104:107], v[200:203], v[52:55], 0\n"
"s_add_u32 s64, s66, 5120\n"
"v_mfma_f32_16x16x32_bf16 v[104:107], v[220:223], v[56:59], v[104:107]\n"
"v_mfma_f32_16x16x32_bf16 v[104:107], v[240:243], v[60:63], v[104:107]\n"
"global_load_dwordx4 v[140:143], v0, s[64:65] offset:0 sc1\n"
"v_mfma_f32_16x16x32_bf16 v[104:107], a[228:231], v[64:67], v[104:107]\n"
"s_nop 0\n"
"ds_write_b128 v25, v[100:103] offset:7168\n"
"v_mfma_f32_16x16x32_bf16 v[104:107], a[248:251], v[68:71], v[104:107]\n"
"v_mfma_f32_16x16x32_bf16 v[108:111], v[204:207], v[52:55], 0\n"
"global_load_dwordx4 v[144:147], v0, s[64:65] offset:1024 sc1\n"
"v_mfma_f32_16x16x32_bf16 v[108:111], v[224:227], v[56:59], v[108:111]\n"
"v_mfma_f32_16x16x32_bf16 v[108:111], v[244:247], v[60:63], v[108:111]\n"
"v_mfma_f32_16x16x32_bf16 v[108:111], a[232:235], v[64:67], v[108:111]\n"
"s_nop 2\n"
"ds_write_b128 v25, v[104:107] offset:8192\n"
"v_mfma_f32_16x16x32_bf16 v[108:111], a[252:255], v[68:71], v[108:111]\n"
"global_load_dwordx4 v[148:151], v0, s[64:65] offset:2048 sc1\n"
"global_load_dwordx4 v[152:155], v0, s[64:65] offset:3072 sc1\n"
"global_load_dwordx4 v[156:159], v1, s[64:65] offset:0 sc1\n"
"s_nop 4\n"
"ds_write_b128 v25, v[108:111] offset:9216\n"
"s_waitcnt lgkmcnt(0)\n"
"s_barrier\n"
"ds_read_b128 v[32:35], v26 offset:0\n"
"ds_read_b128 v[44:47], v26 offset:10240\n"
"ds_read_b128 v[56:59], v26 offset:20480\n"
"ds_read_b128 v[72:75], v26 offset:30720\n"
"ds_read_b128 v[36:39], v26 offset:1024\n"
"ds_read_b128 v[48:51], v26 offset:11264\n"
"ds_read_b128 v[60:63], v26 offset:21504\n"
"ds_read_b128 v[76:79], v26 offset:31744\n"
"ds_read_b128 v[40:43], v26 offset:2048\n"
"ds_read_b128 v[52:55], v26 offset:12288\n"
"ds_read_b128 v[64:67], v26 offset:22528\n"
"ds_read_b128 v[80:83], v26 offset:32768\n"
"v_lshlrev_b32 v84, 16, v12\n"
"v_and_b32 v85, 0xffff0000, v12\n"
"v_lshlrev_b32 v86, 16, v13\n"
"v_and_b32 v87, 0xffff0000, v13\n"
"v_lshlrev_b32 v88, 16, v14\n"
"v_and_b32 v89, 0xffff0000, v14\n"
"v_lshlrev_b32 v90, 16, v15\n"
"v_and_b32 v91, 0xffff0000, v15\n"
"v_lshlrev_b32 v92, 16, v16\n"
"v_and_b32 v93, 0xffff0000, v16\n"
"v_lshlrev_b32 v94, 16, v17\n"
"v_and_b32 v95, 0xffff0000, v17\n"
"s_add_u32 s64, s59, 40960\n"
"s_add_u32 s28, s96, 4096\n"
"v_mov_b32 v112, 0\n"
"v_mov_b32 v113, 0\n"
"s_waitcnt lgkmcnt(8)\n"
"v_pk_add_f32 v[32:33], v[32:33], v[44:45]\n"
"v_pk_add_f32 v[56:57], v[56:57], v[72:73]\n"
"v_pk_add_f32 v[34:35], v[34:35], v[46:47]\n"
"v_pk_add_f32 v[58:59], v[58:59], v[74:75]\n"
"v_pk_add_f32 v[32:33], v[32:33], v[84:85]\n"
"v_pk_add_f32 v[34:35], v[34:35], v[86:87]\n"
"v_pk_add_f32 v[32:33], v[32:33], v[56:57]\n"
"v_pk_add_f32 v[34:35], v[34:35], v[58:59]\n"
"v_cvt_pk_bf16_f32 v12, v32, v33\n"
"v_cvt_pk_bf16_f32 v13, v34, v35\n"
"global_store_dwordx2 v27, v[12:13], s[64:65] offset:0 sc1\n"
"s_waitcnt lgkmcnt(4)\n"
"v_pk_add_f32 v[36:37], v[36:37], v[48:49]\n"
"v_pk_add_f32 v[60:61], v[60:61], v[76:77]\n"
"v_pk_add_f32 v[38:39], v[38:39], v[50:51]\n"
"v_pk_add_f32 v[62:63], v[62:63], v[78:79]\n"
"v_pk_add_f32 v[36:37], v[36:37], v[88:89]\n"
"v_pk_add_f32 v[38:39], v[38:39], v[90:91]\n"
"v_pk_add_f32 v[36:37], v[36:37], v[60:61]\n"
"v_pk_add_f32 v[38:39], v[38:39], v[62:63]\n"
"v_cvt_pk_bf16_f32 v14, v36, v37\n"
"v_cvt_pk_bf16_f32 v15, v38, v39\n"
"global_store_dwordx2 v27, v[14:15], s[64:65] offset:512 sc1\n"
"s_cmp_ge_u32 s56, 2\n"
"s_cbranch_scc1 .Lred_fb\n"
"s_waitcnt lgkmcnt(0)\n"
"v_pk_add_f32 v[40:41], v[40:41], v[52:53]\n"
"v_pk_add_f32 v[64:65], v[64:65], v[80:81]\n"
"v_pk_add_f32 v[42:43], v[42:43], v[54:55]\n"
"v_pk_add_f32 v[66:67], v[66:67], v[82:83]\n"
"v_pk_add_f32 v[40:41], v[40:41], v[92:93]\n"
"v_pk_add_f32 v[42:43], v[42:43], v[94:95]\n"
"v_pk_add_f32 v[40:41], v[40:41], v[64:65]\n"
"v_pk_add_f32 v[42:43], v[42:43], v[66:67]\n"
"v_cvt_pk_bf16_f32 v16, v40, v41\n"
"v_cvt_pk_bf16_f32 v17, v42, v43\n"
".Lred_fb:\n"
"buffer_store_dwordx2 v[16:17], v27, s[20:23], s10 offen offset:1024 sc1\n"
"ds_read_b128 a[200:203], v0 offset:0\n"
"ds_read_b128 a[204:207], v0 offset:10240\n"
"ds_read_b128 a[208:211], v0 offset:20480\n"
"ds_read_b128 a[212:215], v0 offset:30720\n"
"ds_read_b128 a[216:219], v0 offset:40960\n"
"ds_read_b128 a[220:223], v0 offset:51200\n"
"ds_read_b128 a[224:227], v0 offset:1024\n"
"ds_read_b128 a[228:231], v0 offset:11264\n"
"ds_read_b128 a[232:235], v0 offset:21504\n"
"s_add_u32 s64, s66, 10240\n"
"global_load_dwordx4 v[160:163], v0, s[64:65] offset:0 sc1\n"
"v_dot2c_f32_bf16 v112, v12, v114\n"
"v_dot2c_f32_bf16 v113, v12, v12\n"
"v_dot2c_f32_bf16 v112, v13, v114\n"
"v_dot2c_f32_bf16 v113, v13, v13\n"
"global_load_dwordx4 v[164:167], v0, s[64:65] offset:1024 sc1\n"
"v_dot2c_f32_bf16 v112, v14, v114\n"
"v_dot2c_f32_bf16 v113, v14, v14\n"
"v_dot2c_f32_bf16 v112, v15, v114\n"
"v_dot2c_f32_bf16 v113, v15, v15\n"
"global_load_dwordx4 v[168:171], v0, s[64:65] offset:2048 sc1\n"
"v_dot2c_f32_bf16 v112, v16, v114\n"
"v_dot2c_f32_bf16 v113, v16, v16\n"
"v_dot2c_f32_bf16 v112, v17, v114\n"
"v_dot2c_f32_bf16 v113, v17, v17\n"
"global_load_dwordx4 v[172:175], v0, s[64:65] offset:3072 sc1\n"
"s_nop 0\n"
"v_add_f32_dpp v112, v112, v112 quad_perm:[1,0,3,2] row_mask:0xf bank_mask:0xf\n"
"v_add_f32_dpp v113, v113, v113 quad_perm:[1,0,3,2] row_mask:0xf bank_mask:0xf\n"
"s_nop 0\n"
"v_add_f32_dpp v112, v112, v112 quad_perm:[2,3,0,1] row_mask:0xf bank_mask:0xf\n"
"v_add_f32_dpp v113, v113, v113 quad_perm:[2,3,0,1] row_mask:0xf bank_mask:0xf\n"
"global_load_dwordx4 v[176:179], v1, s[64:65] offset:0 sc1\n"
"global_store_dwordx2 v28, v[112:113], s[28:29] offset:0 sc1\n"
"s_waitcnt vmcnt(14)\n"
"v_max_u32 v30, v120, v121\n"
"v_max3_u32 v30, v122, v123, v30\n"
"v_max3_u32 v30, v124, v125, v30\n"
"v_max3_u32 v30, v126, v127, v30\n"
"v_max3_u32 v30, v128, v129, v30\n"
"v_max3_u32 v30, v130, v131, v30\n"
"v_max3_u32 v30, v132, v133, v30\n"
"v_max3_u32 v30, v134, v135, v30\n"
"v_max3_u32 v30, v136, v137, v30\n"
"v_max3_u32 v30, v138, v139, v30\n"
"v_cmp_eq_u32 vcc, 0xffbfffbf, v30\n"
"s_nop 1\n"
"s_cbranch_vccnz .Lre_h0_0\n"
".Lok_h0_0:\n"
"s_waitcnt lgkmcnt(7)\n"
"v_mfma_f32_16x16x32_bf16 v[192:195], a[200:203], v[120:123], 0\n"
"ds_read_b128 a[236:239], v0 offset:31744\n"
"v_lshlrev_b32 v25, 9, v4\n"
"v_mfma_f32_16x16x32_bf16 v[196:199], a[204:207], v[120:123], 0\n"
"s_waitcnt lgkmcnt(6)\n"
"ds_read_b128 a[200:203], v0 offset:41984\n"
"v_lshl_add_u32 v25, v3, 4, v25\n"
"v_mfma_f32_16x16x32_bf16 v[208:211], a[208:211], v[120:123], 0\n"
"ds_read_b128 a[204:207], v0 offset:52224\n"
"v_and_b32 v26, 1, v4\n"
"v_mfma_f32_16x16x32_bf16 v[212:215], a[212:215], v[120:123], 0\n"
"s_waitcnt lgkmcnt(6)\n"
"ds_read_b128 a[208:211], v0 offset:2048\n"
"s_add_u32 s64, s66, 15360\n"
"v_mfma_f32_16x16x32_bf16 v[224:227], a[216:219], v[120:123], 0\n"
"ds_read_b128 a[212:215], v0 offset:12288\n"
"global_load_dwordx4 v[180:183], v0, s[64:65] offset:0 sc1\n"
"v_mfma_f32_16x16x32_bf16 v[228:231], a[220:223], v[120:123], 0\n"
"s_waitcnt lgkmcnt(6)\n"
"ds_read_b128 a[216:219], v0 offset:22528\n"
"v_mul_u32_u24 v26, 110592, v26\n"
"v_mfma_f32_16x16x32_bf16 v[192:195], a[224:227], v[124:127], v[192:195]\n"
"ds_read_b128 a[220:223], v0 offset:32768\n"
"v_lshrrev_b32 v27, 1, v4\n"
"v_mfma_f32_16x16x32_bf16 v[196:199], a[228:231], v[124:127], v[196:199]\n"
"s_waitcnt lgkmcnt(6)\n"
"ds_read_b128 a[224:227], v0 offset:43008\n"
"v_lshl_add_u32 v26, v27, 8, v26\n"
"v_mfma_f32_16x16x32_bf16 v[208:211], a[232:235], v[124:127], v[208:211]\n"
"ds_read_b128 a[228:231], v0 offset:53248\n"
"s_barrier\n"
"global_load_dwordx4 v[184:187], v0, s[64:65] offset:1024 sc1\n"
"v_mfma_f32_16x16x32_bf16 v[212:215], a[236:239], v[124:127], v[212:215]\n"
"s_waitcnt lgkmcnt(6)\n"
"ds_read_b128 a[232:235], v0 offset:3072\n"
"v_lshl_add_u32 v5, v3, 4, v26\n"
"v_mfma_f32_16x16x32_bf16 v[224:227], a[200:203], v[124:127], v[224:227]\n"
"ds_read_b128 a[236:239], v0 offset:13312\n"
"v_add_u32 v3, 65536, v0\n"
"v_mfma_f32_16x16x32_bf16 v[228:231], a[204:207], v[124:127], v[228:231]\n"
"s_waitcnt lgkmcnt(6)\n"
"ds_read_b128 a[200:203], v0 offset:23552\n"
"v_add_u32 v4, 131072, v0\n"
"v_mfma_f32_16x16x32_bf16 v[192:195], a[208:211], v[128:131], v[192:195]\n"
"ds_read_b128 a[204:207], v0 offset:33792\n"
"global_load_dwordx4 v[188:191], v0, s[64:65] offset:2048 sc1\n"
"v_mfma_f32_16x16x32_bf16 v[196:199], a[212:215], v[128:131], v[196:199]\n"
"s_waitcnt lgkmcnt(6)\n"
"ds_read_b128 a[208:211], v0 offset:44032\n"
"v_mfma_f32_16x16x32_bf16 v[208:211], a[216:219], v[128:131], v[208:211]\n"
"ds_read_b128 a[212:215], v0 offset:54272\n"
"v_mfma_f32_16x16x32_bf16 v[212:215], a[220:223], v[128:131], v[212:215]\n"
"s_waitcnt lgkmcnt(6)\n"
"ds_read_b128 a[216:219], v0 offset:4096\n"
"v_mfma_f32_16x16x32_bf16 v[224:227], a[224:227], v[128:131], v[224:227]\n"
".Lhop7:\n"
"s_cbranch_execz .Lhop11\n"
"ds_read_b128 a[220:223], v0 offset:14336\n"
"global_load_dwordx4 v[112:115], v0, s[64:65] offset:3072 sc1\n"
"v_mfma_f32_16x16x32_bf16 v[228:231], a[228:231], v[128:131], v[228:231]\n"
"s_waitcnt lgkmcnt(6)\n"
"ds_read_b128 a[224:227], v0 offset:24576\n"
"v_mfma_f32_16x16x32_bf16 v[192:195], a[232:235], v[132:135], v[192:195]\n"
"ds_read_b128 a[228:231], v0 offset:34816\n"
"s_waitcnt vmcnt(13)\n"
"v_mfma_f32_16x16x32_bf16 v[196:199], a[236:239], v[132:135], v[196:199]\n"
"s_waitcnt lgkmcnt(6)\n"
"ds_read_b128 a[232:235], v0 offset:45056\n"
"v_max_u32 v30, v140, v141\n"
"v_mfma_f32_16x16x32_bf16 v[208:211], a[200:203], v[132:135], v[208:211]\n"
"ds_read_b128 a[236:239], v0 offset:55296\n"
"global_load_dwordx4 v[116:119], v1, s[64:65] offset:0 sc1\n"
"v_mfma_f32_16x16x32_bf16 v[212:215], a[204:207], v[132:135], v[212:215]\n"
"s_waitcnt lgkmcnt(6)\n"
"ds_read_b128 a[200:203], v0 offset:5120\n"
"s_add_u32 s64, s67, 0\n"
"v_mfma_f32_16x16x32_bf16 v[224:227], a[208:211], v[132:135], v[224:227]\n"
"ds_read_b128 a[204:207], v0 offset:15360\n"
"v_max3_u32 v30, v142, v143, v30\n"
"v_mfma_f32_16x16x32_bf16 v[228:231], a[212:215], v[132:135], v[228:231]\n"
"s_waitcnt lgkmcnt(6)\n"
"ds_read_b128 a[208:211], v0 offset:25600\n"
"v_max3_u32 v30, v144, v145, v30\n"
"v_mfma_f32_16x16x32_bf16 v[192:195], a[216:219], v[136:139], v[192:195]\n"
"ds_read_b128 a[212:215], v0 offset:35840\n"
"global_load_dwordx4 v[240:243], v25, s[64:65] offset:0 sc1\n"
"v_mfma_f32_16x16x32_bf16 v[196:199], a[220:223], v[136:139], v[196:199]\n"
"s_waitcnt lgkmcnt(6)\n"
"ds_read_b128 a[216:219], v0 offset:46080\n"
"v_max3_u32 v30, v146, v147, v30\n"
"v_mfma_f32_16x16x32_bf16 v[208:211], a[224:227], v[136:139], v[208:211]\n"
"ds_read_b128 a[220:223], v0 offset:56320\n"
"v_max3_u32 v30, v148, v149, v30\n"
"v_mfma_f32_16x16x32_bf16 v[212:215], a[228:231], v[136:139], v[212:215]\n"
"s_waitcnt lgkmcnt(6)\n"
"ds_read_b128 a[224:227], v0 offset:6144\n"
"v_max3_u32 v30, v150, v151, v30\n"
"v_mfma_f32_16x16x32_bf16 v[224:227], a[232:235], v[136:139], v[224:227]\n"
"ds_read_b128 a[228:231], v0 offset:16384\n"
"global_load_dwordx4 v[244:247], v25, s[64:65] offset:256 sc1\n"
"v_mfma_f32_16x16x32_bf16 v[228:231], a[236:239], v[136:139], v[228:231]\n"
"s_waitcnt lgkmcnt(6)\n"
"ds_read_b128 a[232:235], v0 offset:26624\n"
"s_add_u32 s64, s66, 40960\n"
"v_max3_u32 v30, v152, v153, v30\n"
"v_max3_u32 v30, v154, v155, v30\n"
"v_max3_u32 v30, v156, v157, v30\n"
"v_max3_u32 v30, v158, v159, v30\n"
"v_cmp_eq_u32 vcc, 0xffbfffbf, v30\n"
"s_nop 1\n"
"s_cbranch_vccnz .Lre_h0_1\n"
".Lok_h0_1:\n"
"v_mfma_f32_16x16x32_bf16 v[192:195], a[200:203], v[140:143], v[192:195]\n"
"ds_read_b128 a[236:239], v0 offset:36864\n"
"v_mfma_f32_16x16x32_bf16 v[196:199], a[204:207], v[140:143], v[196:199]\n"
"s_waitcnt lgkmcnt(6)\n"
"ds_read_b128 a[200:203], v0 offset:47104\n"
"v_mfma_f32_16x16x32_bf16 v[208:211], a[208:211], v[140:143], v[208:211]\n"
"ds_read_b128 a[204:207], v0 offset:57344\n"
"global_load_dwordx4 v[32:35], v0, s[64:65] offset:0 sc1\n"
"v_mfma_f32_16x16x32_bf16 v[212:215], a[212:215], v[140:143], v[212:215]\n"
"s_waitcnt lgkmcnt(6)\n"
"ds_read_b128 a[208:211], v0 offset:7168\n"
"v_mfma_f32_16x16x32_bf16 v[224:227], a[216:219], v[140:143], v[224:227]\n"
"ds_read_b128 a[212:215], v0 offset:17408\n"
"v_mfma_f32_16x16x32_bf16 v[228:231], a[220:223], v[140:143], v[228:231]\n"
"s_waitcnt lgkmcnt(6)\n"
"ds_read_b128 a[216:219], v0 offset:27648\n"
"v_mfma_f32_16x16x32_bf16 v[192:195], a[224:227], v[144:147], v[192:195]\n"
"ds_read_b128 a[220:223], v0 offset:37888\n"
"global_load_dwordx4 v[36:39], v0, s[64:65] offset:1024 sc1\n"
"v_mfma_f32_16x16x32_bf16 v[196:199], a[228:231], v[144:147], v[196:199]\n"
"s_waitcnt lgkmcnt(6)\n"
"ds_read_b128 a[224:227], v0 offset:48128\n"
"v_mfma_f32_16x16x32_bf16 v[208:211], a[232:235], v[144:147], v[208:211]\n"
"ds_read_b128 a[228:231], v0 offset:58368\n"
"v_mfma_f32_16x16x32_bf16 v[212:215], a[236:239], v[144:147], v[212:215]\n"
"s_waitcnt lgkmcnt(6)\n"
"ds_read_b128 a[232:235], v0 offset:8192\n"
"v_mfma_f32_16x16x32_bf16 v[224:227], a[200:203], v[144:147], v[224:227]\n"
"ds_read_b128 a[236:239], v0 offset:18432\n"
"global_load_dwordx4 v[40:43], v0, s[64:65] offset:2048 sc1\n"
"v_mfma_f32_16x16x32_bf16 v[228:231], a[204:207], v[144:147], v[228:231]\n"
"s_waitcnt lgkmcnt(6)\n"
"ds_read_b128 a[200:203], v0 offset:28672\n"
"v_mfma_f32_16x16x32_bf16 v[192:195], a[208:211], v[148:151], v[192:195]\n"
"ds_read_b128 a[204:207], v0 offset:38912\n"
"v_mfma_f32_16x16x32_bf16 v[196:199], a[212:215], v[148:151], v[196:199]\n"
"s_waitcnt lgkmcnt(6)\n"
"ds_read_b128 a[208:211], v0 offset:49152\n"
"v_mfma_f32_16x16x32_bf16 v[208:211], a[216:219], v[148:151], v[208:211]\n"
"ds_read_b128 a[212:215], v0 offset:59392\n"
"global_load_dwordx4 v[44:47], v0, s[64:65] offset:3072 sc1\n"
"v_mfma_f32_16x16x32_bf16 v[212:215], a[220:223], v[148:151], v[212:215]\n"
"s_waitcnt lgkmcnt(6)\n"
"ds_read_b128 a[216:219], v0 offset:9216\n"
"v_mfma_f32_16x16x32_bf16 v[224:227], a[224:227], v[148:151], v[224:227]\n"
"ds_read_b128 a[220:223], v0 offset:19456\n"
"v_mfma_f32_16x16x32_bf16 v[228:231], a[228:231], v[148:151], v[228:231]\n"
"s_waitcnt lgkmcnt(6)\n"
"ds_read_b128 a[224:227], v0 offset:29696\n"
"v_mfma_f32_16x16x32_bf16 v[192:195], a[232:235], v[152:155], v[192:195]\n"
"ds_read_b128 a[228:231], v0 offset:39936\n"
"global_load_dwordx4 v[48:51], v1, s[64:65] offset:0 sc1\n"
"v_mfma_f32_16x16x32_bf16 v[196:199], a[236:239], v[152:155], v[196:199]\n"
"s_waitcnt lgkmcnt(6)\n"
"ds_read_b128 a[232:235], v0 offset:50176\n"
"s_add_u32 s64, s87, 40960\n"
"v_mfma_f32_16x16x32_bf16 v[208:211], a[200:203], v[152:155], v[208:211]\n"
"ds_read_b128 a[236:239], v0 offset:60416\n"
"s_waitcnt vmcnt(13)\n"
"v_mfma_f32_16x16x32_bf16 v[212:215], a[204:207], v[152:155], v[212:215]\n"
"s_waitcnt lgkmcnt(6)\n"
"ds_read_b128 a[200:203], v0 offset:61440\n"
"v_max_u32 v30, v160, v161\n"
"v_mfma_f32_16x16x32_bf16 v[224:227], a[208:211], v[152:155], v[224:227]\n"
"ds_read_b128 a[204:207], v3 offset:6144\n"
"global_load_dwordx4 a[240:243], v0, s[64:65] offset:0 sc1\n"
"v_mfma_f32_16x16x32_bf16 v[228:231], a[212:215], v[152:155], v[228:231]\n"
"s_waitcnt lgkmcnt(6)\n"
"ds_read_b128 a[208:211], v3 offset:16384\n"
"s_add_u32 s64, s87, 82944\n"
"v_mfma_f32_16x16x32_bf16 v[192:195], a[216:219], v[156:159], v[192:195]\n"
"ds_read_b128 a[212:215], v3 offset:26624\n"
"v_max3_u32 v30, v162, v163, v30\n"
"v_mfma_f32_16x16x32_bf16 v[196:199], a[220:223], v[156:159], v[196:199]\n"
"s_waitcnt lgkmcnt(6)\n"
"ds_read_b128 a[216:219], v3 offset:36864\n"
"v_max3_u32 v30, v164, v165, v30\n"
"v_mfma_f32_16x16x32_bf16 v[208:211], a[224:227], v[156:159], v[208:211]\n"
"ds_read_b128 a[220:223], v3 offset:47104\n"
"global_load_dwordx4 a[244:247], v0, s[64:65] offset:0 sc1\n"
"v_mfma_f32_16x16x32_bf16 v[212:215], a[228:231], v[156:159], v[212:215]\n"
"s_waitcnt lgkmcnt(6)\n"
"ds_read_b128 a[224:227], v0 offset:62464\n"
"s_add_u32 s64, s87, 124928\n"
"v_mfma_f32_16x16x32_bf16 v[224:227], a[232:235], v[156:159], v[224:227]\n"
"ds_read_b128 a[228:231], v3 offset:7168\n"
"v_max3_u32 v30, v166, v167, v30\n"
"v_mfma_f32_16x16x32_bf16 v[228:231], a[236:239], v[156:159], v[228:231]\n"
"s_waitcnt lgkmcnt(6)\n"
"ds_read_b128 a[232:235], v3 offset:17408\n"
"v_max3_u32 v30, v168, v169, v30\n"
"v_max3_u32 v30, v170, v171, v30\n"
"v_max3_u32 v30, v172, v173, v30\n"
"v_max3_u32 v30, v174, v175, v30\n"
"v_max3_u32 v30, v176, v177, v30\n"
"v_max3_u32 v30, v178, v179, v30\n"
"v_cmp_eq_u32 vcc, 0xffbfffbf, v30\n"
"s_nop 1\n"
"s_cbranch_vccnz .Lre_h0_2\n"
".Lok_h0_2:\n"
"v_mfma_f32_16x16x32_bf16 v[192:195], a[200:203], v[160:163], v[192:195]\n"
"ds_read_b128 a[236:239], v3 offset:27648\n"
"global_load_dwordx4 a[248:251], v0, s[64:65] offset:0 sc1\n"
"v_mfma_f32_16x16x32_bf16 v[196:199], a[204:207], v[160:163], v[196:199]\n"
"s_waitcnt lgkmcnt(6)\n"
"ds_read_b128 a[200:203], v3 offset:37888\n"
"s_add_u32 s64, s66, 46080\n"
"v_mfma_f32_16x16x32_bf16 v[208:211], a[208:211], v[160:163], v[208:211]\n"
"ds_read_b128 a[204:207], v3 offset:48128\n"
"s_waitcnt vmcnt(8)\n"
"v_mfma_f32_16x16x32_bf16 v[212:215], a[212:215], v[160:163], v[212:215]\n"
"s_waitcnt lgkmcnt(6)\n"
"ds_read_b128 a[208:211], v0 offset:63488\n"
"v_max_u32 v30, v240, v241\n"
"v_mfma_f32_16x16x32_bf16 v[224:227], a[216:219], v[160:163], v[224:227]\n"
"ds_read_b128 a[212:215], v3 offset:8192\n"
"global_load_dwordx4 v[52:55], v0, s[64:65] offset:0 sc1\n"
"v_mfma_f32_16x16x32_bf16 v[228:231], a[220:223], v[160:163], v[228:231]\n"
"s_waitcnt lgkmcnt(6)\n"
"ds_read_b128 a[216:219], v3 offset:18432\n"
"v_max3_u32 v30, v242, v243, v30\n"
"v_mfma_f32_16x16x32_bf16 v[192:195], a[224:227], v[164:167], v[192:195]\n"
"ds_read_b128 a[220:223], v3 offset:28672\n"
"v_max3_u32 v30, v244, v245, v30\n"
"v_max3_u32 v30, v246, v247, v30\n"
"v_cmp_eq_u32 vcc, 0xffbfffbf, v30\n"
"s_nop 1\n"
"s_cbranch_vccnz .Lre_st0\n"
".Lok_st0:\n"
"v_mfma_f32_16x16x32_bf16 v[196:199], a[228:231], v[164:167], v[196:199]\n"
"s_waitcnt lgkmcnt(6)\n"
"ds_read_b128 a[224:227], v3 offset:38912\n"
"v_mfma_f32_16x16x32_bf16 v[208:211], a[232:235], v[164:167], v[208:211]\n"
"ds_read_b128 a[228:231], v3 offset:49152\n"
"global_load_dwordx4 v[56:59], v0, s[64:65] offset:1024 sc1\n"
"v_mfma_f32_16x16x32_bf16 v[212:215], a[236:239], v[164:167], v[212:215]\n"
"s_waitcnt lgkmcnt(6)\n"
"ds_read_b128 a[232:235], v0 offset:64512\n"
"v_add_f32 v240, v240, v244\n"
"v_mfma_f32_16x16x32_bf16 v[224:227], a[200:203], v[164:167], v[224:227]\n"
"ds_read_b128 a[236:239], v3 offset:9216\n"
"v_add_f32 v241, v241, v245\n"
"v_mfma_f32_16x16x32_bf16 v[228:231], a[204:207], v[164:167], v[228:231]\n"
"s_waitcnt lgkmcnt(6)\n"
"ds_read_b128 a[200:203], v3 offset:19456\n"
"v_add_f32 v242, v242, v246\n"
"v_mfma_f32_16x16x32_bf16 v[192:195], a[208:211], v[168:171], v[192:195]\n"
"ds_read_b128 a[204:207], v3 offset:29696\n"
"global_load_dwordx4 v[60:63], v0, s[64:65] offset:2048 sc1\n"
"v_mfma_f32_16x16x32_bf16 v[196:199], a[212:215], v[168:171], v[196:199]\n"
"s_waitcnt lgkmcnt(6)\n"
"ds_read_b128 a[208:211], v3 offset:39936\n"
"v_add_f32 v243, v243, v247\n"
"v_mfma_f32_16x16x32_bf16 v[208:211], a[216:219], v[168:171], v[208:211]\n"
"ds_read_b128 a[212:215], v3 offset:50176\n"
"v_add_f32 v240, v240, v242\n"
"v_mfma_f32_16x16x32_bf16 v[212:215], a[220:223], v[168:171], v[212:215]\n"
"s_waitcnt lgkmcnt(6)\n"
"ds_read_b128 a[216:219], v3 offset:0\n"
"v_add_f32 v241, v241, v243\n"
"v_mfma_f32_16x16x32_bf16 v[224:227], a[224:227], v[168:171], v[224:227]\n"
"ds_read_b128 a[220:223], v3 offset:10240\n"
"global_load_dwordx4 v[64:67], v0, s[64:65] offset:3072 sc1\n"
"v_mfma_f32_16x16x32_bf16 v[228:231], a[228:231], v[168:171], v[228:231]\n"
"s_waitcnt lgkmcnt(6)\n"
"ds_read_b128 a[224:227], v3 offset:20480\n"
"v_mov_b32 v242, v240\n"
"v_mfma_f32_16x16x32_bf16 v[192:195], a[232:235], v[172:175], v[192:195]\n"
"ds_read_b128 a[228:231], v3 offset:30720\n"
"v_mfma_f32_16x16x32_bf16 v[196:199], a[236:239], v[172:175], v[196:199]\n"
"s_waitcnt lgkmcnt(6)\n"
"ds_read_b128 a[232:235], v3 offset:40960\n"
"v_max_u32 v30, v180, v181\n"
"v_mfma_f32_16x16x32_bf16 v[208:211], a[200:203], v[172:175], v[208:211]\n"
"ds_read_b128 a[236:239], v3 offset:51200\n"
"global_load_dwordx4 v[68:71], v1, s[64:65] offset:0 sc1\n"
"v_mfma_f32_16x16x32_bf16 v[212:215], a[204:207], v[172:175], v[212:215]\n"
"s_waitcnt lgkmcnt(6)\n"
"ds_read_b128 a[200:203], v3 offset:1024\n"
"s_add_u32 s64, s66, 51200\n"
"v_mfma_f32_16x16x32_bf16 v[224:227], a[208:211], v[172:175], v[224:227]\n"
"ds_read_b128 a[204:207], v3 offset:11264\n"
"v_max3_u32 v30, v182, v183, v30\n"
"v_mfma_f32_16x16x32_bf16 v[228:231], a[212:215], v[172:175], v[228:231]\n"
"s_waitcnt lgkmcnt(6)\n"
"ds_read_b128 a[208:211], v3 offset:21504\n"
"v_max3_u32 v30, v184, v185, v30\n"
"v_mfma_f32_16x16x32_bf16 v[192:195], a[216:219], v[176:179], v[192:195]\n"
"ds_read_b128 a[212:215], v3 offset:31744\n"
"global_load_dwordx4 v[72:75], v0, s[64:65] offset:0 sc1\n"
"v_mfma_f32_16x16x32_bf16 v[196:199], a[220:223], v[176:179], v[196:199]\n"
"s_waitcnt lgkmcnt(6)\n"
"ds_read_b128 a[216:219], v3 offset:41984\n"
"v_max3_u32 v30, v186, v187, v30\n"
"v_mfma_f32_16x16x32_bf16 v[208:211], a[224:227], v[176:179], v[208:211]\n"
"ds_read_b128 a[220:223], v3 offset:52224\n"
"v_max3_u32 v30, v188, v189, v30\n"
"v_mfma_f32_16x16x32_bf16 v[212:215], a[228:231], v[176:179], v[212:215]\n"
"s_waitcnt lgkmcnt(6)\n"
"ds_read_b128 a[224:227], v3 offset:2048\n"
"v_max3_u32 v30, v190, v191, v30\n"
"v_mfma_f32_16x16x32_bf16 v[224:227], a[232:235], v[176:179], v[224:227]\n"
"ds_read_b128 a[228:231], v3 offset:12288\n"
"global_load_dwordx4 v[76:79], v0, s[64:65] offset:1024 sc1\n"
"v_mfma_f32_16x16x32_bf16 v[228:231], a[236:239], v[176:179], v[228:231]\n"
"s_waitcnt lgkmcnt(6)\n"
"ds_read_b128 a[232:235], v3 offset:22528\n"
"v_max3_u32 v30, v112, v113, v30\n"
"v_max3_u32 v30, v114, v115, v30\n"
"v_max3_u32 v30, v116, v117, v30\n"
"v_max3_u32 v30, v118, v119, v30\n"
"v_cmp_eq_u32 vcc, 0xffbfffbf, v30\n"
"s_nop 1\n"
"s_cbranch_vccnz .Lre_h0_3\n"
".Lok_h0_3:\n"
"v_mfma_f32_16x16x32_bf16 v[192:195], a[200:203], v[180:183], v[192:195]\n"
"ds_read_b128 a[236:239], v3 offset:32768\n"
"v_mov_b32 v243, v241\n"
"v_mfma_f32_16x16x32_bf16 v[196:199], a[204:207], v[180:183], v[196:199]\n"
"s_waitcnt lgkmcnt(6)\n"
"ds_read_b128 a[200:203], v3 offset:43008\n"
"v_permlane16_swap_b32 v240, v242\n"
"v_mfma_f32_16x16x32_bf16 v[208:211], a[208:211], v[180:183], v[208:211]\n"
"ds_read_b128 a[204:207], v3 offset:53248\n"
"global_load_dwordx4 v[80:83], v0, s[64:65] offset:2048 sc1\n"
"v_mfma_f32_16x16x32_bf16 v[212:215], a[212:215], v[180:183], v[212:215]\n"
"s_waitcnt lgkmcnt(6)\n"
"ds_read_b128 a[208:211], v3 offset:3072\n"
"v_permlane16_swap_b32 v241, v243\n"
"v_mfma_f32_16x16x32_bf16 v[224:227], a[216:219], v[180:183], v[224:227]\n"
"ds_read_b128 a[212:215], v3 offset:13312\n"
"v_add_f32 v240, v240, v242\n"
"v_mfma_f32_16x16x32_bf16 v[228:231], a[220:223], v[180:183], v[228:231]\n"
"s_waitcnt lgkmcnt(6)\n"
"ds_read_b128 a[216:219], v3 offset:23552\n"
"v_add_f32 v241, v241, v243\n"
"v_mfma_f32_16x16x32_bf16 v[192:195], a[224:227], v[184:187], v[192:195]\n"
"ds_read_b128 a[220:223], v3 offset:33792\n"
"global_load_dwordx4 v[84:87], v0, s[64:65] offset:3072 sc1\n"
"v_mfma_f32_16x16x32_bf16 v[196:199], a[228:231], v[184:187], v[196:199]\n"
"s_waitcnt lgkmcnt(6)\n"
"ds_read_b128 a[224:227], v3 offset:44032\n"
"v_mov_b32 v242, v240\n"
"v_mfma_f32_16x16x32_bf16 v[208:211], a[232:235], v[184:187], v[208:211]\n"
"ds_read_b128 a[228:231], v3 offset:54272\n"
"v_mov_b32 v243, v241\n"
"v_mfma_f32_16x16x32_bf16 v[212:215], a[236:239], v[184:187], v[212:215]\n"
"s_waitcnt lgkmcnt(6)\n"
"ds_read_b128 a[232:235], v3 offset:4096\n"
"v_permlane32_swap_b32 v240, v242\n"
"v_mfma_f32_16x16x32_bf16 v[224:227], a[200:203], v[184:187], v[224:227]\n"
"ds_read_b128 a[236:239], v3 offset:14336\n"
"global_load_dwordx4 v[88:91], v1, s[64:65] offset:0 sc1\n"
"v_mfma_f32_16x16x32_bf16 v[228:231], a[204:207], v[184:187], v[228:231]\n"
"s_waitcnt lgkmcnt(6)\n"
"ds_read_b128 a[200:203], v3 offset:24576\n"
"s_add_u32 s64, s66, 56320\n"
"v_mfma_f32_16x16x32_bf16 v[192:195], a[208:211], v[188:191], v[192:195]\n"
"ds_read_b128 a[204:207], v3 offset:34816\n"
"v_permlane32_swap_b32 v241, v243\n"
"v_mfma_f32_16x16x32_bf16 v[196:199], a[212:215], v[188:191], v[196:199]\n"
"s_waitcnt lgkmcnt(6)\n"
"ds_read_b128 a[208:211], v3 offset:45056\n"
"v_add_f32 v240, v240, v242\n"
"v_mfma_f32_16x16x32_bf16 v[208:211], a[216:219], v[188:191], v[208:211]\n"
"ds_read_b128 a[212:215], v3 offset:55296\n"
"global_load_dwordx4 v[92:95], v0, s[64:65] offset:0 sc1\n"
"v_mfma_f32_16x16x32_bf16 v[212:215], a[220:223], v[188:191], v[212:215]\n"
"s_waitcnt lgkmcnt(6)\n"
"ds_read_b128 a[216:219], v3 offset:5120\n"
"v_add_f32 v241, v241, v243\n"
"v_mfma_f32_16x16x32_bf16 v[224:227], a[224:227], v[188:191], v[224:227]\n"
"ds_read_b128 a[220:223], v3 offset:15360\n"
"v_mul_f32 v240, 0x3acccccd, v240\n"
"v_mfma_f32_16x16x32_bf16 v[228:231], a[228:231], v[188:191], v[228:231]\n"
"s_waitcnt lgkmcnt(6)\n"
"ds_read_b128 a[224:227], v3 offset:25600\n"
"v_mul_f32 v241, 0x3acccccd, v241\n"
"v_mfma_f32_16x16x32_bf16 v[192:195], a[232:235], v[112:115], v[192:195]\n"
"ds_read_b128 a[228:231], v3 offset:35840\n"
"global_load_dwordx4 v[96:99], v0, s[64:65] offset:1024 sc1\n"
"v_mfma_f32_16x16x32_bf16 v[196:199], a[236:239], v[112:115], v[196:199]\n"
"s_waitcnt lgkmcnt(6)\n"
"ds_read_b128 a[232:235], v3 offset:46080\n"
"s_waitcnt vmcnt(15)\n"
"v_mfma_f32_16x16x32_bf16 v[208:211], a[200:203], v[112:115], v[208:211]\n"
"ds_read_b128 a[236:239], v3 offset:56320\n"
"v_max_u32 v30, v32, v33\n"
"v_mfma_f32_16x16x32_bf16 v[212:215], a[204:207], v[112:115], v[212:215]\n"
"s_waitcnt lgkmcnt(6)\n"
"ds_read_b128 a[200:203], v0 offset:0\n"
"v_max3_u32 v30, v34, v35, v30\n"
"v_mfma_f32_16x16x32_bf16 v[224:227], a[208:211], v[112:115], v[224:227]\n"
"ds_read_b128 a[204:207], v0 offset:10240\n"
"global_load_dwordx4 v[100:103], v0, s[64:65] offset:2048 sc1\n"
"v_mfma_f32_16x16x32_bf16 v[228:231], a[212:215], v[112:115], v[228:231]\n"
"s_waitcnt lgkmcnt(6)\n"
"ds_read_b128 a[208:211], v0 offset:20480\n"
"v_max3_u32 v30, v36, v37, v30\n"
"v_mfma_f32_16x16x32_bf16 v[192:195], a[216:219], v[116:119], v[192:195]\n"
"ds_read_b128 a[212:215], v0 offset:30720\n"
"v_max3_u32 v30, v38, v39, v30\n"
"v_mfma_f32_16x16x32_bf16 v[196:199], a[220:223], v[116:119], v[196:199]\n"
"s_waitcnt lgkmcnt(6)\n"
"ds_read_b128 a[216:219], v0 offset:40960\n"
"v_max3_u32 v30, v40, v41, v30\n"
"v_mfma_f32_16x16x32_bf16 v[208:211], a[224:227], v[116:119], v[208:211]\n"
"ds_read_b128 a[220:223], v0 offset:51200\n"
"global_load_dwordx4 v[104:107], v0, s[64:65] offset:3072 sc1\n"
"v_mfma_f32_16x16x32_bf16 v[212:215], a[228:231], v[116:119], v[212:215]\n"
"s_waitcnt lgkmcnt(6)\n"
"ds_read_b128 a[224:227], v0 offset:1024\n"
"v_max3_u32 v30, v42, v43, v30\n"
"v_mfma_f32_16x16x32_bf16 v[224:227], a[232:235], v[116:119], v[224:227]\n"
"ds_read_b128 a[228:231], v0 offset:11264\n"
"v_max3_u32 v30, v44, v45, v30\n"
"v_mfma_f32_16x16x32_bf16 v[228:231], a[236:239], v[116:119], v[228:231]\n"
"s_waitcnt lgkmcnt(6)\n"
"ds_read_b128 a[232:235], v0 offset:21504\n"
"v_max3_u32 v30, v46, v47, v30\n"
"v_fma_f32 v241, -v240, v240, v241\n"
"v_max_f32 v241, 0, v241\n"
"v_cvt_pk_bf16_f32 v26, -v240, -v240\n"
"v_cvt_pk_bf16_f32 v241, v241, v241\n"
"v_and_b32 v241, 0xffff0000, v241\n"
"v_add_f32 v241, 0x3727c5ac, v241\n"
"v_cvt_pk_bf16_f32 v241, v241, v241\n"
"v_and_b32 v241, 0xffff0000, v241\n"
"v_sqrt_f32 v241, v241\n"
"s_nop 0\n"
"v_cvt_pk_bf16_f32 v19, v241, v241\n"
"v_and_b32 v19, 0xffff0000, v19\n"
"v_rcp_f32 v18, v19\n"
"v_mov_b32 v27, v26\n"
"v_mul_f32 v18, 0xbfb8aa3b, v18\n"
"v_mul_f32 v19, v19, v19\n"
"s_waitcnt vmcnt(16)\n"
"v_mfma_f32_16x16x16_bf16 v[192:195], a[240:241], v[26:27], v[192:195]\n"
"v_max3_u32 v30, v48, v49, v30\n"
"v_max3_u32 v30, v50, v51, v30\n"
"v_cmp_eq_u32 vcc, 0xffbfffbf, v30\n"
"v_mfma_f32_16x16x16_bf16 v[196:199], a[242:243], v[26:27], v[196:199]\n"
"s_waitcnt vmcnt(15)\n"
"v_mfma_f32_16x16x16_bf16 v[208:211], a[244:245], v[26:27], v[208:211]\n"
"v_mfma_f32_16x16x16_bf16 v[212:215], a[246:247], v[26:27], v[212:215]\n"
"s_waitcnt vmcnt(14)\n"
"v_mfma_f32_16x16x16_bf16 v[224:227], a[248:249], v[26:27], v[224:227]\n"
"v_mfma_f32_16x16x16_bf16 v[228:231], a[250:251], v[26:27], v[228:231]\n"
"s_cbranch_vccnz .Lre_h1_0\n"
".Lok_h1_0:\n"
"v_mfma_f32_16x16x32_bf16 v[200:203], a[200:203], v[32:35], 0\n"
"ds_read_b128 a[236:239], v0 offset:31744\n"
"global_load_dwordx4 v[108:111], v1, s[64:65] offset:0 sc1\n"
"v_mfma_f32_16x16x32_bf16 v[204:207], a[204:207], v[32:35], 0\n"
"s_waitcnt lgkmcnt(6)\n"
"ds_read_b128 a[200:203], v0 offset:41984\n"
"s_add_u32 s64, s67, 4096\n"
"v_mfma_f32_16x16x32_bf16 v[216:219], a[208:211], v[32:35], 0\n"
"ds_read_b128 a[204:207], v0 offset:52224\n"
"v_mul_f32 v196, v196, v192\n"
"v_mfma_f32_16x16x32_bf16 v[220:223], a[212:215], v[32:35], 0\n"
"s_waitcnt lgkmcnt(6)\n"
"ds_read_b128 a[208:211], v0 offset:2048\n"
"v_mul_f32 v192, v18, v192\n"
"v_mfma_f32_16x16x32_bf16 v[232:235], a[216:219], v[32:35], 0\n"
"ds_read_b128 a[212:215], v0 offset:12288\n"
"global_load_dwordx4 v[248:251], v25, s[64:65] offset:0 sc1\n"
"v_mfma_f32_16x16x32_bf16 v[236:239], a[220:223], v[32:35], 0\n"
"s_waitcnt lgkmcnt(6)\n"
"ds_read_b128 a[216:219], v0 offset:22528\n"
"v_mul_f32 v197, v197, v193\n"
"v_mfma_f32_16x16x32_bf16 v[200:203], a[224:227], v[36:39], v[200:203]\n"
"ds_read_b128 a[220:223], v0 offset:32768\n"
"v_mul_f32 v193, v18, v193\n"
"v_mfma_f32_16x16x32_bf16 v[204:207], a[228:231], v[36:39], v[204:207]\n"
"s_waitcnt lgkmcnt(6)\n"
"ds_read_b128 a[224:227], v0 offset:43008\n"
"v_mul_f32 v198, v198, v194\n"
"v_mfma_f32_16x16x32_bf16 v[216:219], a[232:235], v[36:39], v[216:219]\n"
"ds_read_b128 a[228:231], v0 offset:53248\n"
"global_load_dwordx4 v[252:255], v25, s[64:65] offset:256 sc1\n"
"v_mfma_f32_16x16x32_bf16 v[220:223], a[236:239], v[36:39], v[220:223]\n"
"s_waitcnt lgkmcnt(6)\n"
"ds_read_b128 a[232:235], v0 offset:3072\n"
"s_add_u32 s64, s60, 125952\n"
"v_mfma_f32_16x16x32_bf16 v[232:235], a[200:203], v[36:39], v[232:235]\n"
"s_add_u32 m0, s86, 122880\n"
"ds_read_b128 a[236:239], v0 offset:13312\n"
"v_mul_f32 v194, v18, v194\n"
"v_mfma_f32_16x16x32_bf16 v[236:239], a[204:207], v[36:39], v[236:239]\n"
"s_waitcnt lgkmcnt(6)\n"
"ds_read_b128 a[200:203], v0 offset:23552\n"
"v_mul_f32 v199, v199, v195\n"
"v_mfma_f32_16x16x32_bf16 v[200:203], a[208:211], v[40:43], v[200:203]\n"
"ds_read_b128 a[204:207], v0 offset:33792\n"
"global_load_lds_dwordx4 v0, s[64:65] offset:0 sc1\n"
"v_mfma_f32_16x16x32_bf16 v[204:207], a[212:215], v[40:43], v[204:207]\n"
"s_waitcnt lgkmcnt(6)\n"
"ds_read_b128 a[208:211], v0 offset:44032\n"
"v_mul_f32 v195, v18, v195\n"
"v_mfma_f32_16x16x32_bf16 v[216:219], a[216:219], v[40:43], v[216:219]\n"
"ds_read_b128 a[212:215], v0 offset:54272\n"
"v_mul_f32 v212, v212, v208\n"
"v_mfma_f32_16x16x32_bf16 v[220:223], a[220:223], v[40:43], v[220:223]\n"
"s_waitcnt lgkmcnt(6)\n"
"ds_read_b128 a[216:219], v0 offset:4096\n"
"v_mul_f32 v208, v18, v208\n"
"v_mfma_f32_16x16x32_bf16 v[232:235], a[224:227], v[40:43], v[232:235]\n"
"ds_read_b128 a[220:223], v0 offset:14336\n"
"global_load_lds_dwordx4 v0, s[64:65] offset:1024 sc1\n"
"v_mfma_f32_16x16x32_bf16 v[236:239], a[228:231], v[40:43], v[236:239]\n"
"s_waitcnt lgkmcnt(6)\n"
"ds_read_b128 a[224:227], v0 offset:24576\n"
"v_mul_f32 v213, v213, v209\n"
"v_mfma_f32_16x16x32_bf16 v[200:203], a[232:235], v[44:47], v[200:203]\n"
"ds_read_b128 a[228:231], v0 offset:34816\n"
"s_waitcnt vmcnt(14)\n"
"v_mfma_f32_16x16x32_bf16 v[204:207], a[236:239], v[44:47], v[204:207]\n"
"s_waitcnt lgkmcnt(6)\n"
"ds_read_b128 a[232:235], v0 offset:45056\n"
"v_max_u32 v30, v52, v53\n"
"v_mfma_f32_16x16x32_bf16 v[216:219], a[200:203], v[44:47], v[216:219]\n"
"ds_read_b128 a[236:239], v0 offset:55296\n"
"global_load_lds_dwordx4 v0, s[64:65] offset:2048 sc1\n"
"v_mfma_f32_16x16x32_bf16 v[220:223], a[204:207], v[44:47], v[220:223]\n"
"s_waitcnt lgkmcnt(6)\n"
"ds_read_b128 a[200:203], v0 offset:5120\n"
"v_max3_u32 v30, v54, v55, v30\n"
"v_mfma_f32_16x16x32_bf16 v[232:235], a[208:211], v[44:47], v[232:235]\n"
"ds_read_b128 a[204:207], v0 offset:15360\n"
"v_max3_u32 v30, v56, v57, v30\n"
"v_mfma_f32_16x16x32_bf16 v[236:239], a[212:215], v[44:47], v[236:239]\n"
"s_waitcnt lgkmcnt(6)\n"
"ds_read_b128 a[208:211], v0 offset:25600\n"
"v_max3_u32 v30, v58, v59, v30\n"
"v_mfma_f32_16x16x32_bf16 v[200:203], a[216:219], v[48:51], v[200:203]\n"
"ds_read_b128 a[212:215], v0 offset:35840\n"
"global_load_lds_dwordx4 v0, s[64:65] offset:3072 sc1\n"
"v_mfma_f32_16x16x32_bf16 v[204:207], a[220:223], v[48:51], v[204:207]\n"
"s_waitcnt lgkmcnt(6)\n"
"ds_read_b128 a[216:219], v0 offset:46080\n"
"s_add_u32 m0, m0, 4096\n"
"v_mfma_f32_16x16x32_bf16 v[216:219], a[224:227], v[48:51], v[216:219]\n"
"ds_read_b128 a[220:223], v0 offset:56320\n"
"v_max3_u32 v30, v60, v61, v30\n"
"v_mfma_f32_16x16x32_bf16 v[220:223], a[228:231], v[48:51], v[220:223]\n"
"s_waitcnt lgkmcnt(6)\n"
"ds_read_b128 a[224:227], v0 offset:6144\n"
"v_max3_u32 v30, v62, v63, v30\n"
"v_mfma_f32_16x16x32_bf16 v[232:235], a[232:235], v[48:51], v[232:235]\n"
"ds_read_b128 a[228:231], v0 offset:16384\n"
"global_load_lds_dwordx4 v1, s[64:65] offset:0 sc1\n"
"v_mfma_f32_16x16x32_bf16 v[236:239], a[236:239], v[48:51], v[236:239]\n"
"s_waitcnt lgkmcnt(6)\n"
"ds_read_b128 a[232:235], v0 offset:26624\n"
"s_add_u32 s64, s18, 125952\n"
"v_max3_u32 v30, v64, v65, v30\n"
"v_max3_u32 v30, v66, v67, v30\n"
"v_max3_u32 v30, v68, v69, v30\n"
"v_max3_u32 v30, v70, v71, v30\n"
"v_cmp_eq_u32 vcc, 0xffbfffbf, v30\n"
"s_nop 1\n"
"s_cbranch_vccnz .Lre_h1_1\n"
".Lok_h1_1:\n"
"v_mfma_f32_16x16x32_bf16 v[200:203], a[200:203], v[52:55], v[200:203]\n"
"s_add_u32 m0, s86, 143360\n"
"ds_read_b128 a[236:239], v0 offset:36864\n"
"v_mul_f32 v209, v18, v209\n"
"v_mfma_f32_16x16x32_bf16 v[204:207], a[204:207], v[52:55], v[204:207]\n"
"s_waitcnt lgkmcnt(6)\n"
"ds_read_b128 a[200:203], v0 offset:47104\n"
"v_mul_f32 v214, v214, v210\n"
"v_mfma_f32_16x16x32_bf16 v[216:219], a[208:211], v[52:55], v[216:219]\n"
"ds_read_b128 a[204:207], v0 offset:57344\n"
"global_load_lds_dwordx4 v0, s[64:65] offset:0 sc1\n"
"v_mfma_f32_16x16x32_bf16 v[220:223], a[212:215], v[52:55], v[220:223]\n"
"s_waitcnt lgkmcnt(6)\n"
"ds_read_b128 a[208:211], v0 offset:7168\n"
"v_mul_f32 v210, v18, v210\n"
"v_mfma_f32_16x16x32_bf16 v[232:235], a[216:219], v[52:55], v[232:235]\n"
"ds_read_b128 a[212:215], v0 offset:17408\n"
"v_mul_f32 v215, v215, v211\n"
"v_mfma_f32_16x16x32_bf16 v[236:239], a[220:223], v[52:55], v[236:239]\n"
"s_waitcnt lgkmcnt(6)\n"
"ds_read_b128 a[216:219], v0 offset:27648\n"
"v_mul_f32 v211, v18, v211\n"
"v_mfma_f32_16x16x32_bf16 v[200:203], a[224:227], v[56:59], v[200:203]\n"
"ds_read_b128 a[220:223], v0 offset:37888\n"
"global_load_lds_dwordx4 v0, s[64:65] offset:1024 sc1\n"
"v_mfma_f32_16x16x32_bf16 v[204:207], a[228:231], v[56:59], v[204:207]\n"
"s_waitcnt lgkmcnt(6)\n"
"ds_read_b128 a[224:227], v0 offset:48128\n"
"v_exp_f32 v192, v192\n"
"v_mfma_f32_16x16x32_bf16 v[216:219], a[232:235], v[56:59], v[216:219]\n"
"ds_read_b128 a[228:231], v0 offset:58368\n"
"v_fma_f32 v192, v192, v19, v19\n"
"v_mfma_f32_16x16x32_bf16 v[220:223], a[236:239], v[56:59], v[220:223]\n"
"s_waitcnt lgkmcnt(6)\n"
"ds_read_b128 a[232:235], v0 offset:8192\n"
"v_exp_f32 v193, v193\n"
"v_mfma_f32_16x16x32_bf16 v[232:235], a[200:203], v[56:59], v[232:235]\n"
"ds_read_b128 a[236:239], v0 offset:18432\n"
"global_load_lds_dwordx4 v0, s[64:65] offset:2048 sc1\n"
"v_mfma_f32_16x16x32_bf16 v[236:239], a[204:207], v[56:59], v[236:239]\n"
"s_waitcnt lgkmcnt(6)\n"
"ds_read_b128 a[200:203], v0 offset:28672\n"
"v_fma_f32 v193, v193, v19, v19\n"
"v_mfma_f32_16x16x32_bf16 v[200:203], a[208:211], v[60:63], v[200:203]\n"
"ds_read_b128 a[204:207], v0 offset:38912\n"
"v_exp_f32 v194, v194\n"
"v_mfma_f32_16x16x32_bf16 v[204:207], a[212:215], v[60:63], v[204:207]\n"
"s_waitcnt lgkmcnt(6)\n"
"ds_read_b128 a[208:211], v0 offset:49152\n"
"v_fma_f32 v194, v194, v19, v19\n"
"v_mfma_f32_16x16x32_bf16 v[216:219], a[216:219], v[60:63], v[216:219]\n"
"ds_read_b128 a[212:215], v0 offset:59392\n"
"global_load_lds_dwordx4 v0, s[64:65] offset:3072 sc1\n"
"v_mfma_f32_16x16x32_bf16 v[220:223], a[220:223], v[60:63], v[220:223]\n"
"s_waitcnt lgkmcnt(6)\n"
"ds_read_b128 a[216:219], v0 offset:9216\n"
"s_add_u32 m0, m0, 4096\n"
"v_mfma_f32_16x16x32_bf16 v[232:235], a[224:227], v[60:63], v[232:235]\n"
"ds_read_b128 a[220:223], v0 offset:19456\n"
"v_exp_f32 v195, v195\n"
"v_mfma_f32_16x16x32_bf16 v[236:239], a[228:231], v[60:63], v[236:239]\n"
"s_waitcnt lgkmcnt(6)\n"
"ds_read_b128 a[224:227], v0 offset:29696\n"
"v_fma_f32 v195, v195, v19, v19\n"
"v_mfma_f32_16x16x32_bf16 v[200:203], a[232:235], v[64:67], v[200:203]\n"
"ds_read_b128 a[228:231], v0 offset:39936\n"
"global_load_lds_dwordx4 v1, s[64:65] offset:0 sc1\n"
"v_mfma_f32_16x16x32_bf16 v[204:207], a[236:239], v[64:67], v[204:207]\n"
"s_waitcnt lgkmcnt(6)\n"
"ds_read_b128 a[232:235], v0 offset:50176\n"
"s_add_u32 s64, s87, 166912\n"
"v_mfma_f32_16x16x32_bf16 v[216:219], a[200:203], v[64:67], v[216:219]\n"
".Lhop8:\n"
"s_cbranch_execz .Lhop_ret\n"
"ds_read_b128 a[236:239], v0 offset:60416\n"
"s_waitcnt vmcnt(17)\n"
"v_mfma_f32_16x16x32_bf16 v[220:223], a[204:207], v[64:67], v[220:223]\n"
"s_waitcnt lgkmcnt(6)\n"
"ds_read_b128 a[200:203], v0 offset:61440\n"
"v_max_u32 v30, v72, v73\n"
"v_mfma_f32_16x16x32_bf16 v[232:235], a[208:211], v[64:67], v[232:235]\n"
"ds_read_b128 a[204:207], v3 offset:6144\n"
"global_load_dwordx4 a[252:255], v0, s[64:65] offset:0 sc1\n"
"v_mfma_f32_16x16x32_bf16 v[236:239], a[212:215], v[64:67], v[236:239]\n"
"s_waitcnt lgkmcnt(6)\n"
"ds_read_b128 a[208:211], v3 offset:16384\n"
"v_max3_u32 v30, v74, v75, v30\n"
"v_mfma_f32_16x16x32_bf16 v[200:203], a[216:219], v[68:71], v[200:203]\n"
"ds_read_b128 a[212:215], v3 offset:26624\n"
"v_max3_u32 v30, v76, v77, v30\n"
"v_mfma_f32_16x16x32_bf16 v[204:207], a[220:223], v[68:71], v[204:207]\n"
"s_waitcnt lgkmcnt(6)\n"
"ds_read_b128 a[216:219], v3 offset:36864\n"
"v_max3_u32 v30, v78, v79, v30\n"
"v_mfma_f32_16x16x32_bf16 v[216:219], a[224:227], v[68:71], v[216:219]\n"
"ds_read_b128 a[220:223], v3 offset:47104\n"
"v_max3_u32 v30, v80, v81, v30\n"
"v_mfma_f32_16x16x32_bf16 v[220:223], a[228:231], v[68:71], v[220:223]\n"
"s_waitcnt lgkmcnt(6)\n"
"ds_read_b128 a[224:227], v0 offset:62464\n"
"v_max3_u32 v30, v82, v83, v30\n"
"v_mfma_f32_16x16x32_bf16 v[232:235], a[232:235], v[68:71], v[232:235]\n"
"ds_read_b128 a[228:231], v3 offset:7168\n"
"v_max3_u32 v30, v84, v85, v30\n"
"v_mfma_f32_16x16x32_bf16 v[236:239], a[236:239], v[68:71], v[236:239]\n"
"s_waitcnt lgkmcnt(6)\n"
"ds_read_b128 a[232:235], v3 offset:17408\n"
"v_max3_u32 v30, v86, v87, v30\n"
"v_max3_u32 v30, v88, v89, v30\n"
"v_max3_u32 v30, v90, v91, v30\n"
"v_cmp_eq_u32 vcc, 0xffbfffbf, v30\n"
"s_nop 1\n"
"s_cbranch_vccnz .Lre_h1_2\n"
".Lok_h1_2:\n"
"v_mfma_f32_16x16x32_bf16 v[200:203], a[200:203], v[72:75], v[200:203]\n"
"ds_read_b128 a[236:239], v3 offset:27648\n"
"s_barrier\n"
"v_rcp_f32 v192, v192\n"
"v_mfma_f32_16x16x32_bf16 v[204:207], a[204:207], v[72:75], v[204:207]\n"
"s_waitcnt lgkmcnt(6)\n"
"ds_read_b128 a[200:203], v3 offset:37888\n"
"s_add_u32 s64, s60, 167936\n"
"v_mfma_f32_16x16x32_bf16 v[216:219], a[208:211], v[72:75], v[216:219]\n"
"s_add_u32 m0, s86, 0\n"
"ds_read_b128 a[204:207], v3 offset:48128\n"
"global_load_lds_dwordx4 v0, s[64:65] offset:0 sc1\n"
"v_mfma_f32_16x16x32_bf16 v[220:223], a[212:215], v[72:75], v[220:223]\n"
"s_waitcnt lgkmcnt(6)\n"
"ds_read_b128 a[208:211], v0 offset:63488\n"
"s_waitcnt vmcnt(12)\n"
"v_mfma_f32_16x16x32_bf16 v[232:235], a[216:219], v[72:75], v[232:235]\n"
"ds_read_b128 a[212:215], v3 offset:8192\n"
"v_max_u32 v30, v248, v249\n"
"v_mfma_f32_16x16x32_bf16 v[236:239], a[220:223], v[72:75], v[236:239]\n"
"s_waitcnt lgkmcnt(6)\n"
"ds_read_b128 a[216:219], v3 offset:18432\n"
"v_max3_u32 v30, v250, v251, v30\n"
"v_mfma_f32_16x16x32_bf16 v[200:203], a[224:227], v[76:79], v[200:203]\n"
"ds_read_b128 a[220:223], v3 offset:28672\n"
"global_load_lds_dwordx4 v0, s[64:65] offset:1024 sc1\n"
"v_max3_u32 v30, v252, v253, v30\n"
"v_max3_u32 v30, v254, v255, v30\n"
"v_cmp_eq_u32 vcc, 0xffbfffbf, v30\n"
"s_nop 1\n"
"s_cbranch_vccnz .Lre_st1\n"
".Lok_st1:\n"
"v_mfma_f32_16x16x32_bf16 v[204:207], a[228:231], v[76:79], v[204:207]\n"
"s_waitcnt lgkmcnt(6)\n"
"ds_read_b128 a[224:227], v3 offset:38912\n"
"v_mul_f32 v192, v192, v196\n"
"v_mfma_f32_16x16x32_bf16 v[216:219], a[232:235], v[76:79], v[216:219]\n"
"ds_read_b128 a[228:231], v3 offset:49152\n"
"v_add_f32 v248, v248, v252\n"
"v_mfma_f32_16x16x32_bf16 v[220:223], a[236:239], v[76:79], v[220:223]\n"
"s_waitcnt lgkmcnt(6)\n"
"ds_read_b128 a[232:235], v0 offset:64512\n"
"v_add_f32 v249, v249, v253\n"
"v_mfma_f32_16x16x32_bf16 v[232:235], a[200:203], v[76:79], v[232:235]\n"
"ds_read_b128 a[236:239], v3 offset:9216\n"
"global_load_lds_dwordx4 v0, s[64:65] offset:2048 sc1\n"
"v_mfma_f32_16x16x32_bf16 v[236:239], a[204:207], v[76:79], v[236:239]\n"
"s_waitcnt lgkmcnt(6)\n"
"ds_read_b128 a[200:203], v3 offset:19456\n"
"v_add_f32 v250, v250, v254\n"
"v_mfma_f32_16x16x32_bf16 v[200:203], a[208:211], v[80:83], v[200:203]\n"
"ds_read_b128 a[204:207], v3 offset:29696\n"
"v_add_f32 v251, v251, v255\n"
"v_mfma_f32_16x16x32_bf16 v[204:207], a[212:215], v[80:83], v[204:207]\n"
"s_waitcnt lgkmcnt(6)\n"
"ds_read_b128 a[208:211], v3 offset:39936\n"
"v_add_f32 v248, v248, v250\n"
"v_mfma_f32_16x16x32_bf16 v[216:219], a[216:219], v[80:83], v[216:219]\n"
"ds_read_b128 a[212:215], v3 offset:50176\n"
"global_load_lds_dwordx4 v0, s[64:65] offset:3072 sc1\n"
"v_mfma_f32_16x16x32_bf16 v[220:223], a[220:223], v[80:83], v[220:223]\n"
"s_waitcnt lgkmcnt(6)\n"
"ds_read_b128 a[216:219], v3 offset:0\n"
"s_add_u32 m0, m0, 4096\n"
"v_mfma_f32_16x16x32_bf16 v[232:235], a[224:227], v[80:83], v[232:235]\n"
"ds_read_b128 a[220:223], v3 offset:10240\n"
"v_add_f32 v249, v249, v251\n"
"v_mfma_f32_16x16x32_bf16 v[236:239], a[228:231], v[80:83], v[236:239]\n"
"s_waitcnt lgkmcnt(6)\n"
"ds_read_b128 a[224:227], v3 offset:20480\n"
"v_mov_b32 v250, v248\n"
"v_mfma_f32_16x16x32_bf16 v[200:203], a[232:235], v[84:87], v[200:203]\n"
"ds_read_b128 a[228:231], v3 offset:30720\n"
"global_load_lds_dwordx4 v1, s[64:65] offset:0 sc1\n"
"v_mfma_f32_16x16x32_bf16 v[204:207], a[236:239], v[84:87], v[204:207]\n"
"s_waitcnt lgkmcnt(6)\n"
"ds_read_b128 a[232:235], v3 offset:40960\n"
"s_add_u32 s64, s18, 167936\n"
"v_mfma_f32_16x16x32_bf16 v[216:219], a[200:203], v[84:87], v[216:219]\n"
"s_add_u32 m0, s86, 20480\n"
"ds_read_b128 a[236:239], v3 offset:51200\n"
"v_mfma_f32_16x16x32_bf16 v[220:223], a[204:207], v[84:87], v[220:223]\n"
"s_waitcnt lgkmcnt(6)\n"
"ds_read_b128 a[200:203], v3 offset:1024\n"
"v_max_u32 v30, v92, v93\n"
"v_mfma_f32_16x16x32_bf16 v[232:235], a[208:211], v[84:87], v[232:235]\n"
"ds_read_b128 a[204:207], v3 offset:11264\n"
"global_load_lds_dwordx4 v0, s[64:65] offset:0 sc1\n"
"v_mfma_f32_16x16x32_bf16 v[236:239], a[212:215], v[84:87], v[236:239]\n"
"s_waitcnt lgkmcnt(6)\n"
"ds_read_b128 a[208:211], v3 offset:21504\n"
"v_max3_u32 v30, v94, v95, v30\n"
"v_mfma_f32_16x16x32_bf16 v[200:203], a[216:219], v[88:91], v[200:203]\n"
"ds_read_b128 a[212:215], v3 offset:31744\n"
"v_max3_u32 v30, v96, v97, v30\n"
"v_mfma_f32_16x16x32_bf16 v[204:207], a[220:223], v[88:91], v[204:207]\n"
"s_waitcnt lgkmcnt(6)\n"
"ds_read_b128 a[216:219], v3 offset:41984\n"
"v_max3_u32 v30, v98, v99, v30\n"
"v_mfma_f32_16x16x32_bf16 v[216:219], a[224:227], v[88:91], v[216:219]\n"
"ds_read_b128 a[220:223], v3 offset:52224\n"
"global_load_lds_dwordx4 v0, s[64:65] offset:1024 sc1\n"
"v_mfma_f32_16x16x32_bf16 v[220:223], a[228:231], v[88:91], v[220:223]\n"
"s_waitcnt lgkmcnt(6)\n"
"ds_read_b128 a[224:227], v3 offset:2048\n"
"v_max3_u32 v30, v100, v101, v30\n"
"v_mfma_f32_16x16x32_bf16 v[232:235], a[232:235], v[88:91], v[232:235]\n"
"ds_read_b128 a[228:231], v3 offset:12288\n"
"v_max3_u32 v30, v102, v103, v30\n"
"v_mfma_f32_16x16x32_bf16 v[236:239], a[236:239], v[88:91], v[236:239]\n"
"s_waitcnt lgkmcnt(6)\n"
"ds_read_b128 a[232:235], v3 offset:22528\n"
"v_max3_u32 v30, v104, v105, v30\n"
"v_max3_u32 v30, v106, v107, v30\n"
"v_max3_u32 v30, v108, v109, v30\n"
"v_max3_u32 v30, v110, v111, v30\n"
"v_cmp_eq_u32 vcc, 0xffbfffbf, v30\n"
"s_nop 1\n"
"s_cbranch_vccnz .Lre_h1_3\n"
".Lok_h1_3:\n"
"v_mfma_f32_16x16x32_bf16 v[200:203], a[200:203], v[92:95], v[200:203]\n"
"ds_read_b128 a[236:239], v3 offset:32768\n"
"global_load_lds_dwordx4 v0, s[64:65] offset:2048 sc1\n"
"v_mfma_f32_16x16x32_bf16 v[204:207], a[204:207], v[92:95], v[204:207]\n"
"s_waitcnt lgkmcnt(6)\n"
"ds_read_b128 a[200:203], v3 offset:43008\n"
"v_mov_b32 v251, v249\n"
"v_mfma_f32_16x16x32_bf16 v[216:219], a[208:211], v[92:95], v[216:219]\n"
"ds_read_b128 a[204:207], v3 offset:53248\n"
"v_permlane16_swap_b32 v248, v250\n"
"v_mfma_f32_16x16x32_bf16 v[220:223], a[212:215], v[92:95], v[220:223]\n"
"s_waitcnt lgkmcnt(6)\n"
"ds_read_b128 a[208:211], v3 offset:3072\n"
"v_permlane16_swap_b32 v249, v251\n"
"v_mfma_f32_16x16x32_bf16 v[232:235], a[216:219], v[92:95], v[232:235]\n"
"ds_read_b128 a[212:215], v3 offset:13312\n"
"global_load_lds_dwordx4 v0, s[64:65] offset:3072 sc1\n"
"v_mfma_f32_16x16x32_bf16 v[236:239], a[220:223], v[92:95], v[236:239]\n"
"s_waitcnt lgkmcnt(6)\n"
"ds_read_b128 a[216:219], v3 offset:23552\n"
"s_add_u32 m0, m0, 4096\n"
"v_mfma_f32_16x16x32_bf16 v[200:203], a[224:227], v[96:99], v[200:203]\n"
"ds_read_b128 a[220:223], v3 offset:33792\n"
"v_add_f32 v248, v248, v250\n"
"v_mfma_f32_16x16x32_bf16 v[204:207], a[228:231], v[96:99], v[204:207]\n"
"s_waitcnt lgkmcnt(6)\n"
"ds_read_b128 a[224:227], v3 offset:44032\n"
"v_add_f32 v249, v249, v251\n"
"v_mfma_f32_16x16x32_bf16 v[216:219], a[232:235], v[96:99], v[216:219]\n"
"ds_read_b128 a[228:231], v3 offset:54272\n"
"global_load_lds_dwordx4 v1, s[64:65] offset:0 sc1\n"
"v_mfma_f32_16x16x32_bf16 v[220:223], a[236:239], v[96:99], v[220:223]\n"
"s_waitcnt lgkmcnt(6)\n"
"ds_read_b128 a[232:235], v3 offset:4096\n"
"s_add_u32 s64, s60, 209920\n"
"v_mfma_f32_16x16x32_bf16 v[232:235], a[200:203], v[96:99], v[232:235]\n"
"s_add_u32 m0, s86, 40960\n"
"ds_read_b128 a[236:239], v3 offset:14336\n"
"v_mov_b32 v250, v248\n"
"v_mfma_f32_16x16x32_bf16 v[236:239], a[204:207], v[96:99], v[236:239]\n"
"s_waitcnt lgkmcnt(6)\n"
"ds_read_b128 a[200:203], v3 offset:24576\n"
"v_mov_b32 v251, v249\n"
"v_mfma_f32_16x16x32_bf16 v[200:203], a[208:211], v[100:103], v[200:203]\n"
"ds_read_b128 a[204:207], v3 offset:34816\n"
"global_load_lds_dwordx4 v0, s[64:65] offset:0 sc1\n"
"v_mfma_f32_16x16x32_bf16 v[204:207], a[212:215], v[100:103], v[204:207]\n"
"s_waitcnt lgkmcnt(6)\n"
"ds_read_b128 a[208:211], v3 offset:45056\n"
"v_permlane32_swap_b32 v248, v250\n"
"v_mfma_f32_16x16x32_bf16 v[216:219], a[216:219], v[100:103], v[216:219]\n"
"ds_read_b128 a[212:215], v3 offset:55296\n"
"v_permlane32_swap_b32 v249, v251\n"
"v_mfma_f32_16x16x32_bf16 v[220:223], a[220:223], v[100:103], v[220:223]\n"
"s_waitcnt lgkmcnt(6)\n"
"ds_read_b128 a[216:219], v3 offset:5120\n"
"v_add_f32 v248, v248, v250\n"
"v_mfma_f32_16x16x32_bf16 v[232:235], a[224:227], v[100:103], v[232:235]\n"
"ds_read_b128 a[220:223], v3 offset:15360\n"
"global_load_lds_dwordx4 v0, s[64:65] offset:1024 sc1\n"
"v_mfma_f32_16x16x32_bf16 v[236:239], a[228:231], v[100:103], v[236:239]\n"
"s_waitcnt lgkmcnt(6)\n"
"ds_read_b128 a[224:227], v3 offset:25600\n"
"v_add_f32 v249, v249, v251\n"
"v_mfma_f32_16x16x32_bf16 v[200:203], a[232:235], v[104:107], v[200:203]\n"
"ds_read_b128 a[228:231], v3 offset:35840\n"
"v_mul_f32 v248, 0x3acccccd, v248\n"
"v_mfma_f32_16x16x32_bf16 v[204:207], a[236:239], v[104:107], v[204:207]\n"
"s_waitcnt lgkmcnt(6)\n"
"ds_read_b128 a[232:235], v3 offset:46080\n"
"v_mul_f32 v249, 0x3acccccd, v249\n"
"v_mfma_f32_16x16x32_bf16 v[216:219], a[200:203], v[104:107], v[216:219]\n"
"ds_read_b128 a[236:239], v3 offset:56320\n"
"global_load_lds_dwordx4 v0, s[64:65] offset:2048 sc1\n"
"v_mfma_f32_16x16x32_bf16 v[220:223], a[204:207], v[104:107], v[220:223]\n"
"s_waitcnt lgkmcnt(6)\n"
"v_fma_f32 v249, -v248, v248, v249\n"
"v_max_f32 v249, 0, v249\n"
"v_mfma_f32_16x16x32_bf16 v[232:235], a[208:211], v[104:107], v[232:235]\n"
"v_cvt_pk_bf16_f32 v28, -v248, -v248\n"
"v_cvt_pk_bf16_f32 v249, v249, v249\n"
"v_mfma_f32_16x16x32_bf16 v[236:239], a[212:215], v[104:107], v[236:239]\n"
"s_waitcnt lgkmcnt(4)\n"
"v_and_b32 v249, 0xffff0000, v249\n"
"v_add_f32 v249, 0x3727c5ac, v249\n"
"v_mfma_f32_16x16x32_bf16 v[200:203], a[216:219], v[108:111], v[200:203]\n"
"global_load_lds_dwordx4 v0, s[64:65] offset:3072 sc1\n"
"v_mfma_f32_16x16x32_bf16 v[204:207], a[220:223], v[108:111], v[204:207]\n"
"s_waitcnt lgkmcnt(2)\n"
"s_add_u32 m0, m0, 4096\n"
"v_cvt_pk_bf16_f32 v249, v249, v249\n"
"v_mfma_f32_16x16x32_bf16 v[216:219], a[224:227], v[108:111], v[216:219]\n"
"s_waitcnt vmcnt(15)\n"
"s_barrier\n"
"ds_read_b128 a[220:223], v3 offset:57344\n"
"v_and_b32 v249, 0xffff0000, v249\n"
"v_mfma_f32_16x16x32_bf16 v[220:223], a[228:231], v[108:111], v[220:223]\n"
"s_waitcnt lgkmcnt(2)\n"
"ds_read_b128 a[224:227], v4 offset:2048\n"
"v_sqrt_f32 v249, v249\n"
"v_mfma_f32_16x16x32_bf16 v[232:235], a[232:235], v[108:111], v[232:235]\n"
"s_waitcnt lgkmcnt(2)\n"
"ds_read_b128 a[228:231], v3 offset:58368\n"
"global_load_lds_dwordx4 v1, s[64:65] offset:0 sc1\n"
"v_mfma_f32_16x16x32_bf16 v[236:239], a[236:239], v[108:111], v[236:239]\n"
"s_waitcnt lgkmcnt(2)\n"
"ds_read_b128 a[232:235], v4 offset:3072\n"
"v_cvt_pk_bf16_f32 v2, v249, v249\n"
"v_and_b32 v2, 0xffff0000, v2\n"
"v_rcp_f32 v25, v2\n"
"v_mov_b32 v29, v28\n"
"v_mul_f32 v25, 0xbfb8aa3b, v25\n"
"v_mul_f32 v2, v2, v2\n"
"v_mfma_f32_16x16x16_bf16 v[200:203], a[240:241], v[28:29], v[200:203]\n"
"v_rcp_f32 v193, v193\n"
"v_mfma_f32_16x16x16_bf16 v[204:207], a[242:243], v[28:29], v[204:207]\n"
"v_rcp_f32 v194, v194\n"
"v_mul_f32 v193, v193, v197\n"
"v_mfma_f32_16x16x16_bf16 v[216:219], a[244:245], v[28:29], v[216:219]\n"
"v_rcp_f32 v195, v195\n"
"v_mul_f32 v194, v194, v198\n"
"v_mfma_f32_16x16x16_bf16 v[220:223], a[246:247], v[28:29], v[220:223]\n"
"v_exp_f32 v208, v208\n"
"v_cvt_pk_bf16_f32 v196, v192, v193\n"
"v_mfma_f32_16x16x16_bf16 v[232:235], a[248:249], v[28:29], v[232:235]\n"
"v_exp_f32 v209, v209\n"
"v_mul_f32 v195, v195, v199\n"
"v_mfma_f32_16x16x16_bf16 v[236:239], a[250:251], v[28:29], v[236:239]\n"
"v_exp_f32 v210, v210\n"
"v_cvt_pk_bf16_f32 v197, v194, v195\n"
"v_mfma_f32_16x16x32_bf16 v[240:243], a[220:223], v[120:123], 0\n"
"ds_read_b128 a[236:239], v3 offset:59392\n"
"s_barrier\n"
"v_fma_f32 v208, v208, v19, v19\n"
"v_mfma_f32_16x16x32_bf16 v[248:251], a[220:223], v[32:35], 0\n"
"s_waitcnt lgkmcnt(3)\n"
"s_add_u32 s64, s18, 209920\n"
"s_add_u32 m0, s86, 61440\n"
"v_mfma_f32_16x16x32_bf16 v[244:247], a[224:227], v[120:123], 0\n"
"ds_read_b128 a[220:223], v4 offset:4096\n"
"v_fma_f32 v209, v209, v19, v19\n"
"v_mfma_f32_16x16x32_bf16 v[252:255], a[224:227], v[32:35], 0\n"
"s_waitcnt lgkmcnt(3)\n"
"global_load_lds_dwordx4 v0, s[64:65] offset:0 sc1\n"
"v_mfma_f32_16x16x32_bf16 v[240:243], a[228:231], v[124:127], v[240:243]\n"
"ds_read_b128 a[224:227], v3 offset:60416\n"
"v_fma_f32 v210, v210, v19, v19\n"
"v_mfma_f32_16x16x32_bf16 v[248:251], a[228:231], v[36:39], v[248:251]\n"
"s_waitcnt lgkmcnt(3)\n"
"v_exp_f32 v211, v211\n"
"v_mfma_f32_16x16x32_bf16 v[244:247], a[232:235], v[124:127], v[244:247]\n"
"ds_read_b128 a[228:231], v4 offset:5120\n"
"v_fma_f32 v211, v211, v19, v19\n"
"v_mfma_f32_16x16x32_bf16 v[252:255], a[232:235], v[36:39], v[252:255]\n"
"s_waitcnt lgkmcnt(3)\n"
"global_load_lds_dwordx4 v0, s[64:65] offset:1024 sc1\n"
"v_mfma_f32_16x16x32_bf16 v[240:243], a[236:239], v[128:131], v[240:243]\n"
"ds_read_b128 a[232:235], v3 offset:61440\n"
"v_mul_f32 v228, v228, v224\n"
"v_mfma_f32_16x16x32_bf16 v[248:251], a[236:239], v[40:43], v[248:251]\n"
"s_waitcnt lgkmcnt(3)\n"
"v_rcp_f32 v208, v208\n"
"v_mfma_f32_16x16x32_bf16 v[244:247], a[220:223], v[128:131], v[244:247]\n"
"ds_read_b128 a[236:239], v4 offset:6144\n"
"v_mul_f32 v208, v208, v212\n"
"v_mfma_f32_16x16x32_bf16 v[252:255], a[220:223], v[40:43], v[252:255]\n"
"s_waitcnt lgkmcnt(3)\n"
"global_load_lds_dwordx4 v0, s[64:65] offset:2048 sc1\n"
"v_mfma_f32_16x16x32_bf16 v[240:243], a[224:227], v[132:135], v[240:243]\n"
"ds_read_b128 a[220:223], v3 offset:62464\n"
"v_mul_f32 v224, v18, v224\n"
"v_mfma_f32_16x16x32_bf16 v[248:251], a[224:227], v[44:47], v[248:251]\n"
"s_waitcnt lgkmcnt(3)\n"
"v_rcp_f32 v209, v209\n"
"v_mfma_f32_16x16x32_bf16 v[244:247], a[228:231], v[132:135], v[244:247]\n"
"ds_read_b128 a[224:227], v4 offset:7168\n"
"v_mul_f32 v209, v209, v213\n"
"v_mfma_f32_16x16x32_bf16 v[252:255], a[228:231], v[44:47], v[252:255]\n"
"s_waitcnt lgkmcnt(3)\n"
"global_load_lds_dwordx4 v0, s[64:65] offset:3072 sc1\n"
"v_mfma_f32_16x16x32_bf16 v[240:243], a[232:235], v[136:139], v[240:243]\n"
"s_add_u32 m0, m0, 4096\n"
"ds_read_b128 a[228:231], v3 offset:63488\n"
"v_cvt_pk_bf16_f32 v212, v208, v209\n"
"v_mfma_f32_16x16x32_bf16 v[248:251], a[232:235], v[48:51], v[248:251]\n"
"s_waitcnt lgkmcnt(3)\n"
"v_rcp_f32 v210, v210\n"
"v_mfma_f32_16x16x32_bf16 v[244:247], a[236:239], v[136:139], v[244:247]\n"
"ds_read_b128 a[232:235], v4 offset:8192\n"
"v_mul_f32 v210, v210, v214\n"
"v_mfma_f32_16x16x32_bf16 v[252:255], a[236:239], v[48:51], v[252:255]\n"
"s_waitcnt lgkmcnt(3)\n"
"global_load_lds_dwordx4 v1, s[64:65] offset:0 sc1\n"
"v_mfma_f32_16x16x32_bf16 v[240:243], a[220:223], v[140:143], v[240:243]\n"
"s_add_u32 s64, s87, 208896\n"
"ds_read_b128 a[236:239], v3 offset:64512\n"
"v_mul_f32 v229, v229, v225\n"
"v_mfma_f32_16x16x32_bf16 v[248:251], a[220:223], v[52:55], v[248:251]\n"
"s_waitcnt lgkmcnt(3)\n"
"v_rcp_f32 v211, v211\n"
"v_mfma_f32_16x16x32_bf16 v[244:247], a[224:227], v[140:143], v[244:247]\n"
"ds_read_b128 a[220:223], v4 offset:9216\n"
"v_mul_f32 v211, v211, v215\n"
"v_mfma_f32_16x16x32_bf16 v[252:255], a[224:227], v[52:55], v[252:255]\n"
"s_waitcnt lgkmcnt(3)\n"
"global_load_dwordx4 a[240:243], v0, s[64:65] offset:0 sc1\n"
"v_mfma_f32_16x16x32_bf16 v[240:243], a[228:231], v[144:147], v[240:243]\n"
"s_add_u32 s64, s60, 251904\n"
"ds_read_b128 a[224:227], v4 offset:0\n"
"s_add_u32 s64, s64, s99\n"
"v_mfma_f32_16x16x32_bf16 v[248:251], a[228:231], v[56:59], v[248:251]\n"
"s_waitcnt lgkmcnt(3)\n"
"s_add_u32 m0, s86, 81920\n"
"v_cvt_pk_bf16_f32 v213, v210, v211\n"
"v_mfma_f32_16x16x32_bf16 v[244:247], a[232:235], v[144:147], v[244:247]\n"
"ds_read_b128 a[228:231], v4 offset:10240\n"
"v_mul_f32 v225, v18, v225\n"
"v_mfma_f32_16x16x32_bf16 v[252:255], a[232:235], v[56:59], v[252:255]\n"
"s_waitcnt lgkmcnt(3)\n"
"buffer_load_dwordx4 v0, s[20:23], s64 offen offset:0 sc1 lds\n"
"v_mfma_f32_16x16x32_bf16 v[240:243], a[236:239], v[148:151], v[240:243]\n"
"ds_read_b128 a[232:235], v4 offset:1024\n"
"v_mul_f32 v230, v230, v226\n"
"v_mfma_f32_16x16x32_bf16 v[248:251], a[236:239], v[60:63], v[248:251]\n"
"s_waitcnt lgkmcnt(3)\n"
"v_exp_f32 v224, v224\n"
"v_mfma_f32_16x16x32_bf16 v[244:247], a[220:223], v[148:151], v[244:247]\n"
"ds_read_b128 a[236:239], v4 offset:11264\n"
"v_mul_f32 v226, v18, v226\n"
"v_mfma_f32_16x16x32_bf16 v[252:255], a[220:223], v[60:63], v[252:255]\n"
"s_waitcnt lgkmcnt(3)\n"
"buffer_load_dwordx4 v0, s[20:23], s64 offen offset:1024 sc1 lds\n"
"v_mfma_f32_16x16x32_bf16 v[240:243], a[224:227], v[152:155], v[240:243]\n"
"ds_read_b128 a[220:223], v4 offset:12288\n"
"v_mul_f32 v231, v231, v227\n"
"v_mfma_f32_16x16x32_bf16 v[248:251], a[224:227], v[64:67], v[248:251]\n"
"s_waitcnt lgkmcnt(3)\n"
"v_exp_f32 v225, v225\n"
"v_mfma_f32_16x16x32_bf16 v[244:247], a[228:231], v[152:155], v[244:247]\n"
"ds_read_b128 a[224:227], v4 offset:22528\n"
"v_mul_f32 v227, v18, v227\n"
"v_mfma_f32_16x16x32_bf16 v[252:255], a[228:231], v[64:67], v[252:255]\n"
"s_waitcnt lgkmcnt(3)\n"
"buffer_load_dwordx4 v0, s[20:23], s64 offen offset:2048 sc1 lds\n"
"v_mfma_f32_16x16x32_bf16 v[240:243], a[232:235], v[156:159], v[240:243]\n"
"ds_read_b128 a[228:231], v4 offset:13312\n"
"v_fma_f32 v224, v224, v19, v19\n"
"v_mfma_f32_16x16x32_bf16 v[248:251], a[232:235], v[68:71], v[248:251]\n"
"s_waitcnt lgkmcnt(3)\n"
"v_exp_f32 v226, v226\n"
"v_mfma_f32_16x16x32_bf16 v[244:247], a[236:239], v[156:159], v[244:247]\n"
"ds_read_b128 a[232:235], v4 offset:23552\n"
"v_fma_f32 v225, v225, v19, v19\n"
"v_mfma_f32_16x16x32_bf16 v[252:255], a[236:239], v[68:71], v[252:255]\n"
"s_waitcnt lgkmcnt(3)\n"
"buffer_load_dwordx4 v0, s[20:23], s64 offen offset:3072 sc1 lds\n"
"v_mfma_f32_16x16x32_bf16 v[240:243], a[220:223], v[160:163], v[240:243]\n"
"s_add_u32 m0, m0, 4096\n"
"ds_read_b128 a[236:239], v4 offset:14336\n"
"v_fma_f32 v226, v226, v19, v19\n"
"v_mfma_f32_16x16x32_bf16 v[248:251], a[220:223], v[72:75], v[248:251]\n"
"s_waitcnt lgkmcnt(3)\n"
"v_exp_f32 v227, v227\n"
"v_mfma_f32_16x16x32_bf16 v[244:247], a[224:227], v[160:163], v[244:247]\n"
"ds_read_b128 a[220:223], v4 offset:24576\n"
"v_fma_f32 v227, v227, v19, v19\n"
"v_mfma_f32_16x16x32_bf16 v[252:255], a[224:227], v[72:75], v[252:255]\n"
"s_waitcnt lgkmcnt(3)\n"
"buffer_load_dwordx4 v1, s[20:23], s64 offen offset:0 sc1 lds\n"
"v_mfma_f32_16x16x32_bf16 v[240:243], a[228:231], v[164:167], v[240:243]\n"
"s_add_u32 s64, s18, 251904\n"
"ds_read_b128 a[224:227], v4 offset:15360\n"
"s_add_u32 s64, s64, s99\n"
"v_mfma_f32_16x16x32_bf16 v[248:251], a[228:231], v[76:79], v[248:251]\n"
"s_waitcnt lgkmcnt(3)\n"
"s_add_u32 m0, s86, 102400\n"
"v_mul_f32 v204, v204, v200\n"
"v_mfma_f32_16x16x32_bf16 v[244:247], a[232:235], v[164:167], v[244:247]\n"
"ds_read_b128 a[228:231], v4 offset:25600\n"
"v_mul_f32 v200, v25, v200\n"
"v_mfma_f32_16x16x32_bf16 v[252:255], a[232:235], v[76:79], v[252:255]\n"
"s_waitcnt lgkmcnt(3)\n"
"buffer_load_dwordx4 v0, s[20:23], s64 offen offset:0 sc1 lds\n"
"v_mfma_f32_16x16x32_bf16 v[240:243], a[236:239], v[168:171], v[240:243]\n"
"ds_read_b128 a[232:235], v4 offset:16384\n"
"v_mul_f32 v205, v205, v201\n"
"v_mfma_f32_16x16x32_bf16 v[248:251], a[236:239], v[80:83], v[248:251]\n"
"s_waitcnt lgkmcnt(3)\n"
"v_rcp_f32 v224, v224\n"
"v_mfma_f32_16x16x32_bf16 v[244:247], a[220:223], v[168:171], v[244:247]\n"
"ds_read_b128 a[236:239], v4 offset:26624\n"
"v_mul_f32 v224, v224, v228\n"
"v_mfma_f32_16x16x32_bf16 v[252:255], a[220:223], v[80:83], v[252:255]\n"
"s_waitcnt lgkmcnt(3)\n"
"buffer_load_dwordx4 v0, s[20:23], s64 offen offset:1024 sc1 lds\n"
"v_mfma_f32_16x16x32_bf16 v[240:243], a[224:227], v[172:175], v[240:243]\n"
"ds_read_b128 a[220:223], v4 offset:17408\n"
"v_mul_f32 v201, v25, v201\n"
"v_mfma_f32_16x16x32_bf16 v[248:251], a[224:227], v[84:87], v[248:251]\n"
"s_waitcnt lgkmcnt(3)\n"
"v_rcp_f32 v225, v225\n"
"v_mfma_f32_16x16x32_bf16 v[244:247], a[228:231], v[172:175], v[244:247]\n"
"ds_read_b128 a[224:227], v4 offset:27648\n"
"v_mul_f32 v225, v225, v229\n"
"v_mfma_f32_16x16x32_bf16 v[252:255], a[228:231], v[84:87], v[252:255]\n"
"s_waitcnt lgkmcnt(3)\n"
"buffer_load_dwordx4 v0, s[20:23], s64 offen offset:2048 sc1 lds\n"
"v_mfma_f32_16x16x32_bf16 v[240:243], a[232:235], v[176:179], v[240:243]\n"
"ds_read_b128 a[228:231], v4 offset:18432\n"
"v_cvt_pk_bf16_f32 v228, v224, v225\n"
"v_mfma_f32_16x16x32_bf16 v[248:251], a[232:235], v[88:91], v[248:251]\n"
"s_waitcnt lgkmcnt(3)\n"
"v_rcp_f32 v226, v226\n"
"v_mfma_f32_16x16x32_bf16 v[244:247], a[236:239], v[176:179], v[244:247]\n"
"ds_read_b128 a[232:235], v4 offset:28672\n"
"v_mul_f32 v226, v226, v230\n"
"v_mfma_f32_16x16x32_bf16 v[252:255], a[236:239], v[88:91], v[252:255]\n"
"s_waitcnt lgkmcnt(3)\n"
"buffer_load_dwordx4 v0, s[20:23], s64 offen offset:3072 sc1 lds\n"
"v_mfma_f32_16x16x32_bf16 v[240:243], a[220:223], v[180:183], v[240:243]\n"
"s_add_u32 m0, m0, 4096\n"
"ds_read_b128 a[236:239], v4 offset:19456\n"
"v_mul_f32 v206, v206, v202\n"
"v_mfma_f32_16x16x32_bf16 v[248:251], a[220:223], v[92:95], v[248:251]\n"
"s_waitcnt lgkmcnt(3)\n"
"v_rcp_f32 v227, v227\n"
"v_mfma_f32_16x16x32_bf16 v[244:247], a[224:227], v[180:183], v[244:247]\n"
"ds_read_b128 a[220:223], v4 offset:29696\n"
"v_mul_f32 v227, v227, v231\n"
"v_mfma_f32_16x16x32_bf16 v[252:255], a[224:227], v[92:95], v[252:255]\n"
"s_waitcnt lgkmcnt(3)\n"
"buffer_load_dwordx4 v1, s[20:23], s64 offen offset:0 sc1 lds\n"
"v_mfma_f32_16x16x32_bf16 v[240:243], a[228:231], v[184:187], v[240:243]\n"
"s_add_u32 s64, s87, 250880\n"
"ds_read_b128 a[224:227], v4 offset:20480\n"
"v_cvt_pk_bf16_f32 v229, v226, v227\n"
"v_mfma_f32_16x16x32_bf16 v[248:251], a[228:231], v[96:99], v[248:251]\n"
"s_waitcnt lgkmcnt(3)\n"
"v_exp_f32 v200, v200\n"
"v_mfma_f32_16x16x32_bf16 v[244:247], a[232:235], v[184:187], v[244:247]\n"
"ds_read_b128 a[228:231], v4 offset:30720\n"
"v_mul_f32 v202, v25, v202\n"
"v_mfma_f32_16x16x32_bf16 v[252:255], a[232:235], v[96:99], v[252:255]\n"
"s_waitcnt lgkmcnt(3)\n"
"global_load_dwordx4 a[244:247], v0, s[64:65] offset:0 sc1\n"
"v_mfma_f32_16x16x32_bf16 v[240:243], a[236:239], v[188:191], v[240:243]\n"
"s_add_u32 s64, s87, 292864\n"
"ds_read_b128 a[232:235], v4 offset:21504\n"
"s_add_u32 s64, s64, s99\n"
"v_mfma_f32_16x16x32_bf16 v[248:251], a[236:239], v[100:103], v[248:251]\n"
"s_waitcnt lgkmcnt(3)\n"
"v_exp_f32 v201, v201\n"
"v_mfma_f32_16x16x32_bf16 v[244:247], a[220:223], v[188:191], v[244:247]\n"
"ds_read_b128 a[236:239], v4 offset:31744\n"
"v_mul_f32 v207, v207, v203\n"
"v_mfma_f32_16x16x32_bf16 v[252:255], a[220:223], v[100:103], v[252:255]\n"
"s_waitcnt lgkmcnt(3)\n"
"buffer_load_dwordx4 a[248:251], v0, s[20:23], s64 offen offset:0 sc1\n"
"v_mfma_f32_16x16x32_bf16 v[240:243], a[224:227], v[112:115], v[240:243]\n"
"s_add_u32 s64, s80, s71\n"
"s_waitcnt vmcnt(23)\n"
"s_barrier\n"
"ds_read_b128 a[220:223], v0 offset:0\n"
"v_mul_f32 v203, v25, v203\n"
"v_mfma_f32_16x16x32_bf16 v[248:251], a[224:227], v[104:107], v[248:251]\n"
"s_waitcnt lgkmcnt(3)\n"
"v_exp_f32 v202, v202\n"
"v_mfma_f32_16x16x32_bf16 v[244:247], a[228:231], v[112:115], v[244:247]\n"
"ds_read_b128 a[224:227], v0 offset:10240\n"
"v_fma_f32 v200, v200, v2, v2\n"
"v_mfma_f32_16x16x32_bf16 v[252:255], a[228:231], v[104:107], v[252:255]\n"
"s_waitcnt lgkmcnt(3)\n"
"global_load_dwordx4 a[160:163], v0, s[64:65] offset:0 sc1\n"
"v_mfma_f32_16x16x32_bf16 v[240:243], a[232:235], v[116:119], v[240:243]\n"
"ds_read_b128 a[228:231], v0 offset:1024\n"
"v_fma_f32 v201, v201, v2, v2\n"
"v_mfma_f32_16x16x32_bf16 v[248:251], a[232:235], v[108:111], v[248:251]\n"
"s_waitcnt lgkmcnt(3)\n"
"v_exp_f32 v203, v203\n"
"v_mfma_f32_16x16x32_bf16 v[244:247], a[236:239], v[116:119], v[244:247]\n"
"ds_read_b128 a[232:235], v0 offset:11264\n"
"v_fma_f32 v202, v202, v2, v2\n"
"v_mfma_f32_16x16x32_bf16 v[252:255], a[236:239], v[108:111], v[252:255]\n"
"s_waitcnt lgkmcnt(3)\n"
"global_load_dwordx4 a[164:167], v20, s[64:65] offset:0 sc1\n"
"v_mfma_f32_16x16x16_bf16 v[240:243], a[252:253], v[26:27], v[240:243]\n"
"v_rcp_f32 v200, v200\n"
"v_fma_f32 v203, v203, v2, v2\n"
"v_mfma_f32_16x16x16_bf16 v[244:247], a[254:255], v[26:27], v[244:247]\n"
"v_rcp_f32 v201, v201\n"
"v_mul_f32 v200, v200, v204\n"
"v_mfma_f32_16x16x16_bf16 v[248:251], a[252:253], v[28:29], v[248:251]\n"
"v_rcp_f32 v202, v202\n"
"v_mul_f32 v201, v201, v205\n"
"v_mfma_f32_16x16x16_bf16 v[252:255], a[254:255], v[28:29], v[252:255]\n"
"v_rcp_f32 v203, v203\n"
"v_mul_f32 v202, v202, v206\n"
"v_cvt_pk_bf16_f32 v198, v200, v201\n"
"v_mul_f32 v203, v203, v207\n"
"v_cvt_pk_bf16_f32 v199, v202, v203\n"
"v_permlane16_swap_b32 v196, v198\n"
"s_nop 0\n"
"v_permlane16_swap_b32 v197, v199\n"
"global_store_dwordx4 v5, v[196:199], s[68:69] offset:0 sc1\n"
"v_mfma_f32_16x16x32_bf16 v[192:195], a[220:223], v[120:123], 0\n"
"ds_read_b128 a[236:239], v0 offset:2048\n"
"v_mul_f32 v220, v220, v216\n"
"v_mfma_f32_16x16x32_bf16 v[200:203], a[220:223], v[32:35], 0\n"
"s_waitcnt lgkmcnt(3)\n"
"v_mul_f32 v216, v25, v216\n"
"v_mul_f32 v221, v221, v217\n"
"v_mfma_f32_16x16x32_bf16 v[196:199], a[224:227], v[120:123], 0\n"
"ds_read_b128 a[220:223], v0 offset:12288\n"
"v_mul_f32 v217, v25, v217\n"
"v_mfma_f32_16x16x32_bf16 v[204:207], a[224:227], v[32:35], 0\n"
"s_waitcnt lgkmcnt(3)\n"
"global_load_dwordx4 a[168:171], v21, s[64:65] offset:0 sc1\n"
"v_mfma_f32_16x16x32_bf16 v[192:195], a[228:231], v[124:127], v[192:195]\n"
"ds_read_b128 a[224:227], v0 offset:3072\n"
"v_mul_f32 v222, v222, v218\n"
"v_mfma_f32_16x16x32_bf16 v[200:203], a[228:231], v[36:39], v[200:203]\n"
"s_waitcnt lgkmcnt(3)\n"
"v_exp_f32 v216, v216\n"
"v_mfma_f32_16x16x32_bf16 v[196:199], a[232:235], v[124:127], v[196:199]\n"
"ds_read_b128 a[228:231], v0 offset:13312\n"
"v_mul_f32 v218, v25, v218\n"
"v_mfma_f32_16x16x32_bf16 v[204:207], a[232:235], v[36:39], v[204:207]\n"
"s_waitcnt lgkmcnt(3)\n"
"global_load_dwordx4 a[172:175], v22, s[64:65] offset:0 sc1\n"
"v_mfma_f32_16x16x32_bf16 v[192:195], a[236:239], v[128:131], v[192:195]\n"
"ds_read_b128 a[232:235], v0 offset:4096\n"
"v_mul_f32 v223, v223, v219\n"
"v_mfma_f32_16x16x32_bf16 v[200:203], a[236:239], v[40:43], v[200:203]\n"
"s_waitcnt lgkmcnt(3)\n"
"v_exp_f32 v217, v217\n"
"v_mfma_f32_16x16x32_bf16 v[196:199], a[220:223], v[128:131], v[196:199]\n"
"ds_read_b128 a[236:239], v0 offset:14336\n"
"v_mul_f32 v219, v25, v219\n"
"v_mfma_f32_16x16x32_bf16 v[204:207], a[220:223], v[40:43], v[204:207]\n"
"s_waitcnt lgkmcnt(3)\n"
"global_load_dwordx4 a[176:179], v23, s[64:65] offset:0 sc1\n"
"v_mfma_f32_16x16x32_bf16 v[192:195], a[224:227], v[132:135], v[192:195]\n"
"s_add_u32 s64, s81, s71\n"
"ds_read_b128 a[220:223], v0 offset:5120\n"
"v_fma_f32 v216, v216, v2, v2\n"
"v_mfma_f32_16x16x32_bf16 v[200:203], a[224:227], v[44:47], v[200:203]\n"
"s_waitcnt lgkmcnt(3)\n"
"v_exp_f32 v218, v218\n"
"v_mfma_f32_16x16x32_bf16 v[196:199], a[228:231], v[132:135], v[196:199]\n"
"ds_read_b128 a[224:227], v0 offset:15360\n"
"v_fma_f32 v217, v217, v2, v2\n"
"v_mfma_f32_16x16x32_bf16 v[204:207], a[228:231], v[44:47], v[204:207]\n"
"s_waitcnt lgkmcnt(3)\n"
"global_load_dwordx4 a[180:183], v0, s[64:65] offset:0 sc1\n"
"v_mfma_f32_16x16x32_bf16 v[192:195], a[232:235], v[136:139], v[192:195]\n"
"ds_read_b128 a[228:231], v0 offset:6144\n"
"v_fma_f32 v218, v218, v2, v2\n"
"v_mfma_f32_16x16x32_bf16 v[200:203], a[232:235], v[48:51], v[200:203]\n"
".Lhop9:\n"
"s_cbranch_execz .Lhop_ret\n"
"s_waitcnt lgkmcnt(3)\n"
"v_exp_f32 v219, v219\n"
"v_mfma_f32_16x16x32_bf16 v[196:199], a[236:239], v[136:139], v[196:199]\n"
"ds_read_b128 a[232:235], v0 offset:16384\n"
"v_fma_f32 v219, v219, v2, v2\n"
"v_mfma_f32_16x16x32_bf16 v[204:207], a[236:239], v[48:51], v[204:207]\n"
"s_waitcnt lgkmcnt(3)\n"
"global_load_dwordx4 a[184:187], v20, s[64:65] offset:0 sc1\n"
"v_mfma_f32_16x16x32_bf16 v[192:195], a[220:223], v[140:143], v[192:195]\n"
"ds_read_b128 a[236:239], v0 offset:7168\n"
"v_mul_f32 v236, v236, v232\n"
"v_mfma_f32_16x16x32_bf16 v[200:203], a[220:223], v[52:55], v[200:203]\n"
"s_waitcnt lgkmcnt(3)\n"
"v_rcp_f32 v216, v216\n"
"v_mfma_f32_16x16x32_bf16 v[196:199], a[224:227], v[140:143], v[196:199]\n"
"ds_read_b128 a[220:223], v0 offset:17408\n"
"v_mul_f32 v216, v216, v220\n"
"v_mfma_f32_16x16x32_bf16 v[204:207], a[224:227], v[52:55], v[204:207]\n"
"s_waitcnt lgkmcnt(3)\n"
"global_load_dwordx4 a[188:191], v21, s[64:65] offset:0 sc1\n"
"v_mfma_f32_16x16x32_bf16 v[192:195], a[228:231], v[144:147], v[192:195]\n"
"ds_read_b128 a[224:227], v0 offset:8192\n"
"v_mul_f32 v232, v25, v232\n"
"v_mfma_f32_16x16x32_bf16 v[200:203], a[228:231], v[56:59], v[200:203]\n"
"s_waitcnt lgkmcnt(3)\n"
"v_rcp_f32 v217, v217\n"
"v_mfma_f32_16x16x32_bf16 v[196:199], a[232:235], v[144:147], v[196:199]\n"
"ds_read_b128 a[228:231], v0 offset:18432\n"
"v_mul_f32 v217, v217, v221\n"
"v_mfma_f32_16x16x32_bf16 v[204:207], a[232:235], v[56:59], v[204:207]\n"
"s_waitcnt lgkmcnt(3)\n"
"global_load_dwordx4 a[192:195], v22, s[64:65] offset:0 sc1\n"
"v_mfma_f32_16x16x32_bf16 v[192:195], a[236:239], v[148:151], v[192:195]\n"
"ds_read_b128 a[232:235], v0 offset:9216\n"
"v_cvt_pk_bf16_f32 v214, v216, v217\n"
"v_mfma_f32_16x16x32_bf16 v[200:203], a[236:239], v[60:63], v[200:203]\n"
"s_waitcnt lgkmcnt(3)\n"
"v_rcp_f32 v218, v218\n"
"v_mfma_f32_16x16x32_bf16 v[196:199], a[220:223], v[148:151], v[196:199]\n"
"ds_read_b128 a[236:239], v0 offset:19456\n"
"v_mul_f32 v218, v218, v222\n"
"v_mfma_f32_16x16x32_bf16 v[204:207], a[220:223], v[60:63], v[204:207]\n"
"s_waitcnt lgkmcnt(3)\n"
"global_load_dwordx4 a[196:199], v23, s[64:65] offset:0 sc1\n"
"v_mfma_f32_16x16x32_bf16 v[192:195], a[224:227], v[152:155], v[192:195]\n"
"s_add_u32 s64, s82, s71\n"
"ds_read_b128 a[220:223], v0 offset:20480\n"
"v_permlane16_swap_b32 v212, v214\n"
"v_mfma_f32_16x16x32_bf16 v[200:203], a[224:227], v[64:67], v[200:203]\n"
"s_waitcnt lgkmcnt(3)\n"
"v_rcp_f32 v219, v219\n"
"v_mfma_f32_16x16x32_bf16 v[196:199], a[228:231], v[152:155], v[196:199]\n"
"ds_read_b128 a[224:227], v0 offset:30720\n"
"v_mul_f32 v219, v219, v223\n"
"v_mfma_f32_16x16x32_bf16 v[204:207], a[228:231], v[64:67], v[204:207]\n"
"s_waitcnt lgkmcnt(3)\n"
"buffer_load_dwordx4 a[200:203], v0, s[20:23], s64 offen offset:0 sc1\n"
"v_mfma_f32_16x16x32_bf16 v[192:195], a[232:235], v[156:159], v[192:195]\n"
"ds_read_b128 a[228:231], v0 offset:21504\n"
"v_cvt_pk_bf16_f32 v215, v218, v219\n"
"v_mfma_f32_16x16x32_bf16 v[200:203], a[232:235], v[68:71], v[200:203]\n"
"s_waitcnt lgkmcnt(3)\n"
"v_exp_f32 v232, v232\n"
"v_mfma_f32_16x16x32_bf16 v[196:199], a[236:239], v[156:159], v[196:199]\n"
"ds_read_b128 a[232:235], v0 offset:31744\n"
"v_permlane16_swap_b32 v213, v215\n"
"v_mfma_f32_16x16x32_bf16 v[204:207], a[236:239], v[68:71], v[204:207]\n"
"s_waitcnt lgkmcnt(3)\n"
"global_store_dwordx4 v5, v[212:215], s[68:69] offset:512 sc1\n"
"v_mfma_f32_16x16x32_bf16 v[192:195], a[220:223], v[160:163], v[192:195]\n"
"ds_read_b128 a[236:239], v0 offset:22528\n"
"v_mul_f32 v237, v237, v233\n"
"v_mfma_f32_16x16x32_bf16 v[200:203], a[220:223], v[72:75], v[200:203]\n"
"s_waitcnt lgkmcnt(3)\n"
"v_mul_f32 v233, v25, v233\n"
"v_mul_f32 v238, v238, v234\n"
"v_mfma_f32_16x16x32_bf16 v[196:199], a[224:227], v[160:163], v[196:199]\n"
"ds_read_b128 a[220:223], v0 offset:32768\n"
"v_mul_f32 v234, v25, v234\n"
"v_mfma_f32_16x16x32_bf16 v[204:207], a[224:227], v[72:75], v[204:207]\n"
"s_waitcnt lgkmcnt(3)\n"
"buffer_load_dwordx4 a[204:207], v20, s[20:23], s64 offen offset:0 sc1\n"
"v_mfma_f32_16x16x32_bf16 v[192:195], a[228:231], v[164:167], v[192:195]\n"
"ds_read_b128 a[224:227], v0 offset:23552\n"
"v_mul_f32 v239, v239, v235\n"
"v_mfma_f32_16x16x32_bf16 v[200:203], a[228:231], v[76:79], v[200:203]\n"
"s_waitcnt lgkmcnt(3)\n"
"v_exp_f32 v233, v233\n"
"v_mfma_f32_16x16x32_bf16 v[196:199], a[232:235], v[164:167], v[196:199]\n"
"ds_read_b128 a[228:231], v0 offset:33792\n"
"v_mul_f32 v235, v25, v235\n"
"v_mfma_f32_16x16x32_bf16 v[204:207], a[232:235], v[76:79], v[204:207]\n"
"s_waitcnt lgkmcnt(3)\n"
"buffer_load_dwordx4 a[208:211], v21, s[20:23], s64 offen offset:0 sc1\n"
"v_mfma_f32_16x16x32_bf16 v[192:195], a[236:239], v[168:171], v[192:195]\n"
"ds_read_b128 a[232:235], v0 offset:24576\n"
"v_fma_f32 v232, v232, v2, v2\n"
"v_mfma_f32_16x16x32_bf16 v[200:203], a[236:239], v[80:83], v[200:203]\n"
"s_waitcnt lgkmcnt(3)\n"
"v_exp_f32 v234, v234\n"
"v_mfma_f32_16x16x32_bf16 v[196:199], a[220:223], v[168:171], v[196:199]\n"
"ds_read_b128 a[236:239], v0 offset:34816\n"
"v_fma_f32 v233, v233, v2, v2\n"
"v_mfma_f32_16x16x32_bf16 v[204:207], a[220:223], v[80:83], v[204:207]\n"
"s_waitcnt lgkmcnt(3)\n"
"buffer_load_dwordx4 a[212:215], v22, s[20:23], s64 offen offset:0 sc1\n"
"v_mfma_f32_16x16x32_bf16 v[192:195], a[224:227], v[172:175], v[192:195]\n"
"ds_read_b128 a[220:223], v0 offset:25600\n"
"v_fma_f32 v234, v234, v2, v2\n"
"v_mfma_f32_16x16x32_bf16 v[200:203], a[224:227], v[84:87], v[200:203]\n"
"s_waitcnt lgkmcnt(3)\n"
"v_exp_f32 v235, v235\n"
"v_mfma_f32_16x16x32_bf16 v[196:199], a[228:231], v[172:175], v[196:199]\n"
"ds_read_b128 a[224:227], v0 offset:35840\n"
"v_fma_f32 v235, v235, v2, v2\n"
"v_mfma_f32_16x16x32_bf16 v[204:207], a[228:231], v[84:87], v[204:207]\n"
"s_waitcnt lgkmcnt(3)\n"
"buffer_load_dwordx4 a[216:219], v23, s[20:23], s64 offen offset:0 sc1\n"
"v_mfma_f32_16x16x32_bf16 v[192:195], a[232:235], v[176:179], v[192:195]\n"
"ds_read_b128 a[228:231], v0 offset:26624\n"
"v_mul_f32 v244, v244, v240\n"
"v_mfma_f32_16x16x32_bf16 v[200:203], a[232:235], v[88:91], v[200:203]\n"
"s_waitcnt lgkmcnt(3)\n"
"v_rcp_f32 v232, v232\n"
"v_mfma_f32_16x16x32_bf16 v[196:199], a[236:239], v[176:179], v[196:199]\n"
"ds_read_b128 a[232:235], v0 offset:36864\n"
"v_mul_f32 v232, v232, v236\n"
"v_mfma_f32_16x16x32_bf16 v[204:207], a[236:239], v[88:91], v[204:207]\n"
"s_waitcnt lgkmcnt(3)\n"
"v_rcp_f32 v233, v233\n"
"v_mfma_f32_16x16x32_bf16 v[192:195], a[220:223], v[180:183], v[192:195]\n"
"ds_read_b128 a[236:239], v0 offset:27648\n"
"v_mul_f32 v233, v233, v237\n"
"v_mfma_f32_16x16x32_bf16 v[200:203], a[220:223], v[92:95], v[200:203]\n"
"s_waitcnt lgkmcnt(3)\n"
"v_rcp_f32 v234, v234\n"
"v_mfma_f32_16x16x32_bf16 v[196:199], a[224:227], v[180:183], v[196:199]\n"
"ds_read_b128 a[220:223], v0 offset:37888\n"
"v_mul_f32 v234, v234, v238\n"
"v_mfma_f32_16x16x32_bf16 v[204:207], a[224:227], v[92:95], v[204:207]\n"
"s_waitcnt lgkmcnt(3)\n"
"v_rcp_f32 v235, v235\n"
"v_mfma_f32_16x16x32_bf16 v[192:195], a[228:231], v[184:187], v[192:195]\n"
"ds_read_b128 a[224:227], v0 offset:28672\n"
"v_cvt_pk_bf16_f32 v230, v232, v233\n"
"v_mfma_f32_16x16x32_bf16 v[200:203], a[228:231], v[96:99], v[200:203]\n"
"s_waitcnt lgkmcnt(3)\n"
"v_mul_f32 v235, v235, v239\n"
"v_permlane16_swap_b32 v228, v230\n"
"v_mfma_f32_16x16x32_bf16 v[196:199], a[232:235], v[184:187], v[196:199]\n"
"ds_read_b128 a[228:231], v0 offset:38912\n"
"v_cvt_pk_bf16_f32 v231, v234, v235\n"
"v_mfma_f32_16x16x32_bf16 v[204:207], a[232:235], v[96:99], v[204:207]\n"
"s_waitcnt lgkmcnt(3)\n"
"v_permlane16_swap_b32 v229, v231\n"
"v_mul_f32 v240, v18, v240\n"
"v_mfma_f32_16x16x32_bf16 v[192:195], a[236:239], v[188:191], v[192:195]\n"
"ds_read_b128 a[232:235], v0 offset:29696\n"
"v_mul_f32 v245, v245, v241\n"
"v_mfma_f32_16x16x32_bf16 v[200:203], a[236:239], v[100:103], v[200:203]\n"
"s_waitcnt lgkmcnt(3)\n"
"global_store_dwordx4 v5, v[228:231], s[68:69] offset:1024 sc1\n"
"v_mfma_f32_16x16x32_bf16 v[196:199], a[220:223], v[188:191], v[196:199]\n"
"ds_read_b128 a[236:239], v0 offset:39936\n"
"v_mul_f32 v241, v18, v241\n"
"v_mfma_f32_16x16x32_bf16 v[204:207], a[220:223], v[100:103], v[204:207]\n"
"s_waitcnt lgkmcnt(3)\n"
"v_exp_f32 v240, v240\n"
"v_mfma_f32_16x16x32_bf16 v[192:195], a[224:227], v[112:115], v[192:195]\n"
"s_waitcnt vmcnt(31)\n"
"s_barrier\n"
"ds_read_b128 a[220:223], v0 offset:40960\n"
"v_mul_f32 v246, v246, v242\n"
"v_mfma_f32_16x16x32_bf16 v[200:203], a[224:227], v[104:107], v[200:203]\n"
"s_waitcnt lgkmcnt(3)\n"
"v_exp_f32 v241, v241\n"
"v_mfma_f32_16x16x32_bf16 v[196:199], a[228:231], v[112:115], v[196:199]\n"
"ds_read_b128 a[224:227], v0 offset:51200\n"
"v_mul_f32 v242, v18, v242\n"
"v_mfma_f32_16x16x32_bf16 v[204:207], a[228:231], v[104:107], v[204:207]\n"
"s_waitcnt lgkmcnt(3)\n"
"v_exp_f32 v242, v242\n"
"v_mfma_f32_16x16x32_bf16 v[192:195], a[232:235], v[116:119], v[192:195]\n"
"ds_read_b128 a[228:231], v0 offset:41984\n"
"v_mul_f32 v247, v247, v243\n"
"v_mfma_f32_16x16x32_bf16 v[200:203], a[232:235], v[108:111], v[200:203]\n"
"s_waitcnt lgkmcnt(3)\n"
"v_mul_f32 v243, v18, v243\n"
"v_fma_f32 v240, v240, v19, v19\n"
"v_mfma_f32_16x16x32_bf16 v[196:199], a[236:239], v[116:119], v[196:199]\n"
"ds_read_b128 a[232:235], v0 offset:52224\n"
"v_fma_f32 v241, v241, v19, v19\n"
"v_mfma_f32_16x16x32_bf16 v[204:207], a[236:239], v[108:111], v[204:207]\n"
"s_waitcnt lgkmcnt(3)\n"
"v_exp_f32 v243, v243\n"
"s_waitcnt vmcnt(30)\n"
"v_mfma_f32_16x16x16_bf16 v[192:195], a[240:241], v[26:27], v[192:195]\n"
"v_rcp_f32 v240, v240\n"
"v_fma_f32 v242, v242, v19, v19\n"
"v_mfma_f32_16x16x16_bf16 v[196:199], a[242:243], v[26:27], v[196:199]\n"
"v_rcp_f32 v241, v241\n"
"v_mul_f32 v240, v240, v244\n"
"v_mfma_f32_16x16x16_bf16 v[200:203], a[240:241], v[28:29], v[200:203]\n"
"v_rcp_f32 v242, v242\n"
"v_mul_f32 v241, v241, v245\n"
"v_mfma_f32_16x16x16_bf16 v[204:207], a[242:243], v[28:29], v[204:207]\n"
"v_fma_f32 v243, v243, v19, v19\n"
"v_mul_f32 v242, v242, v246\n"
"v_cvt_pk_bf16_f32 v244, v240, v241\n"
"v_mfma_f32_16x16x32_bf16 v[208:211], a[220:223], v[120:123], 0\n"
"ds_read_b128 a[236:239], v0 offset:43008\n"
"v_mul_f32 v252, v252, v248\n"
"v_mfma_f32_16x16x32_bf16 v[216:219], a[220:223], v[32:35], 0\n"
"s_waitcnt lgkmcnt(3)\n"
"v_rcp_f32 v243, v243\n"
"v_mfma_f32_16x16x32_bf16 v[212:215], a[224:227], v[120:123], 0\n"
"ds_read_b128 a[220:223], v0 offset:53248\n"
"v_mul_f32 v243, v243, v247\n"
"v_mfma_f32_16x16x32_bf16 v[220:223], a[224:227], v[32:35], 0\n"
"s_waitcnt lgkmcnt(3)\n"
"v_cvt_pk_bf16_f32 v245, v242, v243\n"
"v_mul_f32 v248, v25, v248\n"
"v_mfma_f32_16x16x32_bf16 v[208:211], a[228:231], v[124:127], v[208:211]\n"
"ds_read_b128 a[224:227], v0 offset:44032\n"
"v_mul_f32 v253, v253, v249\n"
"v_mfma_f32_16x16x32_bf16 v[216:219], a[228:231], v[36:39], v[216:219]\n"
"s_waitcnt lgkmcnt(3)\n"
"v_exp_f32 v248, v248\n"
"v_mfma_f32_16x16x32_bf16 v[212:215], a[232:235], v[124:127], v[212:215]\n"
"ds_read_b128 a[228:231], v0 offset:54272\n"
"v_mul_f32 v249, v25, v249\n"
"v_mfma_f32_16x16x32_bf16 v[220:223], a[232:235], v[36:39], v[220:223]\n"
"s_waitcnt lgkmcnt(3)\n"
"v_exp_f32 v249, v249\n"
"v_mfma_f32_16x16x32_bf16 v[208:211], a[236:239], v[128:131], v[208:211]\n"
"ds_read_b128 a[232:235], v0 offset:45056\n"
"v_mul_f32 v254, v254, v250\n"
"v_mfma_f32_16x16x32_bf16 v[216:219], a[236:239], v[40:43], v[216:219]\n"
"s_waitcnt lgkmcnt(3)\n"
"v_mul_f32 v250, v25, v250\n"
"v_mul_f32 v255, v255, v251\n"
"v_mfma_f32_16x16x32_bf16 v[212:215], a[220:223], v[128:131], v[212:215]\n"
"ds_read_b128 a[236:239], v0 offset:55296\n"
"v_mul_f32 v251, v25, v251\n"
"v_mfma_f32_16x16x32_bf16 v[220:223], a[220:223], v[40:43], v[220:223]\n"
"s_waitcnt lgkmcnt(3)\n"
"v_exp_f32 v250, v250\n"
"v_mfma_f32_16x16x32_bf16 v[208:211], a[224:227], v[132:135], v[208:211]\n"
"ds_read_b128 a[220:223], v0 offset:46080\n"
"v_fma_f32 v248, v248, v2, v2\n"
"v_mfma_f32_16x16x32_bf16 v[216:219], a[224:227], v[44:47], v[216:219]\n"
"s_waitcnt lgkmcnt(3)\n"
"v_exp_f32 v251, v251\n"
"v_mfma_f32_16x16x32_bf16 v[212:215], a[228:231], v[132:135], v[212:215]\n"
"ds_read_b128 a[224:227], v0 offset:56320\n"
"v_fma_f32 v249, v249, v2, v2\n"
"v_mfma_f32_16x16x32_bf16 v[220:223], a[228:231], v[44:47], v[220:223]\n"
"s_waitcnt lgkmcnt(3)\n"
"v_rcp_f32 v248, v248\n"
"v_mfma_f32_16x16x32_bf16 v[208:211], a[232:235], v[136:139], v[208:211]\n"
"ds_read_b128 a[228:231], v0 offset:47104\n"
"v_mul_f32 v248, v248, v252\n"
"v_mfma_f32_16x16x32_bf16 v[216:219], a[232:235], v[48:51], v[216:219]\n"
"s_waitcnt lgkmcnt(3)\n"
"v_rcp_f32 v249, v249\n"
"v_mfma_f32_16x16x32_bf16 v[212:215], a[236:239], v[136:139], v[212:215]\n"
"ds_read_b128 a[232:235], v0 offset:57344\n"
"v_fma_f32 v250, v250, v2, v2\n"
"v_mfma_f32_16x16x32_bf16 v[220:223], a[236:239], v[48:51], v[220:223]\n"
"s_waitcnt lgkmcnt(3)\n"
"v_rcp_f32 v250, v250\n"
"v_mfma_f32_16x16x32_bf16 v[208:211], a[220:223], v[140:143], v[208:211]\n"
"ds_read_b128 a[236:239], v0 offset:48128\n"
"v_mul_f32 v249, v249, v253\n"
"v_mfma_f32_16x16x32_bf16 v[216:219], a[220:223], v[52:55], v[216:219]\n"
"s_waitcnt lgkmcnt(3)\n"
"v_fma_f32 v251, v251, v2, v2\n"
"v_mul_f32 v250, v250, v254\n"
"v_mfma_f32_16x16x32_bf16 v[212:215], a[224:227], v[140:143], v[212:215]\n"
"ds_read_b128 a[220:223], v0 offset:58368\n"
"v_cvt_pk_bf16_f32 v246, v248, v249\n"
"v_mfma_f32_16x16x32_bf16 v[220:223], a[224:227], v[52:55], v[220:223]\n"
"s_waitcnt lgkmcnt(3)\n"
"v_rcp_f32 v251, v251\n"
"v_mfma_f32_16x16x32_bf16 v[208:211], a[228:231], v[144:147], v[208:211]\n"
"ds_read_b128 a[224:227], v0 offset:49152\n"
"v_mul_f32 v251, v251, v255\n"
"v_mfma_f32_16x16x32_bf16 v[216:219], a[228:231], v[56:59], v[216:219]\n"
"s_waitcnt lgkmcnt(3)\n"
"v_cvt_pk_bf16_f32 v247, v250, v251\n"
"v_permlane16_swap_b32 v244, v246\n"
"v_mfma_f32_16x16x32_bf16 v[212:215], a[232:235], v[144:147], v[212:215]\n"
"ds_read_b128 a[228:231], v0 offset:59392\n"
"v_permlane16_swap_b32 v245, v247\n"
"v_mfma_f32_16x16x32_bf16 v[220:223], a[232:235], v[56:59], v[220:223]\n"
"s_waitcnt lgkmcnt(3)\n"
"global_store_dwordx4 v5, v[244:247], s[68:69] offset:1536 sc1\n"
"v_mfma_f32_16x16x32_bf16 v[208:211], a[236:239], v[148:151], v[208:211]\n"
"ds_read_b128 a[232:235], v0 offset:50176\n"
"s_add_u32 s28, s72, s70\n"
"v_mfma_f32_16x16x32_bf16 v[216:219], a[236:239], v[60:63], v[216:219]\n"
"s_waitcnt lgkmcnt(3)\n"
"v_mul_f32 v196, v196, v192\n"
"v_mul_f32 v192, v18, v192\n"
"v_mfma_f32_16x16x32_bf16 v[212:215], a[220:223], v[148:151], v[212:215]\n"
"ds_read_b128 a[236:239], v0 offset:60416\n"
"v_mul_f32 v197, v197, v193\n"
"v_mfma_f32_16x16x32_bf16 v[220:223], a[220:223], v[60:63], v[220:223]\n"
"s_waitcnt lgkmcnt(3)\n"
"global_load_dwordx4 v[240:243], v0, s[28:29] offset:0 sc1\n"
"v_mfma_f32_16x16x32_bf16 v[208:211], a[224:227], v[152:155], v[208:211]\n"
"ds_read_b128 a[220:223], v0 offset:61440\n"
"v_mul_f32 v193, v18, v193\n"
"v_mfma_f32_16x16x32_bf16 v[216:219], a[224:227], v[64:67], v[216:219]\n"
"s_waitcnt lgkmcnt(3)\n"
"v_exp_f32 v192, v192\n"
"v_mfma_f32_16x16x32_bf16 v[212:215], a[228:231], v[152:155], v[212:215]\n"
"ds_read_b128 a[224:227], v3 offset:6144\n"
"v_mul_f32 v198, v198, v194\n"
"v_mfma_f32_16x16x32_bf16 v[220:223], a[228:231], v[64:67], v[220:223]\n"
"s_waitcnt lgkmcnt(3)\n"
"global_load_dwordx4 v[244:247], v20, s[28:29] offset:0 sc1\n"
"v_mfma_f32_16x16x32_bf16 v[208:211], a[232:235], v[156:159], v[208:211]\n"
"ds_read_b128 a[228:231], v0 offset:62464\n"
"v_mul_f32 v194, v18, v194\n"
"v_mfma_f32_16x16x32_bf16 v[216:219], a[232:235], v[68:71], v[216:219]\n"
"s_waitcnt lgkmcnt(3)\n"
"v_exp_f32 v193, v193\n"
"v_mfma_f32_16x16x32_bf16 v[212:215], a[236:239], v[156:159], v[212:215]\n"
"ds_read_b128 a[232:235], v3 offset:7168\n"
"v_mul_f32 v199, v199, v195\n"
"v_mfma_f32_16x16x32_bf16 v[220:223], a[236:239], v[68:71], v[220:223]\n"
"s_waitcnt lgkmcnt(3)\n"
"global_load_dwordx4 v[248:251], v21, s[28:29] offset:0 sc1\n"
"v_mfma_f32_16x16x32_bf16 v[208:211], a[220:223], v[160:163], v[208:211]\n"
"ds_read_b128 a[236:239], v0 offset:63488\n"
"v_mul_f32 v195, v18, v195\n"
"v_mfma_f32_16x16x32_bf16 v[216:219], a[220:223], v[72:75], v[216:219]\n"
"s_waitcnt lgkmcnt(3)\n"
"v_exp_f32 v194, v194\n"
"v_mfma_f32_16x16x32_bf16 v[212:215], a[224:227], v[160:163], v[212:215]\n"
"ds_read_b128 a[220:223], v3 offset:8192\n"
"v_fma_f32 v192, v192, v19, v19\n"
"v_mfma_f32_16x16x32_bf16 v[220:223], a[224:227], v[72:75], v[220:223]\n"
"s_waitcnt lgkmcnt(3)\n"
"global_load_dwordx4 v[252:255], v22, s[28:29] offset:0 sc1\n"
"v_mfma_f32_16x16x32_bf16 v[208:211], a[228:231], v[164:167], v[208:211]\n"
"ds_read_b128 a[224:227], v0 offset:64512\n"
"v_fma_f32 v193, v193, v19, v19\n"
"v_mfma_f32_16x16x32_bf16 v[216:219], a[228:231], v[76:79], v[216:219]\n"
"s_waitcnt lgkmcnt(3)\n"
"v_exp_f32 v195, v195\n"
"v_mfma_f32_16x16x32_bf16 v[212:215], a[232:235], v[164:167], v[212:215]\n"
"ds_read_b128 a[228:231], v3 offset:9216\n"
"v_fma_f32 v194, v194, v19, v19\n"
"v_mfma_f32_16x16x32_bf16 v[220:223], a[232:235], v[76:79], v[220:223]\n"
"s_waitcnt lgkmcnt(3)\n"
"v_rcp_f32 v192, v192\n"
"v_mfma_f32_16x16x32_bf16 v[208:211], a[236:239], v[168:171], v[208:211]\n"
"ds_read_b128 a[232:235], v3 offset:0\n"
"v_mul_f32 v192, v192, v196\n"
"v_mfma_f32_16x16x32_bf16 v[216:219], a[236:239], v[80:83], v[216:219]\n"
"s_waitcnt lgkmcnt(3)\n"
"v_rcp_f32 v193, v193\n"
"v_mfma_f32_16x16x32_bf16 v[212:215], a[220:223], v[168:171], v[212:215]\n"
"ds_read_b128 a[236:239], v3 offset:10240\n"
"v_mul_f32 v193, v193, v197\n"
"v_mfma_f32_16x16x32_bf16 v[220:223], a[220:223], v[80:83], v[220:223]\n"
"s_waitcnt lgkmcnt(3)\n"
"v_rcp_f32 v194, v194\n"
"v_mfma_f32_16x16x32_bf16 v[208:211], a[224:227], v[172:175], v[208:211]\n"
"ds_read_b128 a[220:223], v3 offset:1024\n"
"v_fma_f32 v195, v195, v19, v19\n"
"v_mfma_f32_16x16x32_bf16 v[216:219], a[224:227], v[84:87], v[216:219]\n"
"s_waitcnt lgkmcnt(3)\n"
"v_rcp_f32 v195, v195\n"
"v_mfma_f32_16x16x32_bf16 v[212:215], a[228:231], v[172:175], v[212:215]\n"
"ds_read_b128 a[224:227], v3 offset:11264\n"
"v_mul_f32 v194, v194, v198\n"
"v_mfma_f32_16x16x32_bf16 v[220:223], a[228:231], v[84:87], v[220:223]\n"
"s_waitcnt lgkmcnt(3)\n"
"v_cvt_pk_bf16_f32 v196, v192, v193\n"
"v_mul_f32 v195, v195, v199\n"
"v_mfma_f32_16x16x32_bf16 v[208:211], a[232:235], v[176:179], v[208:211]\n"
"ds_read_b128 a[228:231], v3 offset:2048\n"
"v_cvt_pk_bf16_f32 v197, v194, v195\n"
"v_mfma_f32_16x16x32_bf16 v[216:219], a[232:235], v[88:91], v[216:219]\n"
"s_waitcnt lgkmcnt(3)\n"
"v_mul_f32 v204, v204, v200\n"
"v_mul_f32 v200, v25, v200\n"
"v_mfma_f32_16x16x32_bf16 v[212:215], a[236:239], v[176:179], v[212:215]\n"
"ds_read_b128 a[232:235], v3 offset:12288\n"
"v_mul_f32 v205, v205, v201\n"
"v_mfma_f32_16x16x32_bf16 v[220:223], a[236:239], v[88:91], v[220:223]\n"
"s_waitcnt lgkmcnt(3)\n"
"v_exp_f32 v200, v200\n"
"v_mfma_f32_16x16x32_bf16 v[208:211], a[220:223], v[180:183], v[208:211]\n"
"ds_read_b128 a[236:239], v3 offset:3072\n"
"v_mul_f32 v201, v25, v201\n"
"v_mfma_f32_16x16x32_bf16 v[216:219], a[220:223], v[92:95], v[216:219]\n"
"s_waitcnt lgkmcnt(3)\n"
"v_exp_f32 v201, v201\n"
"v_mfma_f32_16x16x32_bf16 v[212:215], a[224:227], v[180:183], v[212:215]\n"
"ds_read_b128 a[220:223], v3 offset:13312\n"
"v_mul_f32 v206, v206, v202\n"
"v_mfma_f32_16x16x32_bf16 v[220:223], a[224:227], v[92:95], v[220:223]\n"
"s_waitcnt lgkmcnt(3)\n"
"v_mul_f32 v202, v25, v202\n"
"v_mul_f32 v207, v207, v203\n"
"v_mfma_f32_16x16x32_bf16 v[208:211], a[228:231], v[184:187], v[208:211]\n"
"ds_read_b128 a[224:227], v3 offset:4096\n"
"v_mul_f32 v203, v25, v203\n"
"v_mfma_f32_16x16x32_bf16 v[216:219], a[228:231], v[96:99], v[216:219]\n"
"s_waitcnt lgkmcnt(3)\n"
"v_exp_f32 v202, v202\n"
"v_mfma_f32_16x16x32_bf16 v[212:215], a[232:235], v[184:187], v[212:215]\n"
"ds_read_b128 a[228:231], v3 offset:14336\n"
"v_fma_f32 v200, v200, v2, v2\n"
"v_mfma_f32_16x16x32_bf16 v[220:223], a[232:235], v[96:99], v[220:223]\n"
"s_waitcnt lgkmcnt(3)\n"
"v_exp_f32 v203, v203\n"
"v_mfma_f32_16x16x32_bf16 v[208:211], a[236:239], v[188:191], v[208:211]\n"
"ds_read_b128 a[232:235], v3 offset:5120\n"
"v_fma_f32 v201, v201, v2, v2\n"
"v_mfma_f32_16x16x32_bf16 v[216:219], a[236:239], v[100:103], v[216:219]\n"
"s_waitcnt lgkmcnt(3)\n"
"v_rcp_f32 v200, v200\n"
"v_mfma_f32_16x16x32_bf16 v[212:215], a[220:223], v[188:191], v[212:215]\n"
"ds_read_b128 a[236:239], v3 offset:15360\n"
"v_mul_f32 v200, v200, v204\n"
"v_mfma_f32_16x16x32_bf16 v[220:223], a[220:223], v[100:103], v[220:223]\n"
"s_waitcnt lgkmcnt(3)\n"
"v_rcp_f32 v201, v201\n"
"v_mfma_f32_16x16x32_bf16 v[208:211], a[224:227], v[112:115], v[208:211]\n"
"s_waitcnt vmcnt(25)\n"
"s_barrier\n"
"ds_read_b128 a[220:223], v3 offset:16384\n"
"v_fma_f32 v202, v202, v2, v2\n"
"v_mfma_f32_16x16x32_bf16 v[216:219], a[224:227], v[104:107], v[216:219]\n"
"s_waitcnt lgkmcnt(3)\n"
"v_rcp_f32 v202, v202\n"
"v_mfma_f32_16x16x32_bf16 v[212:215], a[228:231], v[112:115], v[212:215]\n"
"ds_read_b128 a[224:227], v3 offset:26624\n"
"v_mul_f32 v201, v201, v205\n"
"v_mfma_f32_16x16x32_bf16 v[220:223], a[228:231], v[104:107], v[220:223]\n"
"s_waitcnt lgkmcnt(3)\n"
"v_fma_f32 v203, v203, v2, v2\n"
"v_mul_f32 v202, v202, v206\n"
"v_mfma_f32_16x16x32_bf16 v[208:211], a[232:235], v[116:119], v[208:211]\n"
"ds_read_b128 a[228:231], v3 offset:17408\n"
"v_cvt_pk_bf16_f32 v198, v200, v201\n"
"v_mfma_f32_16x16x32_bf16 v[216:219], a[232:235], v[108:111], v[216:219]\n"
"s_waitcnt lgkmcnt(3)\n"
"v_rcp_f32 v203, v203\n"
"v_mfma_f32_16x16x32_bf16 v[212:215], a[236:239], v[116:119], v[212:215]\n"
"ds_read_b128 a[232:235], v3 offset:27648\n"
"v_mul_f32 v203, v203, v207\n"
"v_mfma_f32_16x16x32_bf16 v[220:223], a[236:239], v[108:111], v[220:223]\n"
"s_waitcnt lgkmcnt(3)\n"
"v_cvt_pk_bf16_f32 v199, v202, v203\n"
"v_permlane16_swap_b32 v196, v198\n"
"s_waitcnt vmcnt(24)\n"
"v_mfma_f32_16x16x16_bf16 v[208:211], a[244:245], v[26:27], v[208:211]\n"
"v_permlane16_swap_b32 v197, v199\n"
"v_mfma_f32_16x16x16_bf16 v[212:215], a[246:247], v[26:27], v[212:215]\n"
"global_store_dwordx4 v5, v[196:199], s[68:69] offset:2048 sc1\n"
"s_add_u32 s28, s73, s70\n"
"v_mfma_f32_16x16x16_bf16 v[216:219], a[244:245], v[28:29], v[216:219]\n"
"v_mfma_f32_16x16x16_bf16 v[220:223], a[246:247], v[28:29], v[220:223]\n"
"v_mfma_f32_16x16x32_bf16 v[224:227], a[220:223], v[120:123], 0\n"
"ds_read_b128 a[236:239], v3 offset:18432\n"
"v_mfma_f32_16x16x32_bf16 v[232:235], a[220:223], v[32:35], 0\n"
"s_waitcnt lgkmcnt(3)\n"
"v_mfma_f32_16x16x32_bf16 v[228:231], a[224:227], v[120:123], 0\n"
"ds_read_b128 a[220:223], v3 offset:28672\n"
"v_mfma_f32_16x16x32_bf16 v[236:239], a[224:227], v[32:35], 0\n"
"s_waitcnt lgkmcnt(3)\n"
"global_load_dwordx4 v[192:195], v0, s[28:29] offset:0 sc1\n"
"v_mfma_f32_16x16x32_bf16 v[224:227], a[228:231], v[124:127], v[224:227]\n"
"ds_read_b128 a[224:227], v3 offset:19456\n"
"v_mul_f32 v212, v212, v208\n"
"v_mfma_f32_16x16x32_bf16 v[232:235], a[228:231], v[36:39], v[232:235]\n"
"s_waitcnt lgkmcnt(3)\n"
"v_mul_f32 v208, v18, v208\n"
"v_mul_f32 v213, v213, v209\n"
"v_mfma_f32_16x16x32_bf16 v[228:231], a[232:235], v[124:127], v[228:231]\n"
"ds_read_b128 a[228:231], v3 offset:29696\n"
"v_mul_f32 v209, v18, v209\n"
"v_mfma_f32_16x16x32_bf16 v[236:239], a[232:235], v[36:39], v[236:239]\n"
"s_waitcnt lgkmcnt(3)\n"
"global_load_dwordx4 v[196:199], v20, s[28:29] offset:0 sc1\n"
"v_mfma_f32_16x16x32_bf16 v[224:227], a[236:239], v[128:131], v[224:227]\n"
"ds_read_b128 a[232:235], v3 offset:20480\n"
"v_mul_f32 v214, v214, v210\n"
"v_mfma_f32_16x16x32_bf16 v[232:235], a[236:239], v[40:43], v[232:235]\n"
"s_waitcnt lgkmcnt(3)\n"
"v_exp_f32 v208, v208\n"
"v_mfma_f32_16x16x32_bf16 v[228:231], a[220:223], v[128:131], v[228:231]\n"
"ds_read_b128 a[236:239], v3 offset:30720\n"
"v_mul_f32 v210, v18, v210\n"
"v_mfma_f32_16x16x32_bf16 v[236:239], a[220:223], v[40:43], v[236:239]\n"
"s_waitcnt lgkmcnt(3)\n"
"global_load_dwordx4 v[200:203], v21, s[28:29] offset:0 sc1\n"
"v_mfma_f32_16x16x32_bf16 v[224:227], a[224:227], v[132:135], v[224:227]\n"
"ds_read_b128 a[220:223], v3 offset:21504\n"
"v_mul_f32 v215, v215, v211\n"
"v_mfma_f32_16x16x32_bf16 v[232:235], a[224:227], v[44:47], v[232:235]\n"
"s_waitcnt lgkmcnt(3)\n"
"v_exp_f32 v209, v209\n"
"v_mfma_f32_16x16x32_bf16 v[228:231], a[228:231], v[132:135], v[228:231]\n"
"ds_read_b128 a[224:227], v3 offset:31744\n"
"v_mul_f32 v211, v18, v211\n"
"v_mfma_f32_16x16x32_bf16 v[236:239], a[228:231], v[44:47], v[236:239]\n"
"s_waitcnt lgkmcnt(3)\n"
"global_load_dwordx4 v[204:207], v22, s[28:29] offset:0 sc1\n"
"v_mfma_f32_16x16x32_bf16 v[224:227], a[232:235], v[136:139], v[224:227]\n"
"ds_read_b128 a[228:231], v3 offset:22528\n"
"v_fma_f32 v208, v208, v19, v19\n"
"v_mfma_f32_16x16x32_bf16 v[232:235], a[232:235], v[48:51], v[232:235]\n"
"s_waitcnt lgkmcnt(3)\n"
"s_add_u32 s64, s74, s70\n"
"v_fma_f32 v209, v209, v19, v19\n"
"v_mfma_f32_16x16x32_bf16 v[228:231], a[236:239], v[136:139], v[228:231]\n"
"ds_read_b128 a[232:235], v3 offset:32768\n"
"v_mul_f32 v220, v220, v216\n"
"v_mfma_f32_16x16x32_bf16 v[236:239], a[236:239], v[48:51], v[236:239]\n"
"s_waitcnt lgkmcnt(3)\n"
"global_load_dwordx4 v[32:35], v0, s[64:65] offset:0 sc1\n"
"v_mfma_f32_16x16x32_bf16 v[224:227], a[220:223], v[140:143], v[224:227]\n"
"ds_read_b128 a[236:239], v3 offset:23552\n"
"v_mul_f32 v216, v25, v216\n"
"v_mfma_f32_16x16x32_bf16 v[232:235], a[220:223], v[52:55], v[232:235]\n"
"s_waitcnt lgkmcnt(3)\n"
"v_exp_f32 v210, v210\n"
"v_mfma_f32_16x16x32_bf16 v[228:231], a[224:227], v[140:143], v[228:231]\n"
"ds_read_b128 a[220:223], v3 offset:33792\n"
"v_fma_f32 v210, v210, v19, v19\n"
"v_mfma_f32_16x16x32_bf16 v[236:239], a[224:227], v[52:55], v[236:239]\n"
"s_waitcnt lgkmcnt(3)\n"
"global_load_dwordx4 v[36:39], v20, s[64:65] offset:0 sc1\n"
"v_mfma_f32_16x16x32_bf16 v[224:227], a[228:231], v[144:147], v[224:227]\n"
"ds_read_b128 a[224:227], v3 offset:24576\n"
"v_mul_f32 v221, v221, v217\n"
"v_mfma_f32_16x16x32_bf16 v[232:235], a[228:231], v[56:59], v[232:235]\n"
"s_waitcnt lgkmcnt(3)\n"
"v_exp_f32 v211, v211\n"
"v_mfma_f32_16x16x32_bf16 v[228:231], a[232:235], v[144:147], v[228:231]\n"
"ds_read_b128 a[228:231], v3 offset:34816\n"
"v_fma_f32 v211, v211, v19, v19\n"
"v_mfma_f32_16x16x32_bf16 v[236:239], a[232:235], v[56:59], v[236:239]\n"
"s_waitcnt lgkmcnt(3)\n"
"global_load_dwordx4 v[40:43], v21, s[64:65] offset:0 sc1\n"
"v_mfma_f32_16x16x32_bf16 v[224:227], a[236:239], v[148:151], v[224:227]\n"
"ds_read_b128 a[232:235], v3 offset:25600\n"
"v_mul_f32 v217, v25, v217\n"
"v_mfma_f32_16x16x32_bf16 v[232:235], a[236:239], v[60:63], v[232:235]\n"
"s_waitcnt lgkmcnt(3)\n"
"v_rcp_f32 v208, v208\n"
"v_mfma_f32_16x16x32_bf16 v[228:231], a[220:223], v[148:151], v[228:231]\n"
"ds_read_b128 a[236:239], v3 offset:35840\n"
"v_mul_f32 v208, v208, v212\n"
"v_mfma_f32_16x16x32_bf16 v[236:239], a[220:223], v[60:63], v[236:239]\n"
"s_waitcnt lgkmcnt(3)\n"
"global_load_dwordx4 v[44:47], v22, s[64:65] offset:0 sc1\n"
"v_mfma_f32_16x16x32_bf16 v[224:227], a[224:227], v[152:155], v[224:227]\n"
"ds_read_b128 a[220:223], v3 offset:36864\n"
"v_mul_f32 v222, v222, v218\n"
"v_mfma_f32_16x16x32_bf16 v[232:235], a[224:227], v[64:67], v[232:235]\n"
"s_waitcnt lgkmcnt(3)\n"
"s_add_u32 s64, s75, s70\n"
"v_mul_f32 v218, v25, v218\n"
"v_mfma_f32_16x16x32_bf16 v[228:231], a[228:231], v[152:155], v[228:231]\n"
"ds_read_b128 a[224:227], v3 offset:47104\n"
"v_mul_f32 v223, v223, v219\n"
"v_mfma_f32_16x16x32_bf16 v[236:239], a[228:231], v[64:67], v[236:239]\n"
"s_waitcnt lgkmcnt(3)\n"
"global_load_dwordx4 v[48:51], v0, s[64:65] offset:0 sc1\n"
"v_mfma_f32_16x16x32_bf16 v[224:227], a[232:235], v[156:159], v[224:227]\n"
"ds_read_b128 a[228:231], v3 offset:37888\n"
"v_mul_f32 v219, v25, v219\n"
"v_mfma_f32_16x16x32_bf16 v[232:235], a[232:235], v[68:71], v[232:235]\n"
"s_waitcnt lgkmcnt(3)\n"
"v_rcp_f32 v209, v209\n"
"v_mfma_f32_16x16x32_bf16 v[228:231], a[236:239], v[156:159], v[228:231]\n"
"ds_read_b128 a[232:235], v3 offset:48128\n"
"v_mul_f32 v209, v209, v213\n"
"v_mfma_f32_16x16x32_bf16 v[236:239], a[236:239], v[68:71], v[236:239]\n"
"s_waitcnt lgkmcnt(3)\n"
"global_load_dwordx4 v[52:55], v20, s[64:65] offset:0 sc1\n"
"v_mfma_f32_16x16x32_bf16 v[224:227], a[220:223], v[160:163], v[224:227]\n"
"ds_read_b128 a[236:239], v3 offset:38912\n"
"v_cvt_pk_bf16_f32 v212, v208, v209\n"
"v_mfma_f32_16x16x32_bf16 v[232:235], a[220:223], v[72:75], v[232:235]\n"
"s_waitcnt lgkmcnt(3)\n"
"v_rcp_f32 v210, v210\n"
"v_mfma_f32_16x16x32_bf16 v[228:231], a[224:227], v[160:163], v[228:231]\n"
"ds_read_b128 a[220:223], v3 offset:49152\n"
"v_mul_f32 v210, v210, v214\n"
"v_mfma_f32_16x16x32_bf16 v[236:239], a[224:227], v[72:75], v[236:239]\n"
"s_waitcnt lgkmcnt(3)\n"
"global_load_dwordx4 v[56:59], v21, s[64:65] offset:0 sc1\n"
"v_mfma_f32_16x16x32_bf16 v[224:227], a[228:231], v[164:167], v[224:227]\n"
"ds_read_b128 a[224:227], v3 offset:39936\n"
"v_mfma_f32_16x16x32_bf16 v[232:235], a[228:231], v[76:79], v[232:235]\n"
"s_waitcnt lgkmcnt(3)\n"
"v_rcp_f32 v211, v211\n"
"v_mfma_f32_16x16x32_bf16 v[228:231], a[232:235], v[164:167], v[228:231]\n"
"ds_read_b128 a[228:231], v3 offset:50176\n"
"v_mul_f32 v211, v211, v215\n"
"v_mfma_f32_16x16x32_bf16 v[236:239], a[232:235], v[76:79], v[236:239]\n"
"s_waitcnt lgkmcnt(3)\n"
"global_load_dwordx4 v[60:63], v22, s[64:65] offset:0 sc1\n"
"v_mfma_f32_16x16x32_bf16 v[224:227], a[236:239], v[168:171], v[224:227]\n"
"ds_read_b128 a[232:235], v3 offset:40960\n"
"v_cvt_pk_bf16_f32 v213, v210, v211\n"
"v_mfma_f32_16x16x32_bf16 v[232:235], a[236:239], v[80:83], v[232:235]\n"
"s_waitcnt lgkmcnt(3)\n"
"s_add_u32 s64, s76, s70\n"
"v_mfma_f32_16x16x32_bf16 v[228:231], a[220:223], v[168:171], v[228:231]\n"
"ds_read_b128 a[236:239], v3 offset:51200\n"
"v_mfma_f32_16x16x32_bf16 v[236:239], a[220:223], v[80:83], v[236:239]\n"
"s_waitcnt lgkmcnt(3)\n"
"global_load_dwordx4 v[64:67], v0, s[64:65] offset:0 sc1\n"
"v_mfma_f32_16x16x32_bf16 v[224:227], a[224:227], v[172:175], v[224:227]\n"
"ds_read_b128 a[220:223], v3 offset:41984\n"
"v_mfma_f32_16x16x32_bf16 v[232:235], a[224:227], v[84:87], v[232:235]\n"
"s_waitcnt lgkmcnt(3)\n"
"v_exp_f32 v216, v216\n"
"v_mfma_f32_16x16x32_bf16 v[228:231], a[228:231], v[172:175], v[228:231]\n"
"ds_read_b128 a[224:227], v3 offset:52224\n"
"v_fma_f32 v216, v216, v2, v2\n"
"v_mfma_f32_16x16x32_bf16 v[236:239], a[228:231], v[84:87], v[236:239]\n"
"s_waitcnt lgkmcnt(3)\n"
"global_load_dwordx4 v[68:71], v20, s[64:65] offset:0 sc1\n"
"v_mfma_f32_16x16x32_bf16 v[224:227], a[232:235], v[176:179], v[224:227]\n"
"ds_read_b128 a[228:231], v3 offset:43008\n"
"v_mfma_f32_16x16x32_bf16 v[232:235], a[232:235], v[88:91], v[232:235]\n"
"s_waitcnt lgkmcnt(3)\n"
"v_exp_f32 v217, v217\n"
"v_mfma_f32_16x16x32_bf16 v[228:231], a[236:239], v[176:179], v[228:231]\n"
".Lhop10:\n"
"s_cbranch_execz .Lhop_ret\n"
"ds_read_b128 a[232:235], v3 offset:53248\n"
"v_fma_f32 v217, v217, v2, v2\n"
"v_mfma_f32_16x16x32_bf16 v[236:239], a[236:239], v[88:91], v[236:239]\n"
"s_waitcnt lgkmcnt(3)\n"
"global_load_dwordx4 v[72:75], v21, s[64:65] offset:0 sc1\n"
"v_mfma_f32_16x16x32_bf16 v[224:227], a[220:223], v[180:183], v[224:227]\n"
"ds_read_b128 a[236:239], v3 offset:44032\n"
"v_mfma_f32_16x16x32_bf16 v[232:235], a[220:223], v[92:95], v[232:235]\n"
"s_waitcnt lgkmcnt(3)\n"
"v_exp_f32 v218, v218\n"
"v_mfma_f32_16x16x32_bf16 v[228:231], a[224:227], v[180:183], v[228:231]\n"
"ds_read_b128 a[220:223], v3 offset:54272\n"
"v_fma_f32 v218, v218, v2, v2\n"
"v_mfma_f32_16x16x32_bf16 v[236:239], a[224:227], v[92:95], v[236:239]\n"
"s_waitcnt lgkmcnt(3)\n"
"global_load_dwordx4 v[76:79], v22, s[64:65] offset:0 sc1\n"
"v_mfma_f32_16x16x32_bf16 v[224:227], a[228:231], v[184:187], v[224:227]\n"
"ds_read_b128 a[224:227], v3 offset:45056\n"
"v_mfma_f32_16x16x32_bf16 v[232:235], a[228:231], v[96:99], v[232:235]\n"
"s_waitcnt lgkmcnt(3)\n"
"s_add_u32 s64, s77, s70\n"
"v_mfma_f32_16x16x32_bf16 v[228:231], a[232:235], v[184:187], v[228:231]\n"
"ds_read_b128 a[228:231], v3 offset:55296\n"
"v_mfma_f32_16x16x32_bf16 v[236:239], a[232:235], v[96:99], v[236:239]\n"
"s_waitcnt lgkmcnt(3)\n"
"global_load_dwordx4 v[80:83], v0, s[64:65] offset:0 sc1\n"
"v_mfma_f32_16x16x32_bf16 v[224:227], a[236:239], v[188:191], v[224:227]\n"
"ds_read_b128 a[232:235], v3 offset:46080\n"
"v_mfma_f32_16x16x32_bf16 v[232:235], a[236:239], v[100:103], v[232:235]\n"
"s_waitcnt lgkmcnt(3)\n"
"v_exp_f32 v219, v219\n"
"v_mfma_f32_16x16x32_bf16 v[228:231], a[220:223], v[188:191], v[228:231]\n"
"ds_read_b128 a[236:239], v3 offset:56320\n"
"v_fma_f32 v219, v219, v2, v2\n"
"v_mfma_f32_16x16x32_bf16 v[236:239], a[220:223], v[100:103], v[236:239]\n"
"s_waitcnt lgkmcnt(3)\n"
"global_load_dwordx4 v[84:87], v20, s[64:65] offset:0 sc1\n"
"v_mfma_f32_16x16x32_bf16 v[224:227], a[224:227], v[112:115], v[224:227]\n"
"v_rcp_f32 v216, v216\n"
"v_mfma_f32_16x16x32_bf16 v[232:235], a[224:227], v[104:107], v[232:235]\n"
"s_waitcnt lgkmcnt(2)\n"
"v_rcp_f32 v217, v217\n"
"v_mfma_f32_16x16x32_bf16 v[228:231], a[228:231], v[112:115], v[228:231]\n"
"v_rcp_f32 v218, v218\n"
"v_mfma_f32_16x16x32_bf16 v[236:239], a[228:231], v[104:107], v[236:239]\n"
"s_waitcnt lgkmcnt(1)\n"
"global_load_dwordx4 v[88:91], v21, s[64:65] offset:0 sc1\n"
"v_mfma_f32_16x16x32_bf16 v[224:227], a[232:235], v[116:119], v[224:227]\n"
"v_rcp_f32 v219, v219\n"
"v_mfma_f32_16x16x32_bf16 v[232:235], a[232:235], v[108:111], v[232:235]\n"
"s_waitcnt lgkmcnt(0)\n"
"v_mul_f32 v216, v216, v220\n"
"v_mul_f32 v217, v217, v221\n"
"v_mfma_f32_16x16x32_bf16 v[228:231], a[236:239], v[116:119], v[228:231]\n"
"v_mul_f32 v218, v218, v222\n"
"v_cvt_pk_bf16_f32 v214, v216, v217\n"
"v_mfma_f32_16x16x32_bf16 v[236:239], a[236:239], v[108:111], v[236:239]\n"
"global_load_dwordx4 v[92:95], v22, s[64:65] offset:0 sc1\n"
"s_waitcnt vmcnt(44)\n"
"v_mfma_f32_16x16x16_bf16 v[224:227], a[248:249], v[26:27], v[224:227]\n"
"v_mul_f32 v219, v219, v223\n"
"v_permlane16_swap_b32 v212, v214\n"
"v_cvt_pk_bf16_f32 v215, v218, v219\n"
"v_mfma_f32_16x16x16_bf16 v[228:231], a[250:251], v[26:27], v[228:231]\n"
"s_nop 0\n"
"v_permlane16_swap_b32 v213, v215\n"
"v_mfma_f32_16x16x16_bf16 v[232:235], a[248:249], v[28:29], v[232:235]\n"
"v_mfma_f32_16x16x16_bf16 v[236:239], a[250:251], v[28:29], v[236:239]\n"
"global_store_dwordx4 v5, v[212:215], s[68:69] offset:2560 sc1\n"
"s_waitcnt vmcnt(22)\n"
".Lchk_hh0:\n"
"v_max_u32 v30, v240, v241\n"
"v_max3_u32 v30, v242, v243, v30\n"
"v_max3_u32 v30, v244, v245, v30\n"
"v_max3_u32 v30, v246, v247, v30\n"
"v_max3_u32 v30, v248, v249, v30\n"
"v_max3_u32 v30, v250, v251, v30\n"
"v_max3_u32 v30, v252, v253, v30\n"
"v_max3_u32 v30, v254, v255, v30\n"
"v_cmp_eq_u32 vcc, 0xffbfffbf, v30\n"
"s_nop 1\n"
"s_cbranch_vccnz .Lre_hh0\n"
".Lok_hh0:\n"
"v_mfma_f32_16x16x32_bf16 v[112:115], a[0:3], v[240:243], 0\n"
"s_add_u32 s64, s78, s70\n"
"v_mul_f32 v228, v228, v224\n"
"v_mul_f32 v224, v18, v224\n"
"v_exp_f32 v224, v224\n"
"v_mfma_f32_16x16x32_bf16 v[116:119], a[4:7], v[240:243], 0\n"
"global_load_dwordx4 v[96:99], v0, s[64:65] offset:0 sc1\n"
"v_mul_f32 v229, v229, v225\n"
"v_mul_f32 v225, v18, v225\n"
"v_mfma_f32_16x16x32_bf16 v[120:123], a[8:11], v[240:243], 0\n"
"v_exp_f32 v225, v225\n"
"v_mul_f32 v230, v230, v226\n"
"v_mul_f32 v226, v18, v226\n"
"v_mfma_f32_16x16x32_bf16 v[124:127], a[12:15], v[240:243], 0\n"
"v_exp_f32 v226, v226\n"
"v_mul_f32 v231, v231, v227\n"
"v_mul_f32 v227, v18, v227\n"
"v_mfma_f32_16x16x32_bf16 v[128:131], a[16:19], v[240:243], 0\n"
"v_exp_f32 v227, v227\n"
"v_fma_f32 v224, v224, v19, v19\n"
"v_mfma_f32_16x16x32_bf16 v[132:135], a[0:3], v[244:247], 0\n"
"global_load_dwordx4 v[100:103], v20, s[64:65] offset:0 sc1\n"
"v_rcp_f32 v224, v224\n"
"v_mfma_f32_16x16x32_bf16 v[136:139], a[4:7], v[244:247], 0\n"
"v_fma_f32 v225, v225, v19, v19\n"
"v_rcp_f32 v225, v225\n"
"v_mul_f32 v224, v224, v228\n"
"v_mfma_f32_16x16x32_bf16 v[140:143], a[8:11], v[244:247], 0\n"
"v_fma_f32 v226, v226, v19, v19\n"
"v_rcp_f32 v226, v226\n"
"v_mul_f32 v225, v225, v229\n"
"v_mfma_f32_16x16x32_bf16 v[144:147], a[12:15], v[244:247], 0\n"
"v_fma_f32 v227, v227, v19, v19\n"
"v_rcp_f32 v227, v227\n"
"v_mul_f32 v226, v226, v230\n"
"v_mfma_f32_16x16x32_bf16 v[148:151], a[16:19], v[244:247], 0\n"
"global_load_dwordx4 v[104:107], v21, s[64:65] offset:0 sc1\n"
"s_waitcnt vmcnt(20)\n"
"v_max_u32 v30, v192, v193\n"
"v_mfma_f32_16x16x32_bf16 v[152:155], a[0:3], v[248:251], 0\n"
"v_max3_u32 v30, v194, v195, v30\n"
"v_max3_u32 v30, v196, v197, v30\n"
"v_max3_u32 v30, v198, v199, v30\n"
"v_max3_u32 v30, v200, v201, v30\n"
"v_mfma_f32_16x16x32_bf16 v[156:159], a[4:7], v[248:251], 0\n"
"v_max3_u32 v30, v202, v203, v30\n"
"v_max3_u32 v30, v204, v205, v30\n"
"v_max3_u32 v30, v206, v207, v30\n"
"v_cmp_eq_u32 vcc, 0xffbfffbf, v30\n"
"v_mfma_f32_16x16x32_bf16 v[160:163], a[8:11], v[248:251], 0\n"
"v_cvt_pk_bf16_f32 v228, v224, v225\n"
"v_mul_f32 v227, v227, v231\n"
"v_cvt_pk_bf16_f32 v229, v226, v227\n"
"v_mul_f32 v236, v236, v232\n"
"v_mfma_f32_16x16x32_bf16 v[164:167], a[12:15], v[248:251], 0\n"
"global_load_dwordx4 v[108:111], v22, s[64:65] offset:0 sc1\n"
"s_add_u32 s64, s79, s70\n"
"v_mul_f32 v232, v25, v232\n"
"v_mfma_f32_16x16x32_bf16 v[168:171], a[16:19], v[248:251], 0\n"
"v_exp_f32 v232, v232\n"
"v_mul_f32 v237, v237, v233\n"
"v_mul_f32 v233, v25, v233\n"
"v_mfma_f32_16x16x32_bf16 v[172:175], a[0:3], v[252:255], 0\n"
"v_exp_f32 v233, v233\n"
"v_mul_f32 v238, v238, v234\n"
"v_mul_f32 v234, v25, v234\n"
"v_mfma_f32_16x16x32_bf16 v[176:179], a[4:7], v[252:255], 0\n"
"v_exp_f32 v234, v234\n"
"v_mul_f32 v239, v239, v235\n"
"v_mul_f32 v235, v25, v235\n"
"v_mfma_f32_16x16x32_bf16 v[180:183], a[8:11], v[252:255], 0\n"
"global_load_dwordx4 v[208:211], v0, s[64:65] offset:0 sc1\n"
"v_exp_f32 v235, v235\n"
"v_mfma_f32_16x16x32_bf16 v[184:187], a[12:15], v[252:255], 0\n"
"v_fma_f32 v232, v232, v2, v2\n"
"v_rcp_f32 v232, v232\n"
"v_fma_f32 v233, v233, v2, v2\n"
"v_mfma_f32_16x16x32_bf16 v[188:191], a[16:19], v[252:255], 0\n"
"v_rcp_f32 v233, v233\n"
"v_mul_f32 v232, v232, v236\n"
"v_fma_f32 v234, v234, v2, v2\n"
"s_cbranch_vccnz .Lre_hh1\n"
".Lok_hh1:\n"
"v_mfma_f32_16x16x32_bf16 v[112:115], a[20:23], v[192:195], v[112:115]\n"
"v_rcp_f32 v234, v234\n"
"v_mul_f32 v233, v233, v237\n"
"v_fma_f32 v235, v235, v2, v2\n"
"v_mfma_f32_16x16x32_bf16 v[116:119], a[24:27], v[192:195], v[116:119]\n"
"global_load_dwordx4 v[212:215], v20, s[64:65] offset:0 sc1\n"
"v_rcp_f32 v235, v235\n"
"v_mfma_f32_16x16x32_bf16 v[120:123], a[28:31], v[192:195], v[120:123]\n"
"v_mul_f32 v234, v234, v238\n"
"v_cvt_pk_bf16_f32 v230, v232, v233\n"
"v_mul_f32 v235, v235, v239\n"
"v_cvt_pk_bf16_f32 v231, v234, v235\n"
"v_mfma_f32_16x16x32_bf16 v[124:127], a[32:35], v[192:195], v[124:127]\n"
"v_permlane16_swap_b32 v228, v230\n"
"v_permlane16_swap_b32 v229, v231\n"
"v_mfma_f32_16x16x32_bf16 v[128:131], a[36:39], v[192:195], v[128:131]\n"
"v_mfma_f32_16x16x32_bf16 v[132:135], a[20:23], v[196:199], v[132:135]\n"
"buffer_store_dwordx4 v[228:231], v5, s[20:23], s57 offen offset:3072 sc1\n"
"v_mfma_f32_16x16x32_bf16 v[136:139], a[24:27], v[196:199], v[136:139]\n"
"s_add_u32 s28, s80, s70\n"
"v_mfma_f32_16x16x32_bf16 v[140:143], a[28:31], v[196:199], v[140:143]\n"
"v_mfma_f32_16x16x32_bf16 v[144:147], a[32:35], v[196:199], v[144:147]\n"
"v_mfma_f32_16x16x32_bf16 v[148:151], a[36:39], v[196:199], v[148:151]\n"
"global_load_dwordx4 v[224:227], v0, s[28:29] offset:0 sc1\n"
"s_waitcnt vmcnt(21)\n"
"v_max_u32 v30, v32, v33\n"
"v_mfma_f32_16x16x32_bf16 v[152:155], a[20:23], v[200:203], v[152:155]\n"
"v_max3_u32 v30, v34, v35, v30\n"
"v_max3_u32 v30, v36, v37, v30\n"
"v_mfma_f32_16x16x32_bf16 v[156:159], a[24:27], v[200:203], v[156:159]\n"
"v_max3_u32 v30, v38, v39, v30\n"
"v_max3_u32 v30, v40, v41, v30\n"
"v_mfma_f32_16x16x32_bf16 v[160:163], a[28:31], v[200:203], v[160:163]\n"
"v_max3_u32 v30, v42, v43, v30\n"
"v_max3_u32 v30, v44, v45, v30\n"
"v_mfma_f32_16x16x32_bf16 v[164:167], a[32:35], v[200:203], v[164:167]\n"
"global_load_dwordx4 v[228:231], v20, s[28:29] offset:0 sc1\n"
"v_mfma_f32_16x16x32_bf16 v[168:171], a[36:39], v[200:203], v[168:171]\n"
"v_max3_u32 v30, v46, v47, v30\n"
"v_cmp_eq_u32 vcc, 0xffbfffbf, v30\n"
"v_mfma_f32_16x16x32_bf16 v[172:175], a[20:23], v[204:207], v[172:175]\n"
"v_mfma_f32_16x16x32_bf16 v[176:179], a[24:27], v[204:207], v[176:179]\n"
"v_mfma_f32_16x16x32_bf16 v[180:183], a[28:31], v[204:207], v[180:183]\n"
"global_load_dwordx4 v[232:235], v21, s[28:29] offset:0 sc1\n"
"v_mfma_f32_16x16x32_bf16 v[184:187], a[32:35], v[204:207], v[184:187]\n"
"v_mfma_f32_16x16x32_bf16 v[188:191], a[36:39], v[204:207], v[188:191]\n"
"s_cbranch_vccnz .Lre_hh2\n"
".Lok_hh2:\n"
"v_mfma_f32_16x16x32_bf16 v[112:115], a[40:43], v[32:35], v[112:115]\n"
"v_mfma_f32_16x16x32_bf16 v[116:119], a[44:47], v[32:35], v[116:119]\n"
"global_load_dwordx4 v[236:239], v22, s[28:29] offset:0 sc1\n"
"v_mfma_f32_16x16x32_bf16 v[120:123], a[48:51], v[32:35], v[120:123]\n"
"v_add_u32 v5, 40960, v24\n"
"v_lshrrev_b32 v2, 4, v0\n"
"v_mfma_f32_16x16x32_bf16 v[124:127], a[52:55], v[32:35], v[124:127]\n"
"s_lshl_b32 s6, s86, 2\n"
"v_lshrrev_b32 v28, 2, v2\n"
"v_and_b32 v28, 8, v28\n"
"v_mfma_f32_16x16x32_bf16 v[128:131], a[56:59], v[32:35], v[128:131]\n"
"v_bfe_u32 v29, v2, 4, 1\n"
"v_xor_b32 v28, v28, v2\n"
"v_mfma_f32_16x16x32_bf16 v[132:135], a[40:43], v[36:39], v[132:135]\n"
"global_load_dwordx4 v[216:219], v21, s[64:65] offset:0 sc1\n"
"v_mfma_f32_16x16x32_bf16 v[136:139], a[44:47], v[36:39], v[136:139]\n"
"v_xor_b32 v28, v28, v29\n"
"v_lshl_add_u32 v25, v28, 4, s6\n"
"v_mfma_f32_16x16x32_bf16 v[140:143], a[48:51], v[36:39], v[140:143]\n"
"v_and_b32 v28, 2, v2\n"
"v_and_b32 v29, 1, v2\n"
"v_mfma_f32_16x16x32_bf16 v[144:147], a[52:55], v[36:39], v[144:147]\n"
"v_lshl_add_u32 v28, v28, 2, v29\n"
"v_lshrrev_b32 v29, 2, v2\n"
"v_mfma_f32_16x16x32_bf16 v[148:151], a[56:59], v[36:39], v[148:151]\n"
"global_load_dwordx4 v[220:223], v22, s[64:65] offset:0 sc1\n"
"v_mfma_f32_16x16x32_bf16 v[152:155], a[40:43], v[40:43], v[152:155]\n"
"s_add_u32 s64, s83, s71\n"
"s_waitcnt vmcnt(22)\n"
"v_max_u32 v30, v48, v49\n"
"v_mfma_f32_16x16x32_bf16 v[156:159], a[44:47], v[40:43], v[156:159]\n"
"v_max3_u32 v30, v50, v51, v30\n"
"v_max3_u32 v30, v52, v53, v30\n"
"v_mfma_f32_16x16x32_bf16 v[160:163], a[48:51], v[40:43], v[160:163]\n"
"v_max3_u32 v30, v54, v55, v30\n"
"v_max3_u32 v30, v56, v57, v30\n"
"v_mfma_f32_16x16x32_bf16 v[164:167], a[52:55], v[40:43], v[164:167]\n"
"global_load_dwordx4 a[0:3], v0, s[64:65] offset:0 sc1\n"
"v_mfma_f32_16x16x32_bf16 v[168:171], a[56:59], v[40:43], v[168:171]\n"
"v_max3_u32 v30, v58, v59, v30\n"
"v_max3_u32 v30, v60, v61, v30\n"
"v_mfma_f32_16x16x32_bf16 v[172:175], a[40:43], v[44:47], v[172:175]\n"
"v_max3_u32 v30, v62, v63, v30\n"
"v_cmp_eq_u32 vcc, 0xffbfffbf, v30\n"
"v_mfma_f32_16x16x32_bf16 v[176:179], a[44:47], v[44:47], v[176:179]\n"
"v_xor_b32 v28, v28, v29\n"
"v_and_b32 v29, 3, v2\n"
"v_mfma_f32_16x16x32_bf16 v[180:183], a[48:51], v[44:47], v[180:183]\n"
"global_load_dwordx4 a[4:7], v20, s[64:65] offset:0 sc1\n"
"v_mfma_f32_16x16x32_bf16 v[184:187], a[52:55], v[44:47], v[184:187]\n"
"v_lshl_add_u32 v28, v29, 4, v28\n"
"s_and_b32 s7, s56, 1\n"
"v_mfma_f32_16x16x32_bf16 v[188:191], a[56:59], v[44:47], v[188:191]\n"
"s_mul_i32 s7, s7, 5120\n"
"s_lshr_b32 s8, s56, 1\n"
"s_mul_i32 s8, s8, 3072\n"
"s_cbranch_vccnz .Lre_hh3\n"
".Lok_hh3:\n"
"v_mfma_f32_16x16x32_bf16 v[112:115], a[60:63], v[48:51], v[112:115]\n"
"s_add_u32 s7, s7, s8\n"
"v_lshl_add_u32 v26, v28, 4, s7\n"
"v_add_u32 v27, 40960, v26\n"
"v_mfma_f32_16x16x32_bf16 v[116:119], a[64:67], v[48:51], v[116:119]\n"
"global_load_dwordx4 a[8:11], v21, s[64:65] offset:0 sc1\n"
"v_mfma_f32_16x16x32_bf16 v[120:123], a[68:71], v[48:51], v[120:123]\n"
"s_barrier\n"
"v_mfma_f32_16x16x32_bf16 v[124:127], a[72:75], v[48:51], v[124:127]\n"
"v_mfma_f32_16x16x32_bf16 v[128:131], a[76:79], v[48:51], v[128:131]\n"
"v_mfma_f32_16x16x32_bf16 v[132:135], a[60:63], v[52:55], v[132:135]\n"
"global_load_dwordx4 a[12:15], v22, s[64:65] offset:0 sc1\n"
"v_mfma_f32_16x16x32_bf16 v[136:139], a[64:67], v[52:55], v[136:139]\n"
"v_mfma_f32_16x16x32_bf16 v[140:143], a[68:71], v[52:55], v[140:143]\n"
"v_mfma_f32_16x16x32_bf16 v[144:147], a[72:75], v[52:55], v[144:147]\n"
"v_mfma_f32_16x16x32_bf16 v[148:151], a[76:79], v[52:55], v[148:151]\n"
"global_load_dwordx4 a[16:19], v23, s[64:65] offset:0 sc1\n"
"v_mfma_f32_16x16x32_bf16 v[152:155], a[60:63], v[56:59], v[152:155]\n"
"s_add_u32 s64, s84, s71\n"
"s_waitcnt vmcnt(23)\n"
"v_max_u32 v30, v64, v65\n"
"v_mfma_f32_16x16x32_bf16 v[156:159], a[64:67], v[56:59], v[156:159]\n"
"v_max3_u32 v30, v66, v67, v30\n"
"v_max3_u32 v30, v68, v69, v30\n"
"v_mfma_f32_16x16x32_bf16 v[160:163], a[68:71], v[56:59], v[160:163]\n"
"v_max3_u32 v30, v70, v71, v30\n"
"v_max3_u32 v30, v72, v73, v30\n"
"v_mfma_f32_16x16x32_bf16 v[164:167], a[72:75], v[56:59], v[164:167]\n"
"global_load_dwordx4 a[20:23], v0, s[64:65] offset:0 sc1\n"
"v_mfma_f32_16x16x32_bf16 v[168:171], a[76:79], v[56:59], v[168:171]\n"
"v_max3_u32 v30, v74, v75, v30\n"
"v_max3_u32 v30, v76, v77, v30\n"
"v_mfma_f32_16x16x32_bf16 v[172:175], a[60:63], v[60:63], v[172:175]\n"
"v_max3_u32 v30, v78, v79, v30\n"
"v_cmp_eq_u32 vcc, 0xffbfffbf, v30\n"
"v_mfma_f32_16x16x32_bf16 v[176:179], a[64:67], v[60:63], v[176:179]\n"
"v_mfma_f32_16x16x32_bf16 v[180:183], a[68:71], v[60:63], v[180:183]\n"
"global_load_dwordx4 a[24:27], v20, s[64:65] offset:0 sc1\n"
"v_mfma_f32_16x16x32_bf16 v[184:187], a[72:75], v[60:63], v[184:187]\n"
"v_mfma_f32_16x16x32_bf16 v[188:191], a[76:79], v[60:63], v[188:191]\n"
"s_cbranch_vccnz .Lre_hh4\n"
".Lok_hh4:\n"
"v_mfma_f32_16x16x32_bf16 v[112:115], a[80:83], v[64:67], v[112:115]\n"
"v_mfma_f32_16x16x32_bf16 v[116:119], a[84:87], v[64:67], v[116:119]\n"
"global_load_dwordx4 a[28:31], v21, s[64:65] offset:0 sc1\n"
"v_mfma_f32_16x16x32_bf16 v[120:123], a[88:91], v[64:67], v[120:123]\n"
"v_mfma_f32_16x16x32_bf16 v[124:127], a[92:95], v[64:67], v[124:127]\n"
"v_mfma_f32_16x16x32_bf16 v[128:131], a[96:99], v[64:67], v[128:131]\n"
"v_mfma_f32_16x16x32_bf16 v[132:135], a[80:83], v[68:71], v[132:135]\n"
"global_load_dwordx4 a[32:35], v22, s[64:65] offset:0 sc1\n"
"v_mfma_f32_16x16x32_bf16 v[136:139], a[84:87], v[68:71], v[136:139]\n"
"v_mfma_f32_16x16x32_bf16 v[140:143], a[88:91], v[68:71], v[140:143]\n"
"v_mfma_f32_16x16x32_bf16 v[144:147], a[92:95], v[68:71], v[144:147]\n"
"v_mfma_f32_16x16x32_bf16 v[148:151], a[96:99], v[68:71], v[148:151]\n"
"global_load_dwordx4 a[36:39], v23, s[64:65] offset:0 sc1\n"
"v_mfma_f32_16x16x32_bf16 v[152:155], a[80:83], v[72:75], v[152:155]\n"
"s_add_u32 s64, s81, s70\n"
"s_waitcnt vmcnt(24)\n"
"v_max_u32 v30, v80, v81\n"
"v_mfma_f32_16x16x32_bf16 v[156:159], a[84:87], v[72:75], v[156:159]\n"
"v_max3_u32 v30, v82, v83, v30\n"
"v_max3_u32 v30, v84, v85, v30\n"
"v_mfma_f32_16x16x32_bf16 v[160:163], a[88:91], v[72:75], v[160:163]\n"
"v_max3_u32 v30, v86, v87, v30\n"
"v_max3_u32 v30, v88, v89, v30\n"
"v_mfma_f32_16x16x32_bf16 v[164:167], a[92:95], v[72:75], v[164:167]\n"
"global_load_dwordx4 v[240:243], v0, s[64:65] offset:0 sc1\n"
"v_mfma_f32_16x16x32_bf16 v[168:171], a[96:99], v[72:75], v[168:171]\n"
"v_max3_u32 v30, v90, v91, v30\n"
"v_max3_u32 v30, v92, v93, v30\n"
"v_mfma_f32_16x16x32_bf16 v[172:175], a[80:83], v[76:79], v[172:175]\n"
"v_max3_u32 v30, v94, v95, v30\n"
"v_cmp_eq_u32 vcc, 0xffbfffbf, v30\n"
"v_mfma_f32_16x16x32_bf16 v[176:179], a[84:87], v[76:79], v[176:179]\n"
"v_mfma_f32_16x16x32_bf16 v[180:183], a[88:91], v[76:79], v[180:183]\n"
"global_load_dwordx4 v[244:247], v20, s[64:65] offset:0 sc1\n"
"v_mfma_f32_16x16x32_bf16 v[184:187], a[92:95], v[76:79], v[184:187]\n"
"v_mfma_f32_16x16x32_bf16 v[188:191], a[96:99], v[76:79], v[188:191]\n"
"s_cbranch_vccnz .Lre_hh5\n"
".Lok_hh5:\n"
"v_mfma_f32_16x16x32_bf16 v[112:115], a[100:103], v[80:83], v[112:115]\n"
"v_mfma_f32_16x16x32_bf16 v[116:119], a[104:107], v[80:83], v[116:119]\n"
"global_load_dwordx4 v[248:251], v21, s[64:65] offset:0 sc1\n"
"v_mfma_f32_16x16x32_bf16 v[120:123], a[108:111], v[80:83], v[120:123]\n"
"v_mfma_f32_16x16x32_bf16 v[124:127], a[112:115], v[80:83], v[124:127]\n"
"v_mfma_f32_16x16x32_bf16 v[128:131], a[116:119], v[80:83], v[128:131]\n"
"v_mfma_f32_16x16x32_bf16 v[132:135], a[100:103], v[84:87], v[132:135]\n"
"global_load_dwordx4 v[252:255], v22, s[64:65] offset:0 sc1\n"
"v_mfma_f32_16x16x32_bf16 v[136:139], a[104:107], v[84:87], v[136:139]\n"
"s_add_u32 s64, s82, s70\n"
"v_mfma_f32_16x16x32_bf16 v[140:143], a[108:111], v[84:87], v[140:143]\n"
"v_mfma_f32_16x16x32_bf16 v[144:147], a[112:115], v[84:87], v[144:147]\n"
"v_mfma_f32_16x16x32_bf16 v[148:151], a[116:119], v[84:87], v[148:151]\n"
"buffer_load_dwordx4 v[192:195], v0, s[20:23], s64 offen offset:0 sc1\n"
"v_mfma_f32_16x16x32_bf16 v[152:155], a[100:103], v[88:91], v[152:155]\n"
"s_waitcnt vmcnt(24)\n"
"v_max_u32 v30, v96, v97\n"
"v_max3_u32 v30, v98, v99, v30\n"
"v_mfma_f32_16x16x32_bf16 v[156:159], a[104:107], v[88:91], v[156:159]\n"
"v_max3_u32 v30, v100, v101, v30\n"
"v_max3_u32 v30, v102, v103, v30\n"
"v_mfma_f32_16x16x32_bf16 v[160:163], a[108:111], v[88:91], v[160:163]\n"
"v_max3_u32 v30, v104, v105, v30\n"
"v_max3_u32 v30, v106, v107, v30\n"
"v_mfma_f32_16x16x32_bf16 v[164:167], a[112:115], v[88:91], v[164:167]\n"
"buffer_load_dwordx4 v[196:199], v20, s[20:23], s64 offen offset:0 sc1\n"
"v_mfma_f32_16x16x32_bf16 v[168:171], a[116:119], v[88:91], v[168:171]\n"
"v_max3_u32 v30, v108, v109, v30\n"
"v_max3_u32 v30, v110, v111, v30\n"
"v_mfma_f32_16x16x32_bf16 v[172:175], a[100:103], v[92:95], v[172:175]\n"
"v_cmp_eq_u32 vcc, 0xffbfffbf, v30\n"
"v_mfma_f32_16x16x32_bf16 v[176:179], a[104:107], v[92:95], v[176:179]\n"
"v_mfma_f32_16x16x32_bf16 v[180:183], a[108:111], v[92:95], v[180:183]\n"
"buffer_load_dwordx4 v[200:203], v21, s[20:23], s64 offen offset:0 sc1\n"
"v_mfma_f32_16x16x32_bf16 v[184:187], a[112:115], v[92:95], v[184:187]\n"
"v_mfma_f32_16x16x32_bf16 v[188:191], a[116:119], v[92:95], v[188:191]\n"
"s_cbranch_vccnz .Lre_hh6\n"
".Lok_hh6:\n"
"v_mfma_f32_16x16x32_bf16 v[112:115], a[120:123], v[96:99], v[112:115]\n"
"v_mfma_f32_16x16x32_bf16 v[116:119], a[124:127], v[96:99], v[116:119]\n"
"buffer_load_dwordx4 v[204:207], v22, s[20:23], s64 offen offset:0 sc1\n"
"v_mfma_f32_16x16x32_bf16 v[120:123], a[128:131], v[96:99], v[120:123]\n"
"s_add_u32 s64, s85, s71\n"
"v_mfma_f32_16x16x32_bf16 v[124:127], a[132:135], v[96:99], v[124:127]\n"
"v_mfma_f32_16x16x32_bf16 v[128:131], a[136:139], v[96:99], v[128:131]\n"
"v_mfma_f32_16x16x32_bf16 v[132:135], a[120:123], v[100:103], v[132:135]\n"
"global_load_dwordx4 a[40:43], v0, s[64:65] offset:0 sc1\n"
"v_mfma_f32_16x16x32_bf16 v[136:139], a[124:127], v[100:103], v[136:139]\n"
"v_mfma_f32_16x16x32_bf16 v[140:143], a[128:131], v[100:103], v[140:143]\n"
"v_mfma_f32_16x16x32_bf16 v[144:147], a[132:135], v[100:103], v[144:147]\n"
"v_mfma_f32_16x16x32_bf16 v[148:151], a[136:139], v[100:103], v[148:151]\n"
"global_load_dwordx4 a[44:47], v20, s[64:65] offset:0 sc1\n"
"v_mfma_f32_16x16x32_bf16 v[152:155], a[120:123], v[104:107], v[152:155]\n"
"s_waitcnt vmcnt(20)\n"
"v_max_u32 v30, v208, v209\n"
"v_max3_u32 v30, v210, v211, v30\n"
"v_mfma_f32_16x16x32_bf16 v[156:159], a[124:127], v[104:107], v[156:159]\n"
"v_max3_u32 v30, v212, v213, v30\n"
"v_max3_u32 v30, v214, v215, v30\n"
"v_mfma_f32_16x16x32_bf16 v[160:163], a[128:131], v[104:107], v[160:163]\n"
"v_max3_u32 v30, v216, v217, v30\n"
"v_max3_u32 v30, v218, v219, v30\n"
"v_mfma_f32_16x16x32_bf16 v[164:167], a[132:135], v[104:107], v[164:167]\n"
"global_load_dwordx4 a[48:51], v21, s[64:65] offset:0 sc1\n"
"v_mfma_f32_16x16x32_bf16 v[168:171], a[136:139], v[104:107], v[168:171]\n"
"v_max3_u32 v30, v220, v221, v30\n"
"v_max3_u32 v30, v222, v223, v30\n"
"v_mfma_f32_16x16x32_bf16 v[172:175], a[120:123], v[108:111], v[172:175]\n"
"v_cmp_eq_u32 vcc, 0xffbfffbf, v30\n"
"v_mfma_f32_16x16x32_bf16 v[176:179], a[124:127], v[108:111], v[176:179]\n"
"v_mfma_f32_16x16x32_bf16 v[180:183], a[128:131], v[108:111], v[180:183]\n"
"global_load_dwordx4 a[52:55], v22, s[64:65] offset:0 sc1\n"
"v_mfma_f32_16x16x32_bf16 v[184:187], a[132:135], v[108:111], v[184:187]\n"
"v_mfma_f32_16x16x32_bf16 v[188:191], a[136:139], v[108:111], v[188:191]\n"
"s_cbranch_vccnz .Lre_hh7\n"
".Lok_hh7:\n"
"v_mfma_f32_16x16x32_bf16 v[112:115], a[140:143], v[208:211], v[112:115]\n"
"v_mfma_f32_16x16x32_bf16 v[116:119], a[144:147], v[208:211], v[116:119]\n"
"global_load_dwordx4 a[56:59], v23, s[64:65] offset:0 sc1\n"
"v_mfma_f32_16x16x32_bf16 v[120:123], a[148:151], v[208:211], v[120:123]\n"
"s_add_u32 s64, s83, s70\n"
"v_mfma_f32_16x16x32_bf16 v[124:127], a[152:155], v[208:211], v[124:127]\n"
"v_mfma_f32_16x16x32_bf16 v[128:131], a[156:159], v[208:211], v[128:131]\n"
"v_mfma_f32_16x16x32_bf16 v[132:135], a[140:143], v[212:215], v[132:135]\n"
"global_load_dwordx4 v[32:35], v0, s[64:65] offset:0 sc1\n"
"v_mfma_f32_16x16x32_bf16 v[136:139], a[144:147], v[212:215], v[136:139]\n"
"v_mfma_f32_16x16x32_bf16 v[140:143], a[148:151], v[212:215], v[140:143]\n"
"v_mfma_f32_16x16x32_bf16 v[144:147], a[152:155], v[212:215], v[144:147]\n"
"v_mfma_f32_16x16x32_bf16 v[148:151], a[156:159], v[212:215], v[148:151]\n"
"global_load_dwordx4 v[36:39], v20, s[64:65] offset:0 sc1\n"
"v_mfma_f32_16x16x32_bf16 v[152:155], a[140:143], v[216:219], v[152:155]\n"
"v_max_u32 v30, v224, v225\n"
"v_max3_u32 v30, v226, v227, v30\n"
"v_mfma_f32_16x16x32_bf16 v[156:159], a[144:147], v[216:219], v[156:159]\n"
"v_max3_u32 v30, v228, v229, v30\n"
"v_max3_u32 v30, v230, v231, v30\n"
"v_mfma_f32_16x16x32_bf16 v[160:163], a[148:151], v[216:219], v[160:163]\n"
"v_max3_u32 v30, v232, v233, v30\n"
"v_max3_u32 v30, v234, v235, v30\n"
"v_mfma_f32_16x16x32_bf16 v[164:167], a[152:155], v[216:219], v[164:167]\n"
"global_load_dwordx4 v[40:43], v21, s[64:65] offset:0 sc1\n"
"v_mfma_f32_16x16x32_bf16 v[168:171], a[156:159], v[216:219], v[168:171]\n"
"v_max3_u32 v30, v236, v237, v30\n"
"v_max3_u32 v30, v238, v239, v30\n"
"v_mfma_f32_16x16x32_bf16 v[172:175], a[140:143], v[220:223], v[172:175]\n"
"v_cmp_eq_u32 vcc, 0xffbfffbf, v30\n"
"v_mfma_f32_16x16x32_bf16 v[176:179], a[144:147], v[220:223], v[176:179]\n"
"v_mfma_f32_16x16x32_bf16 v[180:183], a[148:151], v[220:223], v[180:183]\n"
"global_load_dwordx4 v[44:47], v22, s[64:65] offset:0 sc1\n"
"v_mfma_f32_16x16x32_bf16 v[184:187], a[152:155], v[220:223], v[184:187]\n"
"s_add_u32 s64, s84, s70\n"
"v_mfma_f32_16x16x32_bf16 v[188:191], a[156:159], v[220:223], v[188:191]\n"
"s_cbranch_vccnz .Lre_hh8\n"
".Lok_hh8:\n"
"v_mfma_f32_16x16x32_bf16 v[112:115], a[160:163], v[224:227], v[112:115]\n"
"v_mfma_f32_16x16x32_bf16 v[116:119], a[164:167], v[224:227], v[116:119]\n"
"global_load_dwordx4 v[48:51], v0, s[64:65] offset:0 sc1\n"
"v_mfma_f32_16x16x32_bf16 v[120:123], a[168:171], v[224:227], v[120:123]\n"
"v_mfma_f32_16x16x32_bf16 v[124:127], a[172:175], v[224:227], v[124:127]\n"
"v_mfma_f32_16x16x32_bf16 v[128:131], a[176:179], v[224:227], v[128:131]\n"
"v_mfma_f32_16x16x32_bf16 v[132:135], a[160:163], v[228:231], v[132:135]\n"
"global_load_dwordx4 v[52:55], v20, s[64:65] offset:0 sc1\n"
"v_mfma_f32_16x16x32_bf16 v[136:139], a[164:167], v[228:231], v[136:139]\n"
"v_mfma_f32_16x16x32_bf16 v[140:143], a[168:171], v[228:231], v[140:143]\n"
"v_mfma_f32_16x16x32_bf16 v[144:147], a[172:175], v[228:231], v[144:147]\n"
"v_mfma_f32_16x16x32_bf16 v[148:151], a[176:179], v[228:231], v[148:151]\n"
"global_load_dwordx4 v[56:59], v21, s[64:65] offset:0 sc1\n"
"v_mfma_f32_16x16x32_bf16 v[152:155], a[160:163], v[232:235], v[152:155]\n"
"s_waitcnt vmcnt(16)\n"
"v_max_u32 v30, v240, v241\n"
"v_max3_u32 v30, v242, v243, v30\n"
"v_mfma_f32_16x16x32_bf16 v[156:159], a[164:167], v[232:235], v[156:159]\n"
"v_max3_u32 v30, v244, v245, v30\n"
"v_max3_u32 v30, v246, v247, v30\n"
"v_mfma_f32_16x16x32_bf16 v[160:163], a[168:171], v[232:235], v[160:163]\n"
"v_max3_u32 v30, v248, v249, v30\n"
"v_max3_u32 v30, v250, v251, v30\n"
"v_mfma_f32_16x16x32_bf16 v[164:167], a[172:175], v[232:235], v[164:167]\n"
"global_load_dwordx4 v[60:63], v22, s[64:65] offset:0 sc1\n"
"v_mfma_f32_16x16x32_bf16 v[168:171], a[176:179], v[232:235], v[168:171]\n"
"s_add_u32 s64, s85, s70\n"
"v_max3_u32 v30, v252, v253, v30\n"
"v_max3_u32 v30, v254, v255, v30\n"
"v_mfma_f32_16x16x32_bf16 v[172:175], a[160:163], v[236:239], v[172:175]\n"
"v_cmp_eq_u32 vcc, 0xffbfffbf, v30\n"
"v_mfma_f32_16x16x32_bf16 v[176:179], a[164:167], v[236:239], v[176:179]\n"
"v_mfma_f32_16x16x32_bf16 v[180:183], a[168:171], v[236:239], v[180:183]\n"
"global_load_dwordx4 v[64:67], v0, s[64:65] offset:0 sc1\n"
"v_mfma_f32_16x16x32_bf16 v[184:187], a[172:175], v[236:239], v[184:187]\n"
"v_mfma_f32_16x16x32_bf16 v[188:191], a[176:179], v[236:239], v[188:191]\n"
"s_cbranch_vccnz .Lre_hh9\n"
".Lok_hh9:\n"
"v_mfma_f32_16x16x32_bf16 v[112:115], a[180:183], v[240:243], v[112:115]\n"
"s_waitcnt vmcnt(14)\n"
"v_max_u32 v30, v192, v193\n"
"v_max3_u32 v30, v194, v195, v30\n"
"v_mfma_f32_16x16x32_bf16 v[116:119], a[184:187], v[240:243], v[116:119]\n"
"global_load_dwordx4 v[68:71], v20, s[64:65] offset:0 sc1\n"
"v_mfma_f32_16x16x32_bf16 v[120:123], a[188:191], v[240:243], v[120:123]\n"
"v_max3_u32 v30, v196, v197, v30\n"
"v_max3_u32 v30, v198, v199, v30\n"
"v_mfma_f32_16x16x32_bf16 v[124:127], a[192:195], v[240:243], v[124:127]\n"
"v_max3_u32 v30, v200, v201, v30\n"
"v_max3_u32 v30, v202, v203, v30\n"
"v_mfma_f32_16x16x32_bf16 v[128:131], a[196:199], v[240:243], v[128:131]\n"
"v_max3_u32 v30, v204, v205, v30\n"
"v_max3_u32 v30, v206, v207, v30\n"
"v_mfma_f32_16x16x32_bf16 v[132:135], a[180:183], v[244:247], v[132:135]\n"
"global_load_dwordx4 v[72:75], v21, s[64:65] offset:0 sc1\n"
"v_mfma_f32_16x16x32_bf16 v[136:139], a[184:187], v[244:247], v[136:139]\n"
"v_cmp_eq_u32 vcc, 0xffbfffbf, v30\n"
"v_mfma_f32_16x16x32_bf16 v[140:143], a[188:191], v[244:247], v[140:143]\n"
"v_mfma_f32_16x16x32_bf16 v[144:147], a[192:195], v[244:247], v[144:147]\n"
"v_mfma_f32_16x16x32_bf16 v[148:151], a[196:199], v[244:247], v[148:151]\n"
"global_load_dwordx4 v[76:79], v22, s[64:65] offset:0 sc1\n"
"s_cbranch_vccnz .Lre_hh10\n"
".Lok_hh10:\n"
"v_mfma_f32_16x16x32_bf16 v[112:115], a[200:203], v[192:195], v[112:115]\n"
"s_waitcnt vmcnt(8)\n"
"v_max_u32 v30, v32, v33\n"
"v_max3_u32 v30, v34, v35, v30\n"
"v_mfma_f32_16x16x32_bf16 v[116:119], a[204:207], v[192:195], v[116:119]\n"
"v_max3_u32 v30, v36, v37, v30\n"
"v_max3_u32 v30, v38, v39, v30\n"
"v_mfma_f32_16x16x32_bf16 v[120:123], a[208:211], v[192:195], v[120:123]\n"
"v_max3_u32 v30, v40, v41, v30\n"
"v_max3_u32 v30, v42, v43, v30\n"
"v_mfma_f32_16x16x32_bf16 v[124:127], a[212:215], v[192:195], v[124:127]\n"
"v_max3_u32 v30, v44, v45, v30\n"
"v_max3_u32 v30, v46, v47, v30\n"
"v_mfma_f32_16x16x32_bf16 v[128:131], a[216:219], v[192:195], v[128:131]\n"
"v_cmp_eq_u32 vcc, 0xffbfffbf, v30\n"
"v_mfma_f32_16x16x32_bf16 v[132:135], a[200:203], v[196:199], v[132:135]\n"
"v_mfma_f32_16x16x32_bf16 v[136:139], a[204:207], v[196:199], v[136:139]\n"
"v_mfma_f32_16x16x32_bf16 v[140:143], a[208:211], v[196:199], v[140:143]\n"
"v_mfma_f32_16x16x32_bf16 v[144:147], a[212:215], v[196:199], v[144:147]\n"
"v_mfma_f32_16x16x32_bf16 v[148:151], a[216:219], v[196:199], v[148:151]\n"
"s_cbranch_vccnz .Lre_hh11\n"
".Lok_hh11:\n"
"v_mfma_f32_16x16x32_bf16 v[112:115], a[0:3], v[32:35], v[112:115]\n"
"s_waitcnt vmcnt(4)\n"
"v_max_u32 v30, v48, v49\n"
"v_max3_u32 v30, v50, v51, v30\n"
"v_mfma_f32_16x16x32_bf16 v[116:119], a[4:7], v[32:35], v[116:119]\n"
"v_max3_u32 v30, v52, v53, v30\n"
"v_max3_u32 v30, v54, v55, v30\n"
"v_mfma_f32_16x16x32_bf16 v[120:123], a[8:11], v[32:35], v[120:123]\n"
"v_max3_u32 v30, v56, v57, v30\n"
"v_max3_u32 v30, v58, v59, v30\n"
"v_mfma_f32_16x16x32_bf16 v[124:127], a[12:15], v[32:35], v[124:127]\n"
"v_max3_u32 v30, v60, v61, v30\n"
"v_max3_u32 v30, v62, v63, v30\n"
"v_mfma_f32_16x16x32_bf16 v[128:131], a[16:19], v[32:35], v[128:131]\n"
"v_cmp_eq_u32 vcc, 0xffbfffbf, v30\n"
"v_mfma_f32_16x16x32_bf16 v[132:135], a[0:3], v[36:39], v[132:135]\n"
"v_mfma_f32_16x16x32_bf16 v[136:139], a[4:7], v[36:39], v[136:139]\n"
"v_mfma_f32_16x16x32_bf16 v[140:143], a[8:11], v[36:39], v[140:143]\n"
"v_mfma_f32_16x16x32_bf16 v[144:147], a[12:15], v[36:39], v[144:147]\n"
"v_mfma_f32_16x16x32_bf16 v[148:151], a[16:19], v[36:39], v[148:151]\n"
"s_cbranch_vccnz .Lre_hh12\n"
".Lok_hh12:\n"
"v_mfma_f32_16x16x32_bf16 v[112:115], a[20:23], v[48:51], v[112:115]\n"
"s_waitcnt vmcnt(0)\n"
"v_max_u32 v30, v64, v65\n"
"v_max3_u32 v30, v66, v67, v30\n"
"v_mfma_f32_16x16x32_bf16 v[116:119], a[24:27], v[48:51], v[116:119]\n"
"v_max3_u32 v30, v68, v69, v30\n"
"v_max3_u32 v30, v70, v71, v30\n"
"v_mfma_f32_16x16x32_bf16 v[120:123], a[28:31], v[48:51], v[120:123]\n"
"v_max3_u32 v30, v72, v73, v30\n"
"v_max3_u32 v30, v74, v75, v30\n"
"v_mfma_f32_16x16x32_bf16 v[124:127], a[32:35], v[48:51], v[124:127]\n"
"v_max3_u32 v30, v76, v77, v30\n"
"v_max3_u32 v30, v78, v79, v30\n"
"v_mfma_f32_16x16x32_bf16 v[128:131], a[36:39], v[48:51], v[128:131]\n"
"v_cmp_eq_u32 vcc, 0xffbfffbf, v30\n"
"s_nop 1\n"
"s_cbranch_vccnz .Lre_hh13\n"
".Lok_hh13:\n"
"v_mfma_f32_16x16x32_bf16 v[112:115], a[40:43], v[64:67], v[112:115]\n"
"v_mfma_f32_16x16x32_bf16 v[116:119], a[44:47], v[64:67], v[116:119]\n"
"v_mfma_f32_16x16x32_bf16 v[120:123], a[48:51], v[64:67], v[120:123]\n"
".Lhop11:\n"
"s_cbranch_execz .Lhop_ret\n"
"v_mfma_f32_16x16x32_bf16 v[124:127], a[52:55], v[64:67], v[124:127]\n"
"v_mfma_f32_16x16x32_bf16 v[128:131], a[56:59], v[64:67], v[128:131]\n"
"v_mfma_f32_16x16x32_bf16 v[132:135], a[20:23], v[52:55], v[132:135]\n"
"v_mfma_f32_16x16x32_bf16 v[136:139], a[24:27], v[52:55], v[136:139]\n"
"s_nop 1\n"
"ds_write_b128 v25, v[112:115] offset:0\n"
"v_mfma_f32_16x16x32_bf16 v[140:143], a[28:31], v[52:55], v[140:143]\n"
"v_mfma_f32_16x16x32_bf16 v[144:147], a[32:35], v[52:55], v[144:147]\n"
"ds_write_b128 v25, v[116:119] offset:1024\n"
"v_mfma_f32_16x16x32_bf16 v[148:151], a[36:39], v[52:55], v[148:151]\n"
"v_mfma_f32_16x16x32_bf16 v[132:135], a[40:43], v[68:71], v[132:135]\n"
"ds_write_b128 v25, v[120:123] offset:2048\n"
"v_mfma_f32_16x16x32_bf16 v[136:139], a[44:47], v[68:71], v[136:139]\n"
"v_mfma_f32_16x16x32_bf16 v[140:143], a[48:51], v[68:71], v[140:143]\n"
"ds_write_b128 v25, v[124:127] offset:3072\n"
"v_mfma_f32_16x16x32_bf16 v[144:147], a[52:55], v[68:71], v[144:147]\n"
"v_mfma_f32_16x16x32_bf16 v[148:151], a[56:59], v[68:71], v[148:151]\n"
"ds_write_b128 v25, v[128:131] offset:4096\n"
"v_mfma_f32_16x16x32_bf16 v[152:155], a[180:183], v[248:251], v[152:155]\n"
"v_mfma_f32_16x16x32_bf16 v[156:159], a[184:187], v[248:251], v[156:159]\n"
"ds_write_b128 v25, v[132:135] offset:5120\n"
"v_mfma_f32_16x16x32_bf16 v[160:163], a[188:191], v[248:251], v[160:163]\n"
"v_mfma_f32_16x16x32_bf16 v[164:167], a[192:195], v[248:251], v[164:167]\n"
"ds_write_b128 v25, v[136:139] offset:6144\n"
"v_mfma_f32_16x16x32_bf16 v[168:171], a[196:199], v[248:251], v[168:171]\n"
"v_mfma_f32_16x16x32_bf16 v[172:175], a[180:183], v[252:255], v[172:175]\n"
"ds_write_b128 v25, v[140:143] offset:7168\n"
"v_mfma_f32_16x16x32_bf16 v[176:179], a[184:187], v[252:255], v[176:179]\n"
"v_mfma_f32_16x16x32_bf16 v[180:183], a[188:191], v[252:255], v[180:183]\n"
"ds_write_b128 v25, v[144:147] offset:8192\n"
"v_mfma_f32_16x16x32_bf16 v[184:187], a[192:195], v[252:255], v[184:187]\n"
"v_mfma_f32_16x16x32_bf16 v[188:191], a[196:199], v[252:255], v[188:191]\n"
"ds_write_b128 v25, v[148:151] offset:9216\n"
"v_mfma_f32_16x16x32_bf16 v[152:155], a[200:203], v[200:203], v[152:155]\n"
"s_nop 0\n"
"s_nop 0\n"
"v_mfma_f32_16x16x32_bf16 v[156:159], a[204:207], v[200:203], v[156:159]\n"
"s_nop 0\n"
"s_waitcnt lgkmcnt(0)\n"
"v_mfma_f32_16x16x32_bf16 v[160:163], a[208:211], v[200:203], v[160:163]\n"
"s_barrier\n"
"ds_read_b128 v[80:83], v26 offset:0\n"
"v_mfma_f32_16x16x32_bf16 v[164:167], a[212:215], v[200:203], v[164:167]\n"
"ds_read_b128 v[92:95], v26 offset:20480\n"
"ds_read_b128 v[208:211], v27 offset:0\n"
"v_mfma_f32_16x16x32_bf16 v[168:171], a[216:219], v[200:203], v[168:171]\n"
"ds_read_b128 v[220:223], v27 offset:20480\n"
"ds_read_b128 v[84:87], v26 offset:1024\n"
"v_mfma_f32_16x16x32_bf16 v[172:175], a[200:203], v[204:207], v[172:175]\n"
"ds_read_b128 v[96:99], v26 offset:21504\n"
"ds_read_b128 v[212:215], v27 offset:1024\n"
"v_mfma_f32_16x16x32_bf16 v[176:179], a[204:207], v[204:207], v[176:179]\n"
"ds_read_b128 v[224:227], v27 offset:21504\n"
"ds_read_b128 v[88:91], v26 offset:2048\n"
"v_mfma_f32_16x16x32_bf16 v[180:183], a[208:211], v[204:207], v[180:183]\n"
"ds_read_b128 v[100:103], v26 offset:22528\n"
"ds_read_b128 v[216:219], v27 offset:2048\n"
"v_mfma_f32_16x16x32_bf16 v[184:187], a[212:215], v[204:207], v[184:187]\n"
"ds_read_b128 v[228:231], v27 offset:22528\n"
"v_lshlrev_b32 v20, 16, v6\n"
"v_mfma_f32_16x16x32_bf16 v[188:191], a[216:219], v[204:207], v[188:191]\n"
"v_and_b32 v21, 0xffff0000, v6\n"
"v_lshlrev_b32 v22, 16, v7\n"
"v_mfma_f32_16x16x32_bf16 v[152:155], a[0:3], v[40:43], v[152:155]\n"
"v_and_b32 v23, 0xffff0000, v7\n"
"s_waitcnt lgkmcnt(8)\n"
"v_mfma_f32_16x16x32_bf16 v[156:159], a[4:7], v[40:43], v[156:159]\n"
"v_add_f32 v80, v80, v92\n"
"v_add_f32 v81, v81, v93\n"
"v_mfma_f32_16x16x32_bf16 v[160:163], a[8:11], v[40:43], v[160:163]\n"
"v_add_f32 v208, v208, v220\n"
"v_add_f32 v209, v209, v221\n"
"v_mfma_f32_16x16x32_bf16 v[164:167], a[12:15], v[40:43], v[164:167]\n"
"v_add_f32 v82, v82, v94\n"
"v_add_f32 v83, v83, v95\n"
"v_mfma_f32_16x16x32_bf16 v[168:171], a[16:19], v[40:43], v[168:171]\n"
"v_add_f32 v210, v210, v222\n"
"v_add_f32 v211, v211, v223\n"
"v_mfma_f32_16x16x32_bf16 v[172:175], a[0:3], v[44:47], v[172:175]\n"
"v_add_f32 v80, v80, v20\n"
"v_add_f32 v81, v81, v21\n"
"v_mfma_f32_16x16x32_bf16 v[176:179], a[4:7], v[44:47], v[176:179]\n"
"v_add_f32 v82, v82, v22\n"
"v_add_f32 v83, v83, v23\n"
"v_mfma_f32_16x16x32_bf16 v[180:183], a[8:11], v[44:47], v[180:183]\n"
"v_add_f32 v80, v80, v208\n"
"v_add_f32 v81, v81, v209\n"
"v_mfma_f32_16x16x32_bf16 v[184:187], a[12:15], v[44:47], v[184:187]\n"
"v_add_f32 v82, v82, v210\n"
"v_add_f32 v83, v83, v211\n"
"v_mfma_f32_16x16x32_bf16 v[188:191], a[16:19], v[44:47], v[188:191]\n"
"v_cvt_pk_bf16_f32 v80, v80, v81\n"
"v_cvt_pk_bf16_f32 v81, v82, v83\n"
"v_mfma_f32_16x16x32_bf16 v[152:155], a[20:23], v[56:59], v[152:155]\n"
"global_store_dwordx2 v24, v[80:81], s[52:53] offset:0\n"
"v_mfma_f32_16x16x32_bf16 v[152:155], a[40:43], v[72:75], v[152:155]\n"
"v_lshlrev_b32 v20, 16, v8\n"
"v_and_b32 v21, 0xffff0000, v8\n"
"v_mfma_f32_16x16x32_bf16 v[156:159], a[24:27], v[56:59], v[156:159]\n"
"v_lshlrev_b32 v22, 16, v9\n"
"v_and_b32 v23, 0xffff0000, v9\n"
"v_mfma_f32_16x16x32_bf16 v[156:159], a[44:47], v[72:75], v[156:159]\n"
"s_waitcnt lgkmcnt(4)\n"
"v_add_f32 v84, v84, v96\n"
"v_mfma_f32_16x16x32_bf16 v[160:163], a[28:31], v[56:59], v[160:163]\n"
"v_add_f32 v85, v85, v97\n"
"v_add_f32 v212, v212, v224\n"
"v_mfma_f32_16x16x32_bf16 v[160:163], a[48:51], v[72:75], v[160:163]\n"
"ds_write_b128 v25, v[152:155] offset:10240\n"
"v_mfma_f32_16x16x32_bf16 v[164:167], a[32:35], v[56:59], v[164:167]\n"
"v_add_f32 v213, v213, v225\n"
"v_add_f32 v86, v86, v98\n"
"v_mfma_f32_16x16x32_bf16 v[164:167], a[52:55], v[72:75], v[164:167]\n"
"ds_write_b128 v25, v[156:159] offset:11264\n"
"v_mfma_f32_16x16x32_bf16 v[168:171], a[36:39], v[56:59], v[168:171]\n"
"v_add_f32 v87, v87, v99\n"
"v_add_f32 v214, v214, v226\n"
"v_mfma_f32_16x16x32_bf16 v[168:171], a[56:59], v[72:75], v[168:171]\n"
"ds_write_b128 v25, v[160:163] offset:12288\n"
"v_mfma_f32_16x16x32_bf16 v[172:175], a[20:23], v[60:63], v[172:175]\n"
"v_add_f32 v215, v215, v227\n"
"v_add_f32 v84, v84, v20\n"
"v_mfma_f32_16x16x32_bf16 v[172:175], a[40:43], v[76:79], v[172:175]\n"
"ds_write_b128 v25, v[164:167] offset:13312\n"
"v_mfma_f32_16x16x32_bf16 v[176:179], a[24:27], v[60:63], v[176:179]\n"
"v_add_f32 v85, v85, v21\n"
"v_add_f32 v86, v86, v22\n"
"v_mfma_f32_16x16x32_bf16 v[176:179], a[44:47], v[76:79], v[176:179]\n"
"ds_write_b128 v25, v[168:171] offset:14336\n"
"v_mfma_f32_16x16x32_bf16 v[180:183], a[28:31], v[60:63], v[180:183]\n"
"v_add_f32 v87, v87, v23\n"
"v_add_f32 v84, v84, v212\n"
"v_mfma_f32_16x16x32_bf16 v[180:183], a[48:51], v[76:79], v[180:183]\n"
"ds_write_b128 v25, v[172:175] offset:15360\n"
"v_mfma_f32_16x16x32_bf16 v[184:187], a[32:35], v[60:63], v[184:187]\n"
"v_add_f32 v85, v85, v213\n"
"v_add_f32 v86, v86, v214\n"
"v_mfma_f32_16x16x32_bf16 v[184:187], a[52:55], v[76:79], v[184:187]\n"
"ds_write_b128 v25, v[176:179] offset:16384\n"
"v_mfma_f32_16x16x32_bf16 v[188:191], a[36:39], v[60:63], v[188:191]\n"
"v_add_f32 v87, v87, v215\n"
"v_cvt_pk_bf16_f32 v84, v84, v85\n"
"v_mfma_f32_16x16x32_bf16 v[188:191], a[56:59], v[76:79], v[188:191]\n"
"ds_write_b128 v25, v[180:183] offset:17408\n"
"s_nop 1\n"
"ds_write_b128 v25, v[184:187] offset:18432\n"
"s_nop 3\n"
"ds_write_b128 v25, v[188:191] offset:19456\n"
"v_cvt_pk_bf16_f32 v85, v86, v87\n"
"global_store_dwordx2 v24, v[84:85], s[52:53] offset:32\n"
"s_cmp_ge_u32 s56, 2\n"
"s_cbranch_scc1 .Lred_fo0\n"
"v_lshlrev_b32 v20, 16, v10\n"
"v_and_b32 v21, 0xffff0000, v10\n"
"v_lshlrev_b32 v22, 16, v11\n"
"v_and_b32 v23, 0xffff0000, v11\n"
"s_waitcnt lgkmcnt(10)\n"
"v_pk_add_f32 v[88:89], v[88:89], v[100:101]\n"
"v_pk_add_f32 v[216:217], v[216:217], v[228:229]\n"
"v_pk_add_f32 v[90:91], v[90:91], v[102:103]\n"
"v_pk_add_f32 v[218:219], v[218:219], v[230:231]\n"
"v_pk_add_f32 v[88:89], v[88:89], v[20:21]\n"
"v_pk_add_f32 v[90:91], v[90:91], v[22:23]\n"
"v_pk_add_f32 v[88:89], v[88:89], v[216:217]\n"
"v_pk_add_f32 v[90:91], v[90:91], v[218:219]\n"
"v_cvt_pk_bf16_f32 v88, v88, v89\n"
"v_cvt_pk_bf16_f32 v89, v90, v91\n"
"global_store_dwordx2 v24, v[88:89], s[52:53] offset:64\n"
".Lred_fo0:\n"
"s_waitcnt lgkmcnt(0)\n"
"s_barrier\n"
"ds_read_b128 v[112:115], v26 offset:10240\n"
"ds_read_b128 v[124:127], v26 offset:30720\n"
"ds_read_b128 v[136:139], v27 offset:10240\n"
"ds_read_b128 v[32:35], v27 offset:30720\n"
"ds_read_b128 v[116:119], v26 offset:11264\n"
"ds_read_b128 v[128:131], v26 offset:31744\n"
"ds_read_b128 v[140:143], v27 offset:11264\n"
"ds_read_b128 v[36:39], v27 offset:31744\n"
"ds_read_b128 v[120:123], v26 offset:12288\n"
"ds_read_b128 v[132:135], v26 offset:32768\n"
"ds_read_b128 v[144:147], v27 offset:12288\n"
"ds_read_b128 v[40:43], v27 offset:32768\n"
"v_lshlrev_b32 v48, 16, v12\n"
"v_and_b32 v49, 0xffff0000, v12\n"
"v_lshlrev_b32 v50, 16, v13\n"
"v_and_b32 v51, 0xffff0000, v13\n"
"v_lshlrev_b32 v52, 16, v14\n"
"v_and_b32 v53, 0xffff0000, v14\n"
"v_lshlrev_b32 v54, 16, v15\n"
"v_and_b32 v55, 0xffff0000, v15\n"
"v_lshlrev_b32 v56, 16, v16\n"
"v_and_b32 v57, 0xffff0000, v16\n"
"v_lshlrev_b32 v58, 16, v17\n"
"v_and_b32 v59, 0xffff0000, v17\n"
"s_waitcnt lgkmcnt(8)\n"
"v_pk_add_f32 v[112:113], v[112:113], v[124:125]\n"
"v_pk_add_f32 v[136:137], v[136:137], v[32:33]\n"
"v_pk_add_f32 v[114:115], v[114:115], v[126:127]\n"
"v_pk_add_f32 v[138:139], v[138:139], v[34:35]\n"
"v_pk_add_f32 v[112:113], v[112:113], v[48:49]\n"
"v_pk_add_f32 v[114:115], v[114:115], v[50:51]\n"
"v_pk_add_f32 v[112:113], v[112:113], v[136:137]\n"
"v_pk_add_f32 v[114:115], v[114:115], v[138:139]\n"
"v_cvt_pk_bf16_f32 v112, v112, v113\n"
"v_cvt_pk_bf16_f32 v113, v114, v115\n"
"global_store_dwordx2 v5, v[112:113], s[52:53] offset:0\n"
"s_waitcnt lgkmcnt(4)\n"
"v_pk_add_f32 v[116:117], v[116:117], v[128:129]\n"
"v_pk_add_f32 v[140:141], v[140:141], v[36:37]\n"
"v_pk_add_f32 v[118:119], v[118:119], v[130:131]\n"
"v_pk_add_f32 v[142:143], v[142:143], v[38:39]\n"
"v_pk_add_f32 v[116:117], v[116:117], v[52:53]\n"
"v_pk_add_f32 v[118:119], v[118:119], v[54:55]\n"
"v_pk_add_f32 v[116:117], v[116:117], v[140:141]\n"
"v_pk_add_f32 v[118:119], v[118:119], v[142:143]\n"
"v_cvt_pk_bf16_f32 v116, v116, v117\n"
"v_cvt_pk_bf16_f32 v117, v118, v119\n"
"global_store_dwordx2 v5, v[116:117], s[52:53] offset:32\n"
"s_cmp_ge_u32 s56, 2\n"
"s_cbranch_scc1 .Lred_fo1\n"
"s_waitcnt lgkmcnt(0)\n"
"v_pk_add_f32 v[120:121], v[120:121], v[132:133]\n"
"v_pk_add_f32 v[144:145], v[144:145], v[40:41]\n"
"v_pk_add_f32 v[122:123], v[122:123], v[134:135]\n"
"v_pk_add_f32 v[146:147], v[146:147], v[42:43]\n"
"v_pk_add_f32 v[120:121], v[120:121], v[56:57]\n"
"v_pk_add_f32 v[122:123], v[122:123], v[58:59]\n"
"v_pk_add_f32 v[120:121], v[120:121], v[144:145]\n"
"v_pk_add_f32 v[122:123], v[122:123], v[146:147]\n"
"v_cvt_pk_bf16_f32 v120, v120, v121\n"
"v_cvt_pk_bf16_f32 v121, v122, v123\n"
"global_store_dwordx2 v5, v[120:121], s[52:53] offset:64\n"
".Lred_fo1:\n"
".Lmk_exit:\n"
"s_endpgm\n"
".Lkf_re:\n"
"s_add_u32 s97, s97, 1\n"
"s_cmp_gt_u32 s97, 1048576\n"
"s_cbranch_scc1 .Lkf_ok\n"
"s_mov_b32 exec_hi, 0\n"
"buffer_load_dwordx2 v[226:227], v25, s[20:23], s18 offen sc1\n"
"s_mov_b32 exec_hi, -1\n"
"s_waitcnt vmcnt(0)\n"
"s_branch .Lkf_chk\n"
".Lslow:\n"
"s_mov_b32 s64, s34\n"
"s_add_u32 s64, s64, 8192\n"
"s_bitset0_b32 s64, 16\n"
"global_load_dwordx4 a[84:87], v0, s[64:65] offset:0 sc1\n"
"global_load_dwordx4 a[92:95], v0, s[64:65] offset:2048 sc1\n"
"global_load_dwordx4 a[96:99], v0, s[64:65] offset:3072 sc1\n"
"global_load_dwordx4 a[88:91], v0, s[64:65] offset:1024 sc1\n"
"global_load_dwordx4 a[100:103], v1, s[64:65] offset:0 sc1\n"
"global_load_dwordx4 a[104:107], v1, s[64:65] offset:1024 sc1\n"
"global_load_dwordx4 a[108:111], v1, s[64:65] offset:2048 sc1\n"
"s_add_u32 s64, s64, 8192\n"
"s_bitset0_b32 s64, 16\n"
"global_load_dwordx4 a[112:115], v0, s[64:65] offset:0 sc1\n"
"global_load_dwordx4 a[120:123], v0, s[64:65] offset:2048 sc1\n"
"global_load_dwordx4 a[124:127], v0, s[64:65] offset:3072 sc1\n"
"global_load_dwordx4 a[116:119], v0, s[64:65] offset:1024 sc1\n"
"global_load_dwordx4 a[128:131], v1, s[64:65] offset:0 sc1\n"
"global_load_dwordx4 a[132:135], v1, s[64:65] offset:1024 sc1\n"
"global_load_dwordx4 a[136:139], v1, s[64:65] offset:2048 sc1\n"
"s_add_u32 s64, s64, 8192\n"
"s_bitset0_b32 s64, 16\n"
"global_load_dwordx4 a[140:143], v0, s[64:65] offset:0 sc1\n"
"global_load_dwordx4 a[148:151], v0, s[64:65] offset:2048 sc1\n"
"global_load_dwordx4 a[152:155], v0, s[64:65] offset:3072 sc1\n"
"global_load_dwordx4 a[144:147], v0, s[64:65] offset:1024 sc1\n"
"global_load_dwordx4 a[156:159], v1, s[64:65] offset:0 sc1\n"
"global_load_dwordx4 a[160:163], v1, s[64:65] offset:1024 sc1\n"
"global_load_dwordx4 a[164:167], v1, s[64:65] offset:2048 sc1\n"
"s_add_u32 s64, s64, 8192\n"
"s_bitset0_b32 s64, 16\n"
"global_load_dwordx4 a[168:171], v0, s[64:65] offset:0 sc1\n"
"global_load_dwordx4 a[176:179], v0, s[64:65] offset:2048 sc1\n"
"global_load_dwordx4 a[180:183], v0, s[64:65] offset:3072 sc1\n"
"global_load_dwordx4 a[172:175], v0, s[64:65] offset:1024 sc1\n"
"global_load_dwordx4 a[184:187], v1, s[64:65] offset:0 sc1\n"
"global_load_dwordx4 a[188:191], v1, s[64:65] offset:1024 sc1\n"
"global_load_dwordx4 a[192:195], v1, s[64:65] offset:2048 sc1\n"
"s_add_u32 s64, s64, 8192\n"
"s_bitset0_b32 s64, 16\n"
"global_load_dwordx4 a[196:199], v0, s[64:65] offset:0 sc1\n"
"global_load_dwordx4 a[204:207], v0, s[64:65] offset:2048 sc1\n"
"global_load_dwordx4 a[208:211], v0, s[64:65] offset:3072 sc1\n"
"global_load_dwordx4 a[200:203], v0, s[64:65] offset:1024 sc1\n"
"global_load_dwordx4 a[212:215], v1, s[64:65] offset:0 sc1\n"
"global_load_dwordx4 a[216:219], v1, s[64:65] offset:1024 sc1\n"
"global_load_dwordx4 a[220:223], v1, s[64:65] offset:2048 sc1\n"
"s_waitcnt vmcnt(0)\n"
"s_lshl_b32 s5, s56, 8\n"
"s_add_u32 s5, s5, 147456\n"
"v_lshl_add_u32 v120, v2, 2, s5\n"
"v_mov_b32 v121, 0xff800000\n"
"ds_write_b32 v120, v121\n"
"v_and_b32 v122, 31, v2\n"
"v_lshlrev_b32 v122, 2, v122\n"
"v_add_u32 v122, 147456, v122\n"
"v_mov_b32 v7, v26\n"
"v_mov_b32 v8, v27\n"
"s_mov_b32 s5, 0\n"
"s_waitcnt lgkmcnt(0)\n"
"s_barrier\n"
".Lslow_u:\n"
"ds_read_b128 v[104:107], v7\n"
"ds_read_b64 v[108:109], v8 offset:32\n"
"v_mov_b32 v121, 0xff800000\n"
"s_waitcnt lgkmcnt(0)\n"
"v_mfma_f32_32x32x16_bf16 v[32:47], a[0:3], v[104:107], 0\n"
"v_mfma_f32_32x32x8_bf16 v[32:47], a[8:9], v[108:109], v[32:47]\n"
"s_nop 11\n"
"v_max3_f32 v121, v32, v33, v121\n"
"v_max3_f32 v121, v34, v35, v121\n"
"v_max3_f32 v121, v36, v37, v121\n"
"v_max3_f32 v121, v38, v39, v121\n"
"v_max3_f32 v121, v40, v41, v121\n"
"v_max3_f32 v121, v42, v43, v121\n"
"v_max3_f32 v121, v44, v45, v121\n"
"v_max3_f32 v121, v46, v47, v121\n"
"v_mfma_f32_32x32x16_bf16 v[48:63], a[4:7], v[104:107], 0\n"
"v_mfma_f32_32x32x8_bf16 v[48:63], a[10:11], v[108:109], v[48:63]\n"
"s_nop 11\n"
"v_max3_f32 v121, v48, v49, v121\n"
"v_max3_f32 v121, v50, v51, v121\n"
"v_max3_f32 v121, v52, v53, v121\n"
"v_max3_f32 v121, v54, v55, v121\n"
"v_max3_f32 v121, v56, v57, v121\n"
"v_max3_f32 v121, v58, v59, v121\n"
"v_max3_f32 v121, v60, v61, v121\n"
"v_max3_f32 v121, v62, v63, v121\n"
"v_mfma_f32_32x32x16_bf16 v[32:47], a[28:31], v[104:107], 0\n"
"v_mfma_f32_32x32x8_bf16 v[32:47], a[36:37], v[108:109], v[32:47]\n"
"s_nop 11\n"
"v_max3_f32 v121, v32, v33, v121\n"
"v_max3_f32 v121, v34, v35, v121\n"
"v_max3_f32 v121, v36, v37, v121\n"
"v_max3_f32 v121, v38, v39, v121\n"
"v_max3_f32 v121, v40, v41, v121\n"
"v_max3_f32 v121, v42, v43, v121\n"
"v_max3_f32 v121, v44, v45, v121\n"
"v_max3_f32 v121, v46, v47, v121\n"
"v_mfma_f32_32x32x16_bf16 v[48:63], a[32:35], v[104:107], 0\n"
"v_mfma_f32_32x32x8_bf16 v[48:63], a[38:39], v[108:109], v[48:63]\n"
"s_nop 11\n"
"v_max3_f32 v121, v48, v49, v121\n"
"v_max3_f32 v121, v50, v51, v121\n"
"v_max3_f32 v121, v52, v53, v121\n"
"v_max3_f32 v121, v54, v55, v121\n"
"v_max3_f32 v121, v56, v57, v121\n"
"v_max3_f32 v121, v58, v59, v121\n"
"v_max3_f32 v121, v60, v61, v121\n"
"v_max3_f32 v121, v62, v63, v121\n"
"v_mfma_f32_32x32x16_bf16 v[32:47], a[56:59], v[104:107], 0\n"
"v_mfma_f32_32x32x8_bf16 v[32:47], a[64:65], v[108:109], v[32:47]\n"
"s_nop 11\n"
"v_max3_f32 v121, v32, v33, v121\n"
"v_max3_f32 v121, v34, v35, v121\n"
"v_max3_f32 v121, v36, v37, v121\n"
"v_max3_f32 v121, v38, v39, v121\n"
"v_max3_f32 v121, v40, v41, v121\n"
"v_max3_f32 v121, v42, v43, v121\n"
"v_max3_f32 v121, v44, v45, v121\n"
"v_max3_f32 v121, v46, v47, v121\n"
"v_mfma_f32_32x32x16_bf16 v[48:63], a[60:63], v[104:107], 0\n"
"v_mfma_f32_32x32x8_bf16 v[48:63], a[66:67], v[108:109], v[48:63]\n"
"s_nop 11\n"
"v_max3_f32 v121, v48, v49, v121\n"
"v_max3_f32 v121, v50, v51, v121\n"
"v_max3_f32 v121, v52, v53, v121\n"
"v_max3_f32 v121, v54, v55, v121\n"
"v_max3_f32 v121, v56, v57, v121\n"
"v_max3_f32 v121, v58, v59, v121\n"
"v_max3_f32 v121, v60, v61, v121\n"
"v_max3_f32 v121, v62, v63, v121\n"
"v_mfma_f32_32x32x16_bf16 v[32:47], a[84:87], v[104:107], 0\n"
"v_mfma_f32_32x32x8_bf16 v[32:47], a[92:93], v[108:109], v[32:47]\n"
"s_nop 11\n"
"v_max3_f32 v121, v32, v33, v121\n"
"v_max3_f32 v121, v34, v35, v121\n"
"v_max3_f32 v121, v36, v37, v121\n"
"v_max3_f32 v121, v38, v39, v121\n"
"v_max3_f32 v121, v40, v41, v121\n"
"v_max3_f32 v121, v42, v43, v121\n"
"v_max3_f32 v121, v44, v45, v121\n"
"v_max3_f32 v121, v46, v47, v121\n"
"v_mfma_f32_32x32x16_bf16 v[48:63], a[88:91], v[104:107], 0\n"
"v_mfma_f32_32x32x8_bf16 v[48:63], a[94:95], v[108:109], v[48:63]\n"
"s_nop 11\n"
"v_max3_f32 v121, v48, v49, v121\n"
"v_max3_f32 v121, v50, v51, v121\n"
"v_max3_f32 v121, v52, v53, v121\n"
"v_max3_f32 v121, v54, v55, v121\n"
"v_max3_f32 v121, v56, v57, v121\n"
"v_max3_f32 v121, v58, v59, v121\n"
"v_max3_f32 v121, v60, v61, v121\n"
"v_max3_f32 v121, v62, v63, v121\n"
"v_mfma_f32_32x32x16_bf16 v[32:47], a[112:115], v[104:107], 0\n"
"v_mfma_f32_32x32x8_bf16 v[32:47], a[120:121], v[108:109], v[32:47]\n"
"s_nop 11\n"
"v_max3_f32 v121, v32, v33, v121\n"
"v_max3_f32 v121, v34, v35, v121\n"
"v_max3_f32 v121, v36, v37, v121\n"
"v_max3_f32 v121, v38, v39, v121\n"
"v_max3_f32 v121, v40, v41, v121\n"
"v_max3_f32 v121, v42, v43, v121\n"
"v_max3_f32 v121, v44, v45, v121\n"
"v_max3_f32 v121, v46, v47, v121\n"
"v_mfma_f32_32x32x16_bf16 v[48:63], a[116:119], v[104:107], 0\n"
"v_mfma_f32_32x32x8_bf16 v[48:63], a[122:123], v[108:109], v[48:63]\n"
"s_nop 11\n"
"v_max3_f32 v121, v48, v49, v121\n"
"v_max3_f32 v121, v50, v51, v121\n"
"v_max3_f32 v121, v52, v53, v121\n"
"v_max3_f32 v121, v54, v55, v121\n"
"v_max3_f32 v121, v56, v57, v121\n"
"v_max3_f32 v121, v58, v59, v121\n"
"v_max3_f32 v121, v60, v61, v121\n"
"v_max3_f32 v121, v62, v63, v121\n"
"v_mfma_f32_32x32x16_bf16 v[32:47], a[140:143], v[104:107], 0\n"
"v_mfma_f32_32x32x8_bf16 v[32:47], a[148:149], v[108:109], v[32:47]\n"
"s_nop 11\n"
"v_max3_f32 v121, v32, v33, v121\n"
"v_max3_f32 v121, v34, v35, v121\n"
"v_max3_f32 v121, v36, v37, v121\n"
"v_max3_f32 v121, v38, v39, v121\n"
"v_max3_f32 v121, v40, v41, v121\n"
"v_max3_f32 v121, v42, v43, v121\n"
"v_max3_f32 v121, v44, v45, v121\n"
"v_max3_f32 v121, v46, v47, v121\n"
"v_mfma_f32_32x32x16_bf16 v[48:63], a[144:147], v[104:107], 0\n"
"v_mfma_f32_32x32x8_bf16 v[48:63], a[150:151], v[108:109], v[48:63]\n"
"s_nop 11\n"
"v_max3_f32 v121, v48, v49, v121\n"
"v_max3_f32 v121, v50, v51, v121\n"
"v_max3_f32 v121, v52, v53, v121\n"
"v_max3_f32 v121, v54, v55, v121\n"
"v_max3_f32 v121, v56, v57, v121\n"
"v_max3_f32 v121, v58, v59, v121\n"
"v_max3_f32 v121, v60, v61, v121\n"
"v_max3_f32 v121, v62, v63, v121\n"
"v_mfma_f32_32x32x16_bf16 v[32:47], a[168:171], v[104:107], 0\n"
"v_mfma_f32_32x32x8_bf16 v[32:47], a[176:177], v[108:109], v[32:47]\n"
"s_nop 11\n"
"v_max3_f32 v121, v32, v33, v121\n"
"v_max3_f32 v121, v34, v35, v121\n"
"v_max3_f32 v121, v36, v37, v121\n"
"v_max3_f32 v121, v38, v39, v121\n"
"v_max3_f32 v121, v40, v41, v121\n"
"v_max3_f32 v121, v42, v43, v121\n"
"v_max3_f32 v121, v44, v45, v121\n"
"v_max3_f32 v121, v46, v47, v121\n"
"v_mfma_f32_32x32x16_bf16 v[48:63], a[172:175], v[104:107], 0\n"
"v_mfma_f32_32x32x8_bf16 v[48:63], a[178:179], v[108:109], v[48:63]\n"
"s_nop 11\n"
"v_max3_f32 v121, v48, v49, v121\n"
"v_max3_f32 v121, v50, v51, v121\n"
"v_max3_f32 v121, v52, v53, v121\n"
"v_max3_f32 v121, v54, v55, v121\n"
"v_max3_f32 v121, v56, v57, v121\n"
"v_max3_f32 v121, v58, v59, v121\n"
"v_max3_f32 v121, v60, v61, v121\n"
"v_max3_f32 v121, v62, v63, v121\n"
"v_mfma_f32_32x32x16_bf16 v[32:47], a[196:199], v[104:107], 0\n"
"v_mfma_f32_32x32x8_bf16 v[32:47], a[204:205], v[108:109], v[32:47]\n"
"s_nop 11\n"
"v_max3_f32 v121, v32, v33, v121\n"
"v_max3_f32 v121, v34, v35, v121\n"
"v_max3_f32 v121, v36, v37, v121\n"
"v_max3_f32 v121, v38, v39, v121\n"
"v_max3_f32 v121, v40, v41, v121\n"
"v_max3_f32 v121, v42, v43, v121\n"
"v_max3_f32 v121, v44, v45, v121\n"
"v_max3_f32 v121, v46, v47, v121\n"
"v_mfma_f32_32x32x16_bf16 v[48:63], a[200:203], v[104:107], 0\n"
"v_mfma_f32_32x32x8_bf16 v[48:63], a[206:207], v[108:109], v[48:63]\n"
"s_nop 11\n"
"v_max3_f32 v121, v48, v49, v121\n"
"v_max3_f32 v121, v50, v51, v121\n"
"v_max3_f32 v121, v52, v53, v121\n"
"v_max3_f32 v121, v54, v55, v121\n"
"v_max3_f32 v121, v56, v57, v121\n"
"v_max3_f32 v121, v58, v59, v121\n"
"v_max3_f32 v121, v60, v61, v121\n"
"v_max3_f32 v121, v62, v63, v121\n"
"ds_max_f32 v122, v121\n"
"v_add_u32 v122, 128, v122\n"
"v_add_u32 v7, 1536, v7\n"
"v_add_u32 v8, 1536, v8\n"
"s_add_u32 s5, s5, 1\n"
"s_cmp_lt_u32 s5, 8\n"
"s_cbranch_scc1 .Lslow_u\n"
"s_waitcnt lgkmcnt(0)\n"
"s_barrier\n"
"ds_read_b32 v121, v120\n"
"s_lshl_b32 s5, s56, 6\n"
"v_add_u32 v122, s5, v2\n"
"v_mul_u32_u24 v122, 48, v122\n"
"v_add_u32 v122, 151552, v122\n"
"s_waitcnt lgkmcnt(0)\n"
"v_sub_f32 v121, 0, v121\n"
"v_cvt_pk_bf16_f32 v123, v121, v121\n"
"v_and_b32 v123, 0xffff0000, v123\n"
"v_sub_f32 v123, v121, v123\n"
"v_cvt_pk_bf16_f32 v124, v121, v123\n"
"v_and_b32 v125, 0xffff0000, v124\n"
"v_sub_f32 v123, v123, v125\n"
"v_cvt_pk_bf16_f32 v125, v123, 0\n"
"v_mov_b32 v126, 0\n"
"ds_write_b32 v215, v126\n"
"ds_write_b64 v122, v[124:125] offset:40\n"
"s_waitcnt lgkmcnt(0)\n"
"s_barrier\n"
"s_lshl_b32 s4, s56, 1\n"
"s_add_u32 s4, s4, 0\n"
"s_and_b32 s4, s4, 7\n"
"s_mul_i32 s4, s4, 1536\n"
"v_add_u32 v7, s4, v26\n"
"v_add_u32 v8, s4, v27\n"
"ds_read_b128 v[104:107], v7\n"
"ds_read_b64 v[108:109], v8 offset:32\n"
"s_lshl_b32 s4, s56, 1\n"
"s_add_u32 s4, s4, 1\n"
"s_and_b32 s4, s4, 7\n"
"s_mul_i32 s4, s4, 1536\n"
"v_add_u32 v7, s4, v26\n"
"v_add_u32 v8, s4, v27\n"
"ds_read_b128 v[110:113], v7\n"
"ds_read_b64 v[114:115], v8 offset:32\n"
"s_waitcnt lgkmcnt(0)\n"
"s_branch .Lslow_done\n"
".Lrdy_re:\n"
"s_add_u32 s97, s97, 1\n"
"s_cmp_gt_u32 s97, 1048576\n"
"s_cbranch_scc1 .Lrdy_ok\n"
"buffer_load_dword v5, v25, s[20:23], s50 offen sc1\n"
"s_waitcnt vmcnt(0)\n"
"s_branch .Lrdy_chk\n"
".Lre_eo:\n"
"s_add_u32 s97, s97, 1\n"
"s_cmp_gt_u32 s97, 1048576\n"
"s_cbranch_scc1 .Lok_eo\n"
"s_add_u32 s64, s55, 0\n"
"global_load_dwordx4 v[128:131], v0, s[64:65] offset:0 sc1\n"
"global_load_dwordx4 v[132:135], v0, s[64:65] offset:1024 sc1\n"
"global_load_dwordx4 v[136:139], v0, s[64:65] offset:2048 sc1\n"
"global_load_dwordx4 v[140:143], v0, s[64:65] offset:3072 sc1\n"
"global_load_dwordx4 v[144:147], v1, s[64:65] offset:0 sc1\n"
"s_add_u32 s64, s55, 20480\n"
"global_load_dwordx4 v[148:151], v0, s[64:65] offset:0 sc1\n"
"global_load_dwordx4 v[152:155], v0, s[64:65] offset:1024 sc1\n"
"global_load_dwordx4 v[156:159], v0, s[64:65] offset:2048 sc1\n"
"global_load_dwordx4 v[160:163], v0, s[64:65] offset:3072 sc1\n"
"global_load_dwordx4 v[164:167], v1, s[64:65] offset:0 sc1\n"
"s_waitcnt vmcnt(0)\n"
"v_max_u32 v29, v128, v129\n"
"v_max3_u32 v29, v130, v131, v29\n"
"v_max3_u32 v29, v132, v133, v29\n"
"v_max3_u32 v29, v134, v135, v29\n"
"v_max3_u32 v29, v136, v137, v29\n"
"v_max3_u32 v29, v138, v139, v29\n"
"v_max3_u32 v29, v140, v141, v29\n"
"v_max3_u32 v29, v142, v143, v29\n"
"v_max3_u32 v29, v144, v145, v29\n"
"v_max3_u32 v29, v146, v147, v29\n"
"v_max3_u32 v29, v148, v149, v29\n"
"v_max3_u32 v29, v150, v151, v29\n"
"v_max3_u32 v29, v152, v153, v29\n"
"v_max3_u32 v29, v154, v155, v29\n"
"v_max3_u32 v29, v156, v157, v29\n"
"v_max3_u32 v29, v158, v159, v29\n"
"v_max3_u32 v29, v160, v161, v29\n"
"v_max3_u32 v29, v162, v163, v29\n"
"v_max3_u32 v29, v164, v165, v29\n"
"v_max3_u32 v29, v166, v167, v29\n"
"v_cmp_eq_u32 vcc, 0xffbfffbf, v29\n"
"s_nop 1\n"
"s_cbranch_vccnz .Lre_eo\n"
"v_mfma_f32_16x16x32_bf16 v[72:75], v[188:191], v[128:131], 0\n"
"v_mfma_f32_16x16x32_bf16 v[72:75], v[208:211], v[132:135], v[72:75]\n"
"v_mfma_f32_16x16x32_bf16 v[72:75], v[228:231], v[136:139], v[72:75]\n"
"v_mfma_f32_16x16x32_bf16 v[72:75], v[248:251], v[140:143], v[72:75]\n"
"v_mfma_f32_16x16x32_bf16 v[72:75], a[236:239], v[144:147], v[72:75]\n"
"v_mfma_f32_16x16x32_bf16 v[76:79], v[192:195], v[128:131], 0\n"
"v_mfma_f32_16x16x32_bf16 v[76:79], v[212:215], v[132:135], v[76:79]\n"
"v_mfma_f32_16x16x32_bf16 v[76:79], v[232:235], v[136:139], v[76:79]\n"
"v_mfma_f32_16x16x32_bf16 v[76:79], v[252:255], v[140:143], v[76:79]\n"
"v_mfma_f32_16x16x32_bf16 v[76:79], a[240:243], v[144:147], v[76:79]\n"
"v_mfma_f32_16x16x32_bf16 v[80:83], v[196:199], v[128:131], 0\n"
"v_mfma_f32_16x16x32_bf16 v[80:83], v[216:219], v[132:135], v[80:83]\n"
"v_mfma_f32_16x16x32_bf16 v[80:83], v[236:239], v[136:139], v[80:83]\n"
"v_mfma_f32_16x16x32_bf16 v[80:83], a[224:227], v[140:143], v[80:83]\n"
"v_mfma_f32_16x16x32_bf16 v[80:83], a[244:247], v[144:147], v[80:83]\n"
"v_mfma_f32_16x16x32_bf16 v[84:87], v[200:203], v[128:131], 0\n"
"ds_write_b128 v25, v[72:75] offset:0\n"
"v_mfma_f32_16x16x32_bf16 v[84:87], v[220:223], v[132:135], v[84:87]\n"
"v_mfma_f32_16x16x32_bf16 v[84:87], v[240:243], v[136:139], v[84:87]\n"
"ds_write_b128 v25, v[76:79] offset:1024\n"
"v_mfma_f32_16x16x32_bf16 v[84:87], a[228:231], v[140:143], v[84:87]\n"
"v_mfma_f32_16x16x32_bf16 v[84:87], a[248:251], v[144:147], v[84:87]\n"
"s_nop 0\n"
"ds_write_b128 v25, v[80:83] offset:2048\n"
"v_mfma_f32_16x16x32_bf16 v[88:91], v[204:207], v[128:131], 0\n"
"v_mfma_f32_16x16x32_bf16 v[88:91], v[224:227], v[132:135], v[88:91]\n"
"v_mfma_f32_16x16x32_bf16 v[88:91], v[244:247], v[136:139], v[88:91]\n"
"v_mfma_f32_16x16x32_bf16 v[88:91], a[232:235], v[140:143], v[88:91]\n"
"s_nop 1\n"
"ds_write_b128 v25, v[84:87] offset:3072\n"
"v_mfma_f32_16x16x32_bf16 v[88:91], a[252:255], v[144:147], v[88:91]\n"
"v_mfma_f32_16x16x32_bf16 v[92:95], v[188:191], v[148:151], 0\n"
"v_mfma_f32_16x16x32_bf16 v[92:95], v[208:211], v[152:155], v[92:95]\n"
"v_mfma_f32_16x16x32_bf16 v[92:95], v[228:231], v[156:159], v[92:95]\n"
"v_mfma_f32_16x16x32_bf16 v[92:95], v[248:251], v[160:163], v[92:95]\n"
"s_nop 3\n"
"ds_write_b128 v25, v[88:91] offset:4096\n"
"v_mfma_f32_16x16x32_bf16 v[92:95], a[236:239], v[164:167], v[92:95]\n"
"v_mfma_f32_16x16x32_bf16 v[96:99], v[192:195], v[148:151], 0\n"
"v_mfma_f32_16x16x32_bf16 v[96:99], v[212:215], v[152:155], v[96:99]\n"
"v_mfma_f32_16x16x32_bf16 v[96:99], v[232:235], v[156:159], v[96:99]\n"
"v_mfma_f32_16x16x32_bf16 v[96:99], v[252:255], v[160:163], v[96:99]\n"
"s_nop 3\n"
"ds_write_b128 v25, v[92:95] offset:5120\n"
"v_mfma_f32_16x16x32_bf16 v[96:99], a[240:243], v[164:167], v[96:99]\n"
"v_mfma_f32_16x16x32_bf16 v[100:103], v[196:199], v[148:151], 0\n"
"v_mfma_f32_16x16x32_bf16 v[100:103], v[216:219], v[152:155], v[100:103]\n"
"v_mfma_f32_16x16x32_bf16 v[100:103], v[236:239], v[156:159], v[100:103]\n"
"v_mfma_f32_16x16x32_bf16 v[100:103], a[224:227], v[160:163], v[100:103]\n"
"s_nop 3\n"
"ds_write_b128 v25, v[96:99] offset:6144\n"
"v_mfma_f32_16x16x32_bf16 v[100:103], a[244:247], v[164:167], v[100:103]\n"
"v_mfma_f32_16x16x32_bf16 v[104:107], v[200:203], v[148:151], 0\n"
"v_mfma_f32_16x16x32_bf16 v[104:107], v[220:223], v[152:155], v[104:107]\n"
"v_mfma_f32_16x16x32_bf16 v[104:107], v[240:243], v[156:159], v[104:107]\n"
"v_mfma_f32_16x16x32_bf16 v[104:107], a[228:231], v[160:163], v[104:107]\n"
"s_nop 3\n"
"ds_write_b128 v25, v[100:103] offset:7168\n"
"v_mfma_f32_16x16x32_bf16 v[104:107], a[248:251], v[164:167], v[104:107]\n"
"v_mfma_f32_16x16x32_bf16 v[108:111], v[204:207], v[148:151], 0\n"
"v_mfma_f32_16x16x32_bf16 v[108:111], v[224:227], v[152:155], v[108:111]\n"
"v_mfma_f32_16x16x32_bf16 v[108:111], v[244:247], v[156:159], v[108:111]\n"
"v_mfma_f32_16x16x32_bf16 v[108:111], a[232:235], v[160:163], v[108:111]\n"
"s_nop 3\n"
"ds_write_b128 v25, v[104:107] offset:8192\n"
"v_mfma_f32_16x16x32_bf16 v[108:111], a[252:255], v[164:167], v[108:111]\n"
"s_nop 7\n"
"ds_write_b128 v25, v[108:111] offset:9216\n"
"s_branch .Lok_eo\n"
".Lre_lo:\n"
"s_add_u32 s97, s97, 1\n"
"s_cmp_gt_u32 s97, 1048576\n"
"s_cbranch_scc1 .Lok_lo\n"
"s_add_u32 s34, s55, 40960\n"
"global_load_dwordx4 v[32:35], v0, s[34:35] offset:0 sc1\n"
"global_load_dwordx4 v[36:39], v0, s[34:35] offset:1024 sc1\n"
"global_load_dwordx4 v[40:43], v0, s[34:35] offset:2048 sc1\n"
"global_load_dwordx4 v[44:47], v0, s[34:35] offset:3072 sc1\n"
"global_load_dwordx4 v[48:51], v1, s[34:35] offset:0 sc1\n"
"s_add_u32 s34, s55, 61440\n"
"global_load_dwordx4 v[52:55], v0, s[34:35] offset:0 sc1\n"
"global_load_dwordx4 v[56:59], v0, s[34:35] offset:1024 sc1\n"
"global_load_dwordx4 v[60:63], v0, s[34:35] offset:2048 sc1\n"
"global_load_dwordx4 v[64:67], v0, s[34:35] offset:3072 sc1\n"
"global_load_dwordx4 v[68:71], v1, s[34:35] offset:0 sc1\n"
"s_waitcnt vmcnt(0)\n"
"s_branch .Lchk_lo\n"
".Lre_lob:\n"
"s_add_u32 s97, s97, 1\n"
"s_cmp_gt_u32 s97, 1048576\n"
"s_cbranch_scc1 .Lok_lob\n"
"s_add_u32 s34, s55, 61440\n"
"global_load_dwordx4 v[52:55], v0, s[34:35] offset:0 sc1\n"
"global_load_dwordx4 v[56:59], v0, s[34:35] offset:1024 sc1\n"
"global_load_dwordx4 v[60:63], v0, s[34:35] offset:2048 sc1\n"
"global_load_dwordx4 v[64:67], v0, s[34:35] offset:3072 sc1\n"
"global_load_dwordx4 v[68:71], v1, s[34:35] offset:0 sc1\n"
"s_waitcnt vmcnt(0)\n"
"v_max_u32 v29, v52, v53\n"
"v_max3_u32 v29, v54, v55, v29\n"
"v_max3_u32 v29, v56, v57, v29\n"
"v_max3_u32 v29, v58, v59, v29\n"
"v_max3_u32 v29, v60, v61, v29\n"
"v_max3_u32 v29, v62, v63, v29\n"
"v_max3_u32 v29, v64, v65, v29\n"
"v_max3_u32 v29, v66, v67, v29\n"
"v_max3_u32 v29, v68, v69, v29\n"
"v_max3_u32 v29, v70, v71, v29\n"
"v_cmp_eq_u32 vcc, 0xffbfffbf, v29\n"
"s_nop 1\n"
"s_cbranch_vccnz .Lre_lob\n"
"s_branch .Lok_lob\n"
".Lre_h0_0:\n"
"s_add_u32 s97, s97, 1\n"
"s_cmp_gt_u32 s97, 1048576\n"
"s_cbranch_scc1 .Lok_h0_0\n"
"s_add_u32 s34, s66, 0\n"
"global_load_dwordx4 v[120:123], v0, s[34:35] offset:0 sc1\n"
"global_load_dwordx4 v[124:127], v0, s[34:35] offset:1024 sc1\n"
"global_load_dwordx4 v[128:131], v0, s[34:35] offset:2048 sc1\n"
"global_load_dwordx4 v[132:135], v0, s[34:35] offset:3072 sc1\n"
"global_load_dwordx4 v[136:139], v1, s[34:35] offset:0 sc1\n"
"s_waitcnt vmcnt(0)\n"
"v_max_u32 v30, v120, v121\n"
"v_max3_u32 v30, v122, v123, v30\n"
"v_max3_u32 v30, v124, v125, v30\n"
"v_max3_u32 v30, v126, v127, v30\n"
"v_max3_u32 v30, v128, v129, v30\n"
"v_max3_u32 v30, v130, v131, v30\n"
"v_max3_u32 v30, v132, v133, v30\n"
"v_max3_u32 v30, v134, v135, v30\n"
"v_max3_u32 v30, v136, v137, v30\n"
"v_max3_u32 v30, v138, v139, v30\n"
"v_cmp_eq_u32 vcc, 0xffbfffbf, v30\n"
"s_nop 1\n"
"s_cbranch_vccnz .Lre_h0_0\n"
"s_branch .Lok_h0_0\n"
".Lre_h0_1:\n"
"s_add_u32 s97, s97, 1\n"
"s_cmp_gt_u32 s97, 1048576\n"
"s_cbranch_scc1 .Lok_h0_1\n"
"s_add_u32 s34, s66, 5120\n"
"global_load_dwordx4 v[140:143], v0, s[34:35] offset:0 sc1\n"
"global_load_dwordx4 v[144:147], v0, s[34:35] offset:1024 sc1\n"
"global_load_dwordx4 v[148:151], v0, s[34:35] offset:2048 sc1\n"
"global_load_dwordx4 v[152:155], v0, s[34:35] offset:3072 sc1\n"
"global_load_dwordx4 v[156:159], v1, s[34:35] offset:0 sc1\n"
"s_waitcnt vmcnt(0)\n"
"v_max_u32 v30, v140, v141\n"
"v_max3_u32 v30, v142, v143, v30\n"
"v_max3_u32 v30, v144, v145, v30\n"
"v_max3_u32 v30, v146, v147, v30\n"
"v_max3_u32 v30, v148, v149, v30\n"
"v_max3_u32 v30, v150, v151, v30\n"
"v_max3_u32 v30, v152, v153, v30\n"
"v_max3_u32 v30, v154, v155, v30\n"
"v_max3_u32 v30, v156, v157, v30\n"
"v_max3_u32 v30, v158, v159, v30\n"
"v_cmp_eq_u32 vcc, 0xffbfffbf, v30\n"
"s_nop 1\n"
"s_cbranch_vccnz .Lre_h0_1\n"
"s_branch .Lok_h0_1\n"
".Lre_h0_2:\n"
"s_add_u32 s97, s97, 1\n"
"s_cmp_gt_u32 s97, 1048576\n"
"s_cbranch_scc1 .Lok_h0_2\n"
"s_add_u32 s34, s66, 10240\n"
"global_load_dwordx4 v[160:163], v0, s[34:35] offset:0 sc1\n"
"global_load_dwordx4 v[164:167], v0, s[34:35] offset:1024 sc1\n"
"global_load_dwordx4 v[168:171], v0, s[34:35] offset:2048 sc1\n"
"global_load_dwordx4 v[172:175], v0, s[34:35] offset:3072 sc1\n"
"global_load_dwordx4 v[176:179], v1, s[34:35] offset:0 sc1\n"
"s_waitcnt vmcnt(0)\n"
"v_max_u32 v30, v160, v161\n"
"v_max3_u32 v30, v162, v163, v30\n"
"v_max3_u32 v30, v164, v165, v30\n"
"v_max3_u32 v30, v166, v167, v30\n"
"v_max3_u32 v30, v168, v169, v30\n"
"v_max3_u32 v30, v170, v171, v30\n"
"v_max3_u32 v30, v172, v173, v30\n"
"v_max3_u32 v30, v174, v175, v30\n"
"v_max3_u32 v30, v176, v177, v30\n"
"v_max3_u32 v30, v178, v179, v30\n"
"v_cmp_eq_u32 vcc, 0xffbfffbf, v30\n"
"s_nop 1\n"
"s_cbranch_vccnz .Lre_h0_2\n"
"s_branch .Lok_h0_2\n"
".Lre_st0:\n"
"s_add_u32 s97, s97, 1\n"
"s_cmp_gt_u32 s97, 1048576\n"
"s_cbranch_scc1 .Lok_st0\n"
"s_add_u32 s34, s67, 0\n"
"global_load_dwordx4 v[240:243], v25, s[34:35] offset:0 sc1\n"
"global_load_dwordx4 v[244:247], v25, s[34:35] offset:256 sc1\n"
"s_waitcnt vmcnt(0)\n"
"v_max_u32 v30, v240, v241\n"
"v_max3_u32 v30, v242, v243, v30\n"
"v_max3_u32 v30, v244, v245, v30\n"
"v_max3_u32 v30, v246, v247, v30\n"
"v_cmp_eq_u32 vcc, 0xffbfffbf, v30\n"
"s_nop 1\n"
"s_cbranch_vccnz .Lre_st0\n"
"s_branch .Lok_st0\n"
".Lre_h0_3:\n"
"s_add_u32 s97, s97, 1\n"
"s_cmp_gt_u32 s97, 1048576\n"
"s_cbranch_scc1 .Lok_h0_3\n"
"s_add_u32 s34, s66, 15360\n"
"global_load_dwordx4 v[180:183], v0, s[34:35] offset:0 sc1\n"
"global_load_dwordx4 v[184:187], v0, s[34:35] offset:1024 sc1\n"
"global_load_dwordx4 v[188:191], v0, s[34:35] offset:2048 sc1\n"
"global_load_dwordx4 v[112:115], v0, s[34:35] offset:3072 sc1\n"
"global_load_dwordx4 v[116:119], v1, s[34:35] offset:0 sc1\n"
"s_waitcnt vmcnt(0)\n"
"v_max_u32 v30, v180, v181\n"
"v_max3_u32 v30, v182, v183, v30\n"
"v_max3_u32 v30, v184, v185, v30\n"
"v_max3_u32 v30, v186, v187, v30\n"
"v_max3_u32 v30, v188, v189, v30\n"
"v_max3_u32 v30, v190, v191, v30\n"
"v_max3_u32 v30, v112, v113, v30\n"
"v_max3_u32 v30, v114, v115, v30\n"
"v_max3_u32 v30, v116, v117, v30\n"
"v_max3_u32 v30, v118, v119, v30\n"
"v_cmp_eq_u32 vcc, 0xffbfffbf, v30\n"
"s_nop 1\n"
"s_cbranch_vccnz .Lre_h0_3\n"
"s_branch .Lok_h0_3\n"
".Lre_h1_0:\n"
"s_add_u32 s97, s97, 1\n"
"s_cmp_gt_u32 s97, 1048576\n"
"s_cbranch_scc1 .Lok_h1_0\n"
"s_add_u32 s34, s66, 40960\n"
"global_load_dwordx4 v[32:35], v0, s[34:35] offset:0 sc1\n"
"global_load_dwordx4 v[36:39], v0, s[34:35] offset:1024 sc1\n"
"global_load_dwordx4 v[40:43], v0, s[34:35] offset:2048 sc1\n"
"global_load_dwordx4 v[44:47], v0, s[34:35] offset:3072 sc1\n"
"global_load_dwordx4 v[48:51], v1, s[34:35] offset:0 sc1\n"
"s_waitcnt vmcnt(0)\n"
"v_max_u32 v30, v32, v33\n"
"v_max3_u32 v30, v34, v35, v30\n"
"v_max3_u32 v30, v36, v37, v30\n"
"v_max3_u32 v30, v38, v39, v30\n"
"v_max3_u32 v30, v40, v41, v30\n"
"v_max3_u32 v30, v42, v43, v30\n"
"v_max3_u32 v30, v44, v45, v30\n"
"v_max3_u32 v30, v46, v47, v30\n"
"v_max3_u32 v30, v48, v49, v30\n"
"v_max3_u32 v30, v50, v51, v30\n"
"v_cmp_eq_u32 vcc, 0xffbfffbf, v30\n"
"s_nop 1\n"
"s_cbranch_vccnz .Lre_h1_0\n"
"s_branch .Lok_h1_0\n"
".Lre_h1_1:\n"
"s_add_u32 s97, s97, 1\n"
"s_cmp_gt_u32 s97, 1048576\n"
"s_cbranch_scc1 .Lok_h1_1\n"
"s_add_u32 s34, s66, 46080\n"
"global_load_dwordx4 v[52:55], v0, s[34:35] offset:0 sc1\n"
"global_load_dwordx4 v[56:59], v0, s[34:35] offset:1024 sc1\n"
"global_load_dwordx4 v[60:63], v0, s[34:35] offset:2048 sc1\n"
"global_load_dwordx4 v[64:67], v0, s[34:35] offset:3072 sc1\n"
"global_load_dwordx4 v[68:71], v1, s[34:35] offset:0 sc1\n"
"s_waitcnt vmcnt(0)\n"
"v_max_u32 v30, v52, v53\n"
"v_max3_u32 v30, v54, v55, v30\n"
"v_max3_u32 v30, v56, v57, v30\n"
"v_max3_u32 v30, v58, v59, v30\n"
"v_max3_u32 v30, v60, v61, v30\n"
"v_max3_u32 v30, v62, v63, v30\n"
"v_max3_u32 v30, v64, v65, v30\n"
"v_max3_u32 v30, v66, v67, v30\n"
"v_max3_u32 v30, v68, v69, v30\n"
"v_max3_u32 v30, v70, v71, v30\n"
"v_cmp_eq_u32 vcc, 0xffbfffbf, v30\n"
"s_nop 1\n"
"s_cbranch_vccnz .Lre_h1_1\n"
"s_branch .Lok_h1_1\n"
".Lre_h1_2:\n"
"s_add_u32 s97, s97, 1\n"
"s_cmp_gt_u32 s97, 1048576\n"
"s_cbranch_scc1 .Lok_h1_2\n"
"s_add_u32 s34, s66, 51200\n"
"global_load_dwordx4 v[72:75], v0, s[34:35] offset:0 sc1\n"
"global_load_dwordx4 v[76:79], v0, s[34:35] offset:1024 sc1\n"
"global_load_dwordx4 v[80:83], v0, s[34:35] offset:2048 sc1\n"
"global_load_dwordx4 v[84:87], v0, s[34:35] offset:3072 sc1\n"
"global_load_dwordx4 v[88:91], v1, s[34:35] offset:0 sc1\n"
"s_waitcnt vmcnt(0)\n"
"v_max_u32 v30, v72, v73\n"
"v_max3_u32 v30, v74, v75, v30\n"
"v_max3_u32 v30, v76, v77, v30\n"
"v_max3_u32 v30, v78, v79, v30\n"
"v_max3_u32 v30, v80, v81, v30\n"
"v_max3_u32 v30, v82, v83, v30\n"
"v_max3_u32 v30, v84, v85, v30\n"
"v_max3_u32 v30, v86, v87, v30\n"
"v_max3_u32 v30, v88, v89, v30\n"
"v_max3_u32 v30, v90, v91, v30\n"
"v_cmp_eq_u32 vcc, 0xffbfffbf, v30\n"
"s_nop 1\n"
"s_cbranch_vccnz .Lre_h1_2\n"
"s_branch .Lok_h1_2\n"
".Lre_st1:\n"
"s_add_u32 s97, s97, 1\n"
"s_cmp_gt_u32 s97, 1048576\n"
"s_cbranch_scc1 .Lok_st1\n"
"s_add_u32 s34, s67, 4096\n"
"global_load_dwordx4 v[248:251], v25, s[34:35] offset:0 sc1\n"
"global_load_dwordx4 v[252:255], v25, s[34:35] offset:256 sc1\n"
"s_waitcnt vmcnt(0)\n"
"v_max_u32 v30, v248, v249\n"
"v_max3_u32 v30, v250, v251, v30\n"
"v_max3_u32 v30, v252, v253, v30\n"
"v_max3_u32 v30, v254, v255, v30\n"
"v_cmp_eq_u32 vcc, 0xffbfffbf, v30\n"
"s_nop 1\n"
"s_cbranch_vccnz .Lre_st1\n"
"s_branch .Lok_st1\n"
".Lre_h1_3:\n"
"s_add_u32 s97, s97, 1\n"
"s_cmp_gt_u32 s97, 1048576\n"
"s_cbranch_scc1 .Lok_h1_3\n"
"s_add_u32 s34, s66, 56320\n"
"global_load_dwordx4 v[92:95], v0, s[34:35] offset:0 sc1\n"
"global_load_dwordx4 v[96:99], v0, s[34:35] offset:1024 sc1\n"
"global_load_dwordx4 v[100:103], v0, s[34:35] offset:2048 sc1\n"
"global_load_dwordx4 v[104:107], v0, s[34:35] offset:3072 sc1\n"
"global_load_dwordx4 v[108:111], v1, s[34:35] offset:0 sc1\n"
"s_waitcnt vmcnt(0)\n"
"v_max_u32 v30, v92, v93\n"
"v_max3_u32 v30, v94, v95, v30\n"
"v_max3_u32 v30, v96, v97, v30\n"
"v_max3_u32 v30, v98, v99, v30\n"
"v_max3_u32 v30, v100, v101, v30\n"
"v_max3_u32 v30, v102, v103, v30\n"
"v_max3_u32 v30, v104, v105, v30\n"
"v_max3_u32 v30, v106, v107, v30\n"
"v_max3_u32 v30, v108, v109, v30\n"
"v_max3_u32 v30, v110, v111, v30\n"
"v_cmp_eq_u32 vcc, 0xffbfffbf, v30\n"
"s_nop 1\n"
"s_cbranch_vccnz .Lre_h1_3\n"
"s_branch .Lok_h1_3\n"
".Lre_hh0:\n"
"s_add_u32 s97, s97, 1\n"
"s_cmp_gt_u32 s97, 1048576\n"
"s_cbranch_scc1 .Lok_hh0\n"
"s_add_u32 s34, s72, s70\n"
"global_load_dwordx4 v[240:243], v0, s[34:35] offset:0 sc1\n"
"global_load_dwordx4 v[244:247], v20, s[34:35] offset:0 sc1\n"
"global_load_dwordx4 v[248:251], v21, s[34:35] offset:0 sc1\n"
"global_load_dwordx4 v[252:255], v22, s[34:35] offset:0 sc1\n"
"s_waitcnt vmcnt(0)\n"
"s_branch .Lchk_hh0\n"
".Lre_hh1:\n"
"s_add_u32 s97, s97, 1\n"
"s_cmp_gt_u32 s97, 1048576\n"
"s_cbranch_scc1 .Lok_hh1\n"
"s_add_u32 s34, s73, s70\n"
"global_load_dwordx4 v[192:195], v0, s[34:35] offset:0 sc1\n"
"global_load_dwordx4 v[196:199], v20, s[34:35] offset:0 sc1\n"
"global_load_dwordx4 v[200:203], v21, s[34:35] offset:0 sc1\n"
"global_load_dwordx4 v[204:207], v22, s[34:35] offset:0 sc1\n"
"s_waitcnt vmcnt(0)\n"
"v_max_u32 v30, v192, v193\n"
"v_max3_u32 v30, v194, v195, v30\n"
"v_max3_u32 v30, v196, v197, v30\n"
"v_max3_u32 v30, v198, v199, v30\n"
"v_max3_u32 v30, v200, v201, v30\n"
"v_max3_u32 v30, v202, v203, v30\n"
"v_max3_u32 v30, v204, v205, v30\n"
"v_max3_u32 v30, v206, v207, v30\n"
"v_cmp_eq_u32 vcc, 0xffbfffbf, v30\n"
"s_nop 1\n"
"s_cbranch_vccnz .Lre_hh1\n"
"s_branch .Lok_hh1\n"
".Lre_hh2:\n"
"s_add_u32 s97, s97, 1\n"
"s_cmp_gt_u32 s97, 1048576\n"
"s_cbranch_scc1 .Lok_hh2\n"
"s_add_u32 s34, s74, s70\n"
"global_load_dwordx4 v[32:35], v0, s[34:35] offset:0 sc1\n"
"global_load_dwordx4 v[36:39], v20, s[34:35] offset:0 sc1\n"
"global_load_dwordx4 v[40:43], v21, s[34:35] offset:0 sc1\n"
"global_load_dwordx4 v[44:47], v22, s[34:35] offset:0 sc1\n"
"s_waitcnt vmcnt(0)\n"
"v_max_u32 v30, v32, v33\n"
"v_max3_u32 v30, v34, v35, v30\n"
"v_max3_u32 v30, v36, v37, v30\n"
"v_max3_u32 v30, v38, v39, v30\n"
"v_max3_u32 v30, v40, v41, v30\n"
"v_max3_u32 v30, v42, v43, v30\n"
"v_max3_u32 v30, v44, v45, v30\n"
"v_max3_u32 v30, v46, v47, v30\n"
"v_cmp_eq_u32 vcc, 0xffbfffbf, v30\n"
"s_nop 1\n"
"s_cbranch_vccnz .Lre_hh2\n"
"s_branch .Lok_hh2\n"
".Lre_hh3:\n"
"s_add_u32 s97, s97, 1\n"
"s_cmp_gt_u32 s97, 1048576\n"
"s_cbranch_scc1 .Lok_hh3\n"
"s_add_u32 s34, s75, s70\n"
"global_load_dwordx4 v[48:51], v0, s[34:35] offset:0 sc1\n"
"global_load_dwordx4 v[52:55], v20, s[34:35] offset:0 sc1\n"
"global_load_dwordx4 v[56:59], v21, s[34:35] offset:0 sc1\n"
"global_load_dwordx4 v[60:63], v22, s[34:35] offset:0 sc1\n"
"s_waitcnt vmcnt(0)\n"
"v_max_u32 v30, v48, v49\n"
"v_max3_u32 v30, v50, v51, v30\n"
"v_max3_u32 v30, v52, v53, v30\n"
"v_max3_u32 v30, v54, v55, v30\n"
"v_max3_u32 v30, v56, v57, v30\n"
"v_max3_u32 v30, v58, v59, v30\n"
"v_max3_u32 v30, v60, v61, v30\n"
"v_max3_u32 v30, v62, v63, v30\n"
"v_cmp_eq_u32 vcc, 0xffbfffbf, v30\n"
"s_nop 1\n"
"s_cbranch_vccnz .Lre_hh3\n"
"s_branch .Lok_hh3\n"
".Lre_hh4:\n"
"s_add_u32 s97, s97, 1\n"
"s_cmp_gt_u32 s97, 1048576\n"
"s_cbranch_scc1 .Lok_hh4\n"
"s_add_u32 s34, s76, s70\n"
"global_load_dwordx4 v[64:67], v0, s[34:35] offset:0 sc1\n"
"global_load_dwordx4 v[68:71], v20, s[34:35] offset:0 sc1\n"
"global_load_dwordx4 v[72:75], v21, s[34:35] offset:0 sc1\n"
"global_load_dwordx4 v[76:79], v22, s[34:35] offset:0 sc1\n"
"s_waitcnt vmcnt(0)\n"
"v_max_u32 v30, v64, v65\n"
"v_max3_u32 v30, v66, v67, v30\n"
"v_max3_u32 v30, v68, v69, v30\n"
"v_max3_u32 v30, v70, v71, v30\n"
"v_max3_u32 v30, v72, v73, v30\n"
"v_max3_u32 v30, v74, v75, v30\n"
"v_max3_u32 v30, v76, v77, v30\n"
"v_max3_u32 v30, v78, v79, v30\n"
"v_cmp_eq_u32 vcc, 0xffbfffbf, v30\n"
"s_nop 1\n"
"s_cbranch_vccnz .Lre_hh4\n"
"s_branch .Lok_hh4\n"
".Lre_hh5:\n"
"s_add_u32 s97, s97, 1\n"
"s_cmp_gt_u32 s97, 1048576\n"
"s_cbranch_scc1 .Lok_hh5\n"
"s_add_u32 s34, s77, s70\n"
"global_load_dwordx4 v[80:83], v0, s[34:35] offset:0 sc1\n"
"global_load_dwordx4 v[84:87], v20, s[34:35] offset:0 sc1\n"
"global_load_dwordx4 v[88:91], v21, s[34:35] offset:0 sc1\n"
"global_load_dwordx4 v[92:95], v22, s[34:35] offset:0 sc1\n"
"s_waitcnt vmcnt(0)\n"
"v_max_u32 v30, v80, v81\n"
"v_max3_u32 v30, v82, v83, v30\n"
"v_max3_u32 v30, v84, v85, v30\n"
"v_max3_u32 v30, v86, v87, v30\n"
"v_max3_u32 v30, v88, v89, v30\n"
"v_max3_u32 v30, v90, v91, v30\n"
"v_max3_u32 v30, v92, v93, v30\n"
"v_max3_u32 v30, v94, v95, v30\n"
"v_cmp_eq_u32 vcc, 0xffbfffbf, v30\n"
"s_nop 1\n"
"s_cbranch_vccnz .Lre_hh5\n"
"s_branch .Lok_hh5\n"
".Lre_hh6:\n"
"s_add_u32 s97, s97, 1\n"
"s_cmp_gt_u32 s97, 1048576\n"
"s_cbranch_scc1 .Lok_hh6\n"
"s_add_u32 s34, s78, s70\n"
"global_load_dwordx4 v[96:99], v0, s[34:35] offset:0 sc1\n"
"global_load_dwordx4 v[100:103], v20, s[34:35] offset:0 sc1\n"
"global_load_dwordx4 v[104:107], v21, s[34:35] offset:0 sc1\n"
"global_load_dwordx4 v[108:111], v22, s[34:35] offset:0 sc1\n"
"s_waitcnt vmcnt(0)\n"
"v_max_u32 v30, v96, v97\n"
"v_max3_u32 v30, v98, v99, v30\n"
"v_max3_u32 v30, v100, v101, v30\n"
"v_max3_u32 v30, v102, v103, v30\n"
"v_max3_u32 v30, v104, v105, v30\n"
"v_max3_u32 v30, v106, v107, v30\n"
"v_max3_u32 v30, v108, v109, v30\n"
"v_max3_u32 v30, v110, v111, v30\n"
"v_cmp_eq_u32 vcc, 0xffbfffbf, v30\n"
"s_nop 1\n"
"s_cbranch_vccnz .Lre_hh6\n"
"s_branch .Lok_hh6\n"
".Lre_hh7:\n"
"s_add_u32 s97, s97, 1\n"
"s_cmp_gt_u32 s97, 1048576\n"
"s_cbranch_scc1 .Lok_hh7\n"
"s_add_u32 s34, s79, s70\n"
"global_load_dwordx4 v[208:211], v0, s[34:35] offset:0 sc1\n"
"global_load_dwordx4 v[212:215], v20, s[34:35] offset:0 sc1\n"
"global_load_dwordx4 v[216:219], v21, s[34:35] offset:0 sc1\n"
"global_load_dwordx4 v[220:223], v22, s[34:35] offset:0 sc1\n"
"s_waitcnt vmcnt(0)\n"
"v_max_u32 v30, v208, v209\n"
"v_max3_u32 v30, v210, v211, v30\n"
"v_max3_u32 v30, v212, v213, v30\n"
"v_max3_u32 v30, v214, v215, v30\n"
"v_max3_u32 v30, v216, v217, v30\n"
"v_max3_u32 v30, v218, v219, v30\n"
"v_max3_u32 v30, v220, v221, v30\n"
"v_max3_u32 v30, v222, v223, v30\n"
"v_cmp_eq_u32 vcc, 0xffbfffbf, v30\n"
"s_nop 1\n"
"s_cbranch_vccnz .Lre_hh7\n"
"s_branch .Lok_hh7\n"
".Lre_hh8:\n"
"s_add_u32 s97, s97, 1\n"
"s_cmp_gt_u32 s97, 1048576\n"
"s_cbranch_scc1 .Lok_hh8\n"
"s_add_u32 s34, s80, s70\n"
"global_load_dwordx4 v[224:227], v0, s[34:35] offset:0 sc1\n"
"global_load_dwordx4 v[228:231], v20, s[34:35] offset:0 sc1\n"
"global_load_dwordx4 v[232:235], v21, s[34:35] offset:0 sc1\n"
"global_load_dwordx4 v[236:239], v22, s[34:35] offset:0 sc1\n"
"s_waitcnt vmcnt(0)\n"
"v_max_u32 v30, v224, v225\n"
"v_max3_u32 v30, v226, v227, v30\n"
"v_max3_u32 v30, v228, v229, v30\n"
"v_max3_u32 v30, v230, v231, v30\n"
"v_max3_u32 v30, v232, v233, v30\n"
"v_max3_u32 v30, v234, v235, v30\n"
"v_max3_u32 v30, v236, v237, v30\n"
"v_max3_u32 v30, v238, v239, v30\n"
"v_cmp_eq_u32 vcc, 0xffbfffbf, v30\n"
"s_nop 1\n"
"s_cbranch_vccnz .Lre_hh8\n"
"s_branch .Lok_hh8\n"
".Lre_hh9:\n"
"s_add_u32 s97, s97, 1\n"
"s_cmp_gt_u32 s97, 1048576\n"
"s_cbranch_scc1 .Lok_hh9\n"
"s_add_u32 s34, s81, s70\n"
"global_load_dwordx4 v[240:243], v0, s[34:35] offset:0 sc1\n"
"global_load_dwordx4 v[244:247], v20, s[34:35] offset:0 sc1\n"
"global_load_dwordx4 v[248:251], v21, s[34:35] offset:0 sc1\n"
"global_load_dwordx4 v[252:255], v22, s[34:35] offset:0 sc1\n"
"s_waitcnt vmcnt(0)\n"
"v_max_u32 v30, v240, v241\n"
"v_max3_u32 v30, v242, v243, v30\n"
"v_max3_u32 v30, v244, v245, v30\n"
"v_max3_u32 v30, v246, v247, v30\n"
"v_max3_u32 v30, v248, v249, v30\n"
"v_max3_u32 v30, v250, v251, v30\n"
"v_max3_u32 v30, v252, v253, v30\n"
"v_max3_u32 v30, v254, v255, v30\n"
"v_cmp_eq_u32 vcc, 0xffbfffbf, v30\n"
"s_nop 1\n"
"s_cbranch_vccnz .Lre_hh9\n"
"s_branch .Lok_hh9\n"
".Lre_hh10:\n"
"s_add_u32 s97, s97, 1\n"
"s_cmp_gt_u32 s97, 1048576\n"
"s_cbranch_scc1 .Lok_hh10\n"
"s_add_u32 s34, s82, s70\n"
"buffer_load_dwordx4 v[192:195], v0, s[20:23], s34 offen offset:0 sc1\n"
"buffer_load_dwordx4 v[196:199], v20, s[20:23], s34 offen offset:0 sc1\n"
"buffer_load_dwordx4 v[200:203], v21, s[20:23], s34 offen offset:0 sc1\n"
"buffer_load_dwordx4 v[204:207], v22, s[20:23], s34 offen offset:0 sc1\n"
"s_waitcnt vmcnt(0)\n"
"v_max_u32 v30, v192, v193\n"
"v_max3_u32 v30, v194, v195, v30\n"
"v_max3_u32 v30, v196, v197, v30\n"
"v_max3_u32 v30, v198, v199, v30\n"
"v_max3_u32 v30, v200, v201, v30\n"
"v_max3_u32 v30, v202, v203, v30\n"
"v_max3_u32 v30, v204, v205, v30\n"
"v_max3_u32 v30, v206, v207, v30\n"
"v_cmp_eq_u32 vcc, 0xffbfffbf, v30\n"
"s_nop 1\n"
"s_cbranch_vccnz .Lre_hh10\n"
"s_branch .Lok_hh10\n"
".Lre_hh11:\n"
"s_add_u32 s97, s97, 1\n"
"s_cmp_gt_u32 s97, 1048576\n"
"s_cbranch_scc1 .Lok_hh11\n"
"s_add_u32 s34, s83, s70\n"
"global_load_dwordx4 v[32:35], v0, s[34:35] offset:0 sc1\n"
"global_load_dwordx4 v[36:39], v20, s[34:35] offset:0 sc1\n"
"global_load_dwordx4 v[40:43], v21, s[34:35] offset:0 sc1\n"
"global_load_dwordx4 v[44:47], v22, s[34:35] offset:0 sc1\n"
"s_waitcnt vmcnt(0)\n"
"v_max_u32 v30, v32, v33\n"
"v_max3_u32 v30, v34, v35, v30\n"
"v_max3_u32 v30, v36, v37, v30\n"
"v_max3_u32 v30, v38, v39, v30\n"
"v_max3_u32 v30, v40, v41, v30\n"
"v_max3_u32 v30, v42, v43, v30\n"
"v_max3_u32 v30, v44, v45, v30\n"
"v_max3_u32 v30, v46, v47, v30\n"
"v_cmp_eq_u32 vcc, 0xffbfffbf, v30\n"
"s_nop 1\n"
"s_cbranch_vccnz .Lre_hh11\n"
"s_branch .Lok_hh11\n"
".Lre_hh12:\n"
"s_add_u32 s97, s97, 1\n"
"s_cmp_gt_u32 s97, 1048576\n"
"s_cbranch_scc1 .Lok_hh12\n"
"s_add_u32 s34, s84, s70\n"
"global_load_dwordx4 v[48:51], v0, s[34:35] offset:0 sc1\n"
"global_load_dwordx4 v[52:55], v20, s[34:35] offset:0 sc1\n"
"global_load_dwordx4 v[56:59], v21, s[34:35] offset:0 sc1\n"
"global_load_dwordx4 v[60:63], v22, s[34:35] offset:0 sc1\n"
"s_waitcnt vmcnt(0)\n"
"v_max_u32 v30, v48, v49\n"
"v_max3_u32 v30, v50, v51, v30\n"
"v_max3_u32 v30, v52, v53, v30\n"
"v_max3_u32 v30, v54, v55, v30\n"
"v_max3_u32 v30, v56, v57, v30\n"
"v_max3_u32 v30, v58, v59, v30\n"
"v_max3_u32 v30, v60, v61, v30\n"
"v_max3_u32 v30, v62, v63, v30\n"
"v_cmp_eq_u32 vcc, 0xffbfffbf, v30\n"
"s_nop 1\n"
"s_cbranch_vccnz .Lre_hh12\n"
"s_branch .Lok_hh12\n"
".Lre_hh13:\n"
"s_add_u32 s97, s97, 1\n"
"s_cmp_gt_u32 s97, 1048576\n"
"s_cbranch_scc1 .Lok_hh13\n"
"s_add_u32 s34, s85, s70\n"
"global_load_dwordx4 v[64:67], v0, s[34:35] offset:0 sc1\n"
"global_load_dwordx4 v[68:71], v20, s[34:35] offset:0 sc1\n"
"global_load_dwordx4 v[72:75], v21, s[34:35] offset:0 sc1\n"
"global_load_dwordx4 v[76:79], v22, s[34:35] offset:0 sc1\n"
"s_waitcnt vmcnt(0)\n"
"v_max_u32 v30, v64, v65\n"
"v_max3_u32 v30, v66, v67, v30\n"
"v_max3_u32 v30, v68, v69, v30\n"
"v_max3_u32 v30, v70, v71, v30\n"
"v_max3_u32 v30, v72, v73, v30\n"
"v_max3_u32 v30, v74, v75, v30\n"
"v_max3_u32 v30, v76, v77, v30\n"
"v_max3_u32 v30, v78, v79, v30\n"
"v_cmp_eq_u32 vcc, 0xffbfffbf, v30\n"
"s_nop 1\n"
"s_cbranch_vccnz .Lre_hh13\n"
"s_branch .Lok_hh13\n"
".Lmk_end:\n"
".p2align 12\n"
      :
      : "s"(__builtin_amdgcn_kernarg_segment_ptr()), "s"(bid), "s"(wave)
      : "v0", "v1", "v2", "v3", "v4", "v5", "v6", "v7", "v8", "v9", "v10", "v11", "v12", "v13", "v14", "v15", "v16", "v17", "v18", "v19", "v20", "v21", "v22", "v23", "v24", "v25", "v26", "v27", "v28", "v29", "v30", "v31", "v32", "v33", "v34", "v35", "v36", "v37", "v38", "v39", "v40", "v41", "v42", "v43", "v44", "v45", "v46", "v47", "v48", "v49", "v50", "v51", "v52", "v53", "v54", "v55", "v56", "v57", "v58", "v59", "v60", "v61", "v62", "v63", "v64", "v65", "v66", "v67", "v68", "v69", "v70", "v71", "v72", "v73", "v74", "v75", "v76", "v77", "v78", "v79", "v80", "v81", "v82", "v83", "v84", "v85", "v86", "v87", "v88", "v89", "v90", "v91", "v92", "v93", "v94", "v95", "v96", "v97", "v98", "v99", "v100", "v101", "v102", "v103", "v104", "v105", "v106", "v107", "v108", "v109", "v110", "v111", "v112", "v113", "v114", "v115", "v116", "v117", "v118", "v119", "v120", "v121", "v122", "v123", "v124", "v125", "v126", "v127", "v128", "v129", "v130", "v131", "v132", "v133", "v134", "v135", "v136", "v137", "v138", "v139", "v140", "v141", "v142", "v143", "v144", "v145", "v146", "v147", "v148", "v149", "v150", "v151", "v152", "v153", "v154", "v155", "v156", "v157", "v158", "v159", "v160", "v161", "v162", "v163", "v164", "v165", "v166", "v167", "v168", "v169", "v170", "v171", "v172", "v173", "v174", "v175", "v176", "v177", "v178", "v179", "v180", "v181", "v182", "v183", "v184", "v185", "v186", "v187", "v188", "v189", "v190", "v191", "v192", "v193", "v194", "v195", "v196", "v197", "v198", "v199", "v200", "v201", "v202", "v203", "v204", "v205", "v206", "v207", "v208", "v209", "v210", "v211", "v212", "v213", "v214", "v215", "v216", "v217", "v218", "v219", "v220", "v221", "v222", "v223", "v224", "v225", "v226", "v227", "v228", "v229", "v230", "v231", "v232", "v233", "v234", "v235", "v236", "v237", "v238", "v239", "v240", "v241", "v242", "v243", "v244", "v245", "v246", "v247", "v248", "v249", "v250", "v251", "v252", "v253", "v254", "v255", "a0", "a1", "a2", "a3", "a4", "a5", "a6", "a7", "a8", "a9", "a10", "a11", "a12", "a13", "a14", "a15", "a16", "a17", "a18", "a19", "a20", "a21", "a22", "a23", "a24", "a25", "a26", "a27", "a28", "a29", "a30", "a31", "a32", "a33", "a34", "a35", "a36", "a37", "a38", "a39", "a40", "a41", "a42", "a43", "a44", "a45", "a46", "a47", "a48", "a49", "a50", "a51", "a52", "a53", "a54", "a55", "a56", "a57", "a58", "a59", "a60", "a61", "a62", "a63", "a64", "a65", "a66", "a67", "a68", "a69", "a70", "a71", "a72", "a73", "a74", "a75", "a76", "a77", "a78", "a79", "a80", "a81", "a82", "a83", "a84", "a85", "a86", "a87", "a88", "a89", "a90", "a91", "a92", "a93", "a94", "a95", "a96", "a97", "a98", "a99", "a100", "a101", "a102", "a103", "a104", "a105", "a106", "a107", "a108", "a109", "a110", "a111", "a112", "a113", "a114", "a115", "a116", "a117", "a118", "a119", "a120", "a121", "a122", "a123", "a124", "a125", "a126", "a127", "a128", "a129", "a130", "a131", "a132", "a133", "a134", "a135", "a136", "a137", "a138", "a139", "a140", "a141", "a142", "a143", "a144", "a145", "a146", "a147", "a148", "a149", "a150", "a151", "a152", "a153", "a154", "a155", "a156", "a157", "a158", "a159", "a160", "a161", "a162", "a163", "a164", "a165", "a166", "a167", "a168", "a169", "a170", "a171", "a172", "a173", "a174", "a175", "a176", "a177", "a178", "a179", "a180", "a181", "a182", "a183", "a184", "a185", "a186", "a187", "a188", "a189", "a190", "a191", "a192", "a193", "a194", "a195", "a196", "a197", "a198", "a199", "a200", "a201", "a202", "a203", "a204", "a205", "a206", "a207", "a208", "a209", "a210", "a211", "a212", "a213", "a214", "a215", "a216", "a217", "a218", "a219", "a220", "a221", "a222", "a223", "a224", "a225", "a226", "a227", "a228", "a229", "a230", "a231", "a232", "a233", "a234", "a235", "a236", "a237", "a238", "a239", "a240", "a241", "a242", "a243", "a244", "a245", "a246", "a247", "a248", "a249", "a250", "a251", "a252", "a253", "a254", "a255", "s4", "s5", "s6", "s7", "s8", "s9", "s10", "s11", "s12", "s13", "s14", "s15", "s16", "s17", "s18", "s19", "s20", "s21", "s22", "s23", "s24", "s25", "s26", "s27", "s28", "s29", "s30", "s31", "s34", "s35", "s36", "s37", "s38", "s39", "s40", "s41", "s42", "s43", "s44", "s45", "s46", "s47", "s48", "s49", "s50", "s51", "s52", "s53", "s54", "s55", "s56", "s57", "s58", "s59", "s60", "s61", "s62", "s63", "s64", "s65", "s66", "s67", "s68", "s69", "s70", "s71", "s72", "s73", "s74", "s75", "s76", "s77", "s78", "s79", "s80", "s81", "s82", "s83", "s84", "s85", "s86", "s87", "s88", "s89", "s90", "s91", "s92", "s93", "s94", "s95", "s96", "s97", "s98", "s99", "vcc", "scc", "memory");
}

extern "C" void kernel_launch(void* const* d_in, const int* in_sizes, int n_in, void* d_out, int out_size, void* d_ws, size_t ws_size,
                              hipStream_t stream) {
  (void)hipFuncSetAttribute((const void*)fused_block, hipFuncAttributeMaxDynamicSharedMemorySize, 163840);
  fused_block<<<256, 256, 163840, stream>>>(d_in[0], d_in[1], d_in[2], d_in[3], d_in[4], d_in[6], d_in[7], d_in[8], d_out, d_ws);
}
```
